```python
import torch

def _softmax(x, axis=-1):
    return torch.softmax(x, dim=axis)
def _gelu(x):
    return torch.nn.functional.gelu(x, approximate="tanh")
def _silu(x):
    return torch.nn.functional.silu(x)
def _sigmoid(x):
    return torch.sigmoid(x)
def _layernorm(x, axis=-1, eps=1e-5):
    m = x.mean(dim=axis, keepdim=True)
    v = x.var(dim=axis, keepdim=True, unbiased=False)
    return (x - m) / torch.sqrt(v + eps)
def _scatter_add(tgt, idx, src, axis=0):
    return tgt.clone().index_add_(axis, idx, src)
def _segment_sum(src, seg, n):
    out = torch.zeros((n,) + src.shape[1:], dtype=src.dtype)
    return out.index_add_(0, seg, src)
def _pad(x, pw):
    flat = tuple(v for pair in reversed(pw) for v in pair)
    return torch.nn.functional.pad(x, flat)

kernel_name = 'recurrent_scan__sliding_block_cached__mixer_channel_mlp__decode'

def setup_inputs():

    _w = (lambda a: (lambda t: t.to(torch.bfloat16) if t.is_floating_point() else t)(torch.as_tensor(a)))
    def _A_setup():
        B, Sq, D = 1, 1, 512
        Hq, Hkv, Sk, Dh = 32, 8, 65536, 128
        assert Hq % Hkv == 0, (Hq, Hkv)
        return {
            "x": _w((lambda *s: torch.randn(*s, dtype=torch.bfloat16) * 0.1)(B, Sq, D)),
            "k_cache": _w((lambda *s: torch.randn(*s, dtype=torch.bfloat16) * 0.8)(B, Hkv, Sk, Dh)),
            "v_cache": _w((lambda *s: torch.randn(*s, dtype=torch.bfloat16) * 3.2)(B, Hkv, Sk, Dh)),
            "wq": _w((lambda *s: torch.randn(*s, dtype=torch.bfloat16) * 0.8)(D, Hq * Dh) * (D ** -0.5)),
            "wo": _w((lambda *s: torch.randn(*s, dtype=torch.bfloat16) * 3.2)(Hq * Dh, D) * ((Hq * Dh) ** -0.5)),
        }
    _A_in = _A_setup()

    def _B_setup():
        B, S, D, F = 1, 1, 512, 1792
        return {
            "x": _w((lambda *s: torch.randn(*s, dtype=torch.bfloat16) * 0.1)(B, S, D)),
            "w1": _w((lambda *s: torch.randn(*s, dtype=torch.bfloat16) * 3.2)(D, F) * (D ** -0.5)),
            "b1": _w((lambda *s: torch.randn(*s, dtype=torch.bfloat16) * 3.2)(F)),
            "w2": _w((lambda *s: torch.randn(*s, dtype=torch.bfloat16) * 3.2)(F, D) * (F ** -0.5)),
            "b2": _w((lambda *s: torch.randn(*s, dtype=torch.bfloat16) * 3.2)(D)),
        }
    _B_in = _B_setup()

    return {
        "A_x": _A_in["x"],
        "A_k_cache": _A_in["k_cache"],
        "A_v_cache": _A_in["v_cache"],
        "A_wq": _A_in["wq"],
        "A_wo": _A_in["wo"],
        "B_x": _B_in["x"],
        "B_w1": _B_in["w1"],
        "B_b1": _B_in["b1"],
        "B_w2": _B_in["w2"],
        "B_b2": _B_in["b2"],
    }

def reference(A_x, A_k_cache, A_v_cache, A_wq, A_wo, B_x, B_w1, B_b1, B_w2, B_b2):
    def _A_ref(x, k_cache, v_cache, wq, wo):
        B, Sq, D = x.shape
        Hq, Dh = 32, 128
        Hkv, Sk = k_cache.shape[1], k_cache.shape[2]
        W = min(4096, max(256, Sk // 4))
        rep = Hq // Hkv
        q = (lambda x, *d: x.permute(*d))((x @ wq).reshape(B, Sq, Hq, Dh), 0, 2, 1, 3)
        k = (lambda x, n, axis: torch.repeat_interleave(x, n, dim=axis))(k_cache[:, :, Sk - W:, :], rep, axis=1)
        v = (lambda x, n, axis: torch.repeat_interleave(x, n, dim=axis))(v_cache[:, :, Sk - W:, :], rep, axis=1)
        scores = torch.einsum("bhqd,bhkd->bhqk", q, k) * (Dh ** -0.5)
        p = _softmax(scores, axis=-1)
        o = torch.einsum("bhqk,bhkd->bhqd", p, v)
        return (lambda x, *d: x.permute(*d))(o, 0, 2, 1, 3).reshape(B, Sq, Hq * Dh) @ wo

    def _B_ref(x, w1, b1, w2, b2):
        y = _layernorm(x, axis=-1)
        h = _gelu(torch.einsum("bsd,df->bsf", y, w1) + b1)
        return torch.einsum("bsf,fd->bsd", h, w2) + b2

    x = A_x
    xn = _layernorm(x, axis=-1)
    s = _A_ref(xn, A_k_cache, A_v_cache, A_wq, A_wo)
    g = _silu(_B_ref(xn, B_w1, B_b1, B_w2, B_b2))
    return x + s * g
```

```cpp
#include <hip/hip_runtime.h>
#include <hip/hip_bf16.h>

typedef unsigned short u16;
__device__ __forceinline__ float bf2f(u16 b) { return __uint_as_float((unsigned)b << 16); }
__device__ __forceinline__ u16 f2bf(float f) { unsigned u = __float_as_uint(f); u += 0x7FFFu + ((u >> 16) & 1u); return (u16)(u >> 16); }
__device__ __forceinline__ float rbf(float f) { return bf2f(f2bf(f)); }

constexpr int D = 512, HQ = 32, HKV = 8, SK = 65536, DH = 128, W = 4096, F = 1792;
constexpr int NSPLIT = 32, KPS = W / NSPLIT;

__device__ float block_sum(float v, float* red) {
  for (int m = 32; m >= 1; m >>= 1) v += __shfl_xor(v, m);
  int w = threadIdx.x >> 6;
  __syncthreads();
  if ((threadIdx.x & 63) == 0) red[w] = v;
  __syncthreads();
  float s = 0;
  for (int i = 0; i < (int)(blockDim.x >> 6); ++i) s += red[i];
  return s;
}

__device__ void ln512(float* sx, float* red) {
  int t = threadIdx.x;
  float a = 0;
  for (int i = t; i < D; i += blockDim.x) a += sx[i];
  float m = rbf(block_sum(a, red) * (1.0f / D));
  float a1 = 0;
  for (int i = t; i < D; i += blockDim.x) a1 += sx[i];
  float mf = block_sum(a1, red) * (1.0f / D);
  float v = 0;
  for (int i = t; i < D; i += blockDim.x) { float d = sx[i] - mf; v += d * d; }
  float var = rbf(block_sum(v, red) * (1.0f / D));
  float sd = rbf(sqrtf(rbf(var + 1e-5f)));
  __syncthreads();
  for (int i = t; i < D; i += blockDim.x) sx[i] = rbf(rbf(sx[i] - m) / sd);
  __syncthreads();
}

__global__ void k1(const u16* x, const u16* wq, const u16* w1, const u16* b1, float* q, float* h) {
  __shared__ float sx[D]; __shared__ float red[16];
  int t = threadIdx.x;
  for (int i = t; i < D; i += blockDim.x) sx[i] = bf2f(x[i]);
  __syncthreads();
  ln512(sx, red);
  int col = blockIdx.x * 64 + (t & 63);
  int kq = t >> 6;
  __shared__ float part[4][64];
  if (blockIdx.x < 64) {
    float a = 0;
    for (int k = kq * 128; k < kq * 128 + 128; ++k) a += sx[k] * bf2f(wq[(size_t)k * 4096 + col]);
    part[kq][t & 63] = a;
    __syncthreads();
    if (t < 64) q[col] = rbf(part[0][t] + part[1][t] + part[2][t] + part[3][t]);
  } else {
    ln512(sx, red);
    col -= 4096;
    float a = 0;
    for (int k = kq * 128; k < kq * 128 + 128; ++k) a += sx[k] * bf2f(w1[(size_t)k * F + col]);
    part[kq][t & 63] = a;
    __syncthreads();
    if (t < 64) {
      float z = rbf(rbf(part[0][t] + part[1][t] + part[2][t] + part[3][t]) + bf2f(b1[col]));
      float u = 0.7978845608028654f * (z + 0.044715f * z * z * z);
      float th = tanhf(u);
      h[col] = rbf(0.5f * z * (1.f + th));
    }
  }
}

__global__ void k2(const u16* kc, const u16* vc, const float* q, float* pm, float* pl, float* po) {
  int kvh = blockIdx.x / NSPLIT, sp = blockIdx.x % NSPLIT;
  __shared__ float sq[4][DH]; __shared__ float sc[4][KPS]; __shared__ float sm[4], sl[4];
  int t = threadIdx.x;
  for (int i = t; i < 512; i += 256) sq[i / DH][i % DH] = q[kvh * 512 + i];
  __syncthreads();
  const u16* kb = kc + ((size_t)kvh * SK + (SK - W) + sp * KPS) * DH;
  const u16* vb = vc + ((size_t)kvh * SK + (SK - W) + sp * KPS) * DH;
  for (int i = t; i < 512; i += 256) {
    int hh = i / KPS, key = i % KPS;
    float a = 0;
    for (int d = 0; d < DH; ++d) a += sq[hh][d] * bf2f(kb[key * DH + d]);
    sc[hh][key] = rbf(rbf(a) * 0.08838834764831845f);
  }
  __syncthreads();
  int w = t >> 6, l = t & 63;
  {
    float m = fmaxf(sc[w][l], sc[w][l + 64]);
    for (int s = 32; s >= 1; s >>= 1) m = fmaxf(m, __shfl_xor(m, s));
    float e0 = __expf(sc[w][l] - m), e1 = __expf(sc[w][l + 64] - m);
    sc[w][l] = e0; sc[w][l + 64] = e1;
    float s2 = e0 + e1;
    for (int s = 32; s >= 1; s >>= 1) s2 += __shfl_xor(s2, s);
    if (l == 0) { sm[w] = m; sl[w] = s2; }
  }
  __syncthreads();
  for (int i = t; i < 512; i += 256) {
    int hh = i / DH, d = i % DH;
    float a = 0;
    for (int key = 0; key < KPS; ++key) a += sc[hh][key] * bf2f(vb[key * DH + d]);
    po[((size_t)(kvh * 4 + hh) * NSPLIT + sp) * DH + d] = a;
  }
  if (t < 4) { pm[(kvh * 4 + t) * NSPLIT + sp] = sm[t]; pl[(kvh * 4 + t) * NSPLIT + sp] = sl[t]; }
}

__global__ void k3(const float* pm, const float* pl, const float* po, const u16* wo, float* ps) {
  int h = blockIdx.x, t = threadIdx.x;
  __shared__ float so[DH];
  if (t < DH) {
    float M = -INFINITY;
    for (int s = 0; s < NSPLIT; ++s) M = fmaxf(M, pm[h * NSPLIT + s]);
    float L = 0, o = 0;
    for (int s = 0; s < NSPLIT; ++s) { float w = __expf(pm[h * NSPLIT + s] - M); L += w * pl[h * NSPLIT + s]; o += w * po[((size_t)h * NSPLIT + s) * DH + t]; }
    so[t] = rbf(o / L);
  }
  __syncthreads();
  for (int c = t; c < D; c += blockDim.x) {
    float a = 0;
    for (int d = 0; d < DH; ++d) a += so[d] * bf2f(wo[(size_t)(h * DH + d) * D + c]);
    ps[h * D + c] = a;
  }
}

__global__ void k4(const u16* x, const float* ps, const float* h, const u16* w2, const u16* b2, u16* out) {
  int t = threadIdx.x, col = blockIdx.x * 64 + (t & 63), w = t >> 6;
  __shared__ float part[4][64];
  float a = 0;
  for (int k = w * 448; k < w * 448 + 448; ++k) a += h[k] * bf2f(w2[(size_t)k * D + col]);
  part[w][t & 63] = a;
  __syncthreads();
  if (t < 64) {
    float z = rbf(rbf(part[0][t] + part[1][t] + part[2][t] + part[3][t]) + bf2f(b2[col]));
    float g = rbf(z / (1.f + __expf(-z)));
    float s = 0;
    for (int i = 0; i < HQ; ++i) s += ps[i * D + col];
    s = rbf(s);
    out[col] = f2bf(bf2f(x[col]) + rbf(s * g));
  }
}

extern "C" void kernel_launch(void* const* d_in, const int* in_sizes, int n_in,
                              void* d_out, int out_size, void* d_ws, size_t ws_size,
                              hipStream_t stream) {
  const u16* x = (const u16*)d_in[0];
  const u16* kc = (const u16*)d_in[1];
  const u16* vc = (const u16*)d_in[2];
  const u16* wq = (const u16*)d_in[3];
  const u16* wo = (const u16*)d_in[4];
  const u16* w1 = (const u16*)d_in[6];
  const u16* b1 = (const u16*)d_in[7];
  const u16* w2 = (const u16*)d_in[8];
  const u16* b2 = (const u16*)d_in[9];
  float* ws = (float*)d_ws;
  float* q = ws; float* h = q + 4096; float* pm = h + 2048; float* pl = pm + 1024; float* po = pl + 1024; float* ps = po + 32 * 32 * 128;
  k1<<<64 + 28, 256, 0, stream>>>(x, wq, w1, b1, q, h);
  k2<<<256, 256, 0, stream>>>(kc, vc, q, pm, pl, po);
  k3<<<32, 256, 0, stream>>>(pm, pl, po, wo, ps);
  k4<<<8, 256, 0, stream>>>(x, ps, h, w2, b2, (u16*)d_out);
}
```

```cpp
#include <hip/hip_runtime.h>
#include <hip/hip_bf16.h>

#define LAS __attribute__((address_space(3)))
#define GAS __attribute__((address_space(1)))
typedef unsigned short u16;
typedef unsigned v4u __attribute__((ext_vector_type(4)));
typedef unsigned v2u __attribute__((ext_vector_type(2)));
typedef float f32x4 __attribute__((ext_vector_type(4)));
typedef short s16x4 __attribute__((ext_vector_type(4)));
typedef short s16x8 __attribute__((ext_vector_type(8)));
typedef __bf16 bf16x8 __attribute__((ext_vector_type(8)));
typedef __bf16 bf16x2 __attribute__((ext_vector_type(2)));

#define KN_NT 1
#define KN_VMODE 1
#define KN_STAMPS 0

constexpr int D = 512, SK = 65536, DH = 128, WIN = 4096, FF = 1792;
constexpr int NB = 256, NTHR = 384;
constexpr int L_WQ = 0, L_K = 16384, L_V = 49152, L_XN = 81920, L_Q = 82944, L_P = 83968, L_ST = 84992, L_FLAG = 85248, L_END = 85504;
constexpr int W_E = 0;
constexpr int W_Q = 4096;
constexpr int W_ML = W_Q + 16384;
constexpr int W_O = W_ML + 16384;
constexpr int W_S = W_O + 1048576;
constexpr int W_M = W_S + 1048576;
constexpr int W_END = W_M + 224 * 4096;
constexpr size_t W_STAMP = (size_t)64 << 20;
constexpr unsigned SPIN_MAX = 1u << 17;

__device__ __forceinline__ float bf2f(unsigned b) { return __uint_as_float(b << 16); }
__device__ __forceinline__ float bflo(unsigned w) { return __uint_as_float(w << 16); }
__device__ __forceinline__ float bfhi(unsigned w) { return __uint_as_float(w & 0xffff0000u); }
__device__ __forceinline__ unsigned pkbf(float lo, float hi) {
  bf16x2 r; r[0] = (__bf16)lo; r[1] = (__bf16)hi; return __builtin_bit_cast(unsigned, r);
}
__device__ __forceinline__ float rbf(float f) { return bflo(pkbf(f, f)); }
__device__ __forceinline__ float dot2(unsigned a, unsigned b, float c) {
  return __builtin_amdgcn_fdot2_f32_bf16(__builtin_bit_cast(bf16x2, a), __builtin_bit_cast(bf16x2, b), c, false);
}
template <int CTRL> __device__ __forceinline__ float dpp(float x) {
  return __builtin_bit_cast(float, __builtin_amdgcn_mov_dpp(__builtin_bit_cast(int, x), CTRL, 0xf, 0xf, true));
}
constexpr int XOR1 = 0xB1, XOR2 = 0x4E, XOR7 = 0x141, XOR15 = 0x140, XOR8 = 0x128;
__device__ __forceinline__ float x16_sum(float x) {
  auto s = __builtin_amdgcn_permlane16_swap(__float_as_uint(x), __float_as_uint(x), false, false);
  return __uint_as_float(s[0]) + __uint_as_float(s[1]);
}
__device__ __forceinline__ float x32_sum(float x) {
  auto s = __builtin_amdgcn_permlane32_swap(__float_as_uint(x), __float_as_uint(x), false, false);
  return __uint_as_float(s[0]) + __uint_as_float(s[1]);
}
__device__ __forceinline__ float x16_max(float x) {
  auto s = __builtin_amdgcn_permlane16_swap(__float_as_uint(x), __float_as_uint(x), false, false);
  return fmaxf(__uint_as_float(s[0]), __uint_as_float(s[1]));
}
__device__ __forceinline__ float x32_max(float x) {
  auto s = __builtin_amdgcn_permlane32_swap(__float_as_uint(x), __float_as_uint(x), false, false);
  return fmaxf(__uint_as_float(s[0]), __uint_as_float(s[1]));
}
__device__ __forceinline__ float wave_sum(float v) {
  v += dpp<XOR1>(v); v += dpp<XOR2>(v); v += dpp<XOR7>(v); v += dpp<XOR15>(v);
  v = x16_sum(v); v = x32_sum(v); return v;
}
__device__ __forceinline__ f32x4 mfma16(v4u a, v4u b, f32x4 c) {
  return __builtin_amdgcn_mfma_f32_16x16x32_bf16(__builtin_bit_cast(bf16x8, a), __builtin_bit_cast(bf16x8, b), c, 0, 0, 0);
}
__device__ __forceinline__ v4u ln_packed(v4u x) {
  float v[8];
#pragma unroll
  for (int i = 0; i < 4; ++i) { v[2 * i] = bflo(x[i]); v[2 * i + 1] = bfhi(x[i]); }
  float s = ((v[0] + v[1]) + (v[2] + v[3])) + ((v[4] + v[5]) + (v[6] + v[7]));
  const float mean = wave_sum(s) * (1.0f / D);
  float q = 0.f;
#pragma unroll
  for (int i = 0; i < 8; ++i) { const float d = v[i] - mean; q = fmaf(d, d, q); }
  const float var = rbf(wave_sum(q) * (1.0f / D));
  const float m = rbf(mean);
  const float sd = rbf(__builtin_sqrtf(rbf(var + 1e-5f)));
  const float inv = 1.0f / sd;
  v4u r;
#pragma unroll
  for (int i = 0; i < 4; ++i) r[i] = pkbf(rbf(v[2 * i] - m) * inv, rbf(v[2 * i + 1] - m) * inv);
  return r;
}
#define WAIT_VM(n) asm volatile("s_waitcnt vmcnt(" #n ")" ::: "memory")
#define WAIT_LGKM() asm volatile("s_waitcnt lgkmcnt(0)" ::: "memory")
#define BARRIER() do { asm volatile("s_waitcnt lgkmcnt(0)\n\ts_barrier" ::: "memory"); } while (0)
#define NTS " nt"
__device__ __forceinline__ void glds4(const void* sbase, unsigned v0, unsigned v1, unsigned v2, unsigned v3, unsigned ldsaddr) {
  unsigned keep;
  asm volatile("s_mov_b32 %0, m0\n\ts_mov_b32 m0, %6\n\ts_nop 0\n\t"
               "global_load_lds_dwordx4 %1, %5" NTS "\n\t"
               "global_load_lds_dwordx4 %2, %5 offset:1024" NTS "\n\t"
               "global_load_lds_dwordx4 %3, %5 offset:2048" NTS "\n\t"
               "global_load_lds_dwordx4 %4, %5 offset:3072" NTS "\n\t"
               "s_mov_b32 m0, %0"
               : "=&s"(keep) : "v"(v0), "v"(v1), "v"(v2), "v"(v3), "s"(sbase), "s"(ldsaddr) : "memory");
}
__device__ __forceinline__ v4u ldw(const void* p) {
  return __builtin_nontemporal_load((const GAS v4u*)p);
}
#define STAMP(i) do { } while (0)

struct Params {
  const u16* x; const u16* kc; const u16* vc; const u16* wq; const u16* wo;
  const u16* w1; const u16* b1; const u16* w2; const u16* b2; u16* out; unsigned char* ws;
};

__global__ void __launch_bounds__(NTHR, 2) fused_kernel(Params p) {
  __shared__ __attribute__((aligned(16))) unsigned char lds[L_END];
  LAS unsigned char* L = (LAS unsigned char*)lds;
  const unsigned lbase = (unsigned)(size_t)L;
  const int tid = threadIdx.x, lane = tid & 63, wave = __builtin_amdgcn_readfirstlane(tid >> 6);
  const int b = blockIdx.x, h = b & 7, j = b >> 3;
  const int g = lane >> 4, c = lane & 15;
  STAMP(0);
  const __amdgpu_buffer_rsrc_t rs = __builtin_amdgcn_make_buffer_rsrc((void*)p.ws, 0, W_END, 0x00020000);
  if (wave < 4) {
    const int w = wave;
    v2u ev;
    asm volatile("global_load_dwordx2 %0, %1, %2 sc1" : "=v"(ev) : "v"(0u), "s"(p.ws) : "memory");
    {
      const unsigned char* sb = (const unsigned char*)p.wq + (size_t)(h * 512 + j * 16) * 2 + (size_t)w * 128 * 8192;
      const unsigned v0 = (unsigned)(lane >> 1) * 8192u + (unsigned)(lane & 1) * 16u;
      glds4(sb, v0, v0 + 32u * 8192u - 1024u, v0 + 64u * 8192u - 2048u, v0 + 96u * 8192u - 3072u, lbase + L_WQ + w * 4096);
    }
    const size_t kvoff = ((size_t)h * SK + (SK - WIN) + (size_t)j * 128 + (size_t)w * 32) * 256;
    {
      const unsigned char* sb = (const unsigned char*)p.kc + kvoff;
      const unsigned v0 = (unsigned)g * 256u + (unsigned)((c ^ g) * 16);
      glds4(sb, v0, v0 ^ 64u, v0 ^ 128u, v0 ^ 192u, lbase + L_K + w * 8192);
      glds4(sb + 4096, v0, v0 ^ 64u, v0 ^ 128u, v0 ^ 192u, lbase + L_K + w * 8192 + 4096);
    }
    const unsigned char* vsb = (const unsigned char*)p.vc + kvoff;
    const unsigned vv0 = (unsigned)g * 256u + (unsigned)((c ^ (g << 2)) * 16);
#define ISSUE_V() do { glds4(vsb, vv0, vv0 ^ 16u, vv0 ^ 32u, vv0 ^ 48u, lbase + L_V + w * 8192); \
                       glds4(vsb + 4096, vv0, vv0 ^ 16u, vv0 ^ 32u, vv0 ^ 48u, lbase + L_V + w * 8192 + 4096); } while (0)
    asm volatile("s_waitcnt vmcnt(8)" : "+v"(ev) :: "memory");
    const unsigned long long E = (unsigned long long)__builtin_amdgcn_readfirstlane(ev[0]) | ((unsigned long long)__builtin_amdgcn_readfirstlane(ev[1]) << 32);
    const unsigned long long T = E * 6364136223846793005ull + 1442695040888963407ull;
    const unsigned tlo = (unsigned)T, thi = (unsigned)(T >> 32);
    STAMP(1);
    BARRIER();
    ISSUE_V();
    WAIT_VM(8);
    STAMP(2);
    v4u kf[2][4];
#pragma unroll
    for (int T2 = 0; T2 < 2; ++T2)
#pragma unroll
      for (int s = 0; s < 4; ++s)
        kf[T2][s] = *(const LAS v4u*)(L + L_K + (w * 32 + T2 * 16 + c) * 256 + (((4 * s + g) ^ c) * 16));
    BARRIER();
    STAMP(3);
    f32x4 sc[2] = {{0.f, 0.f, 0.f, 0.f}, {0.f, 0.f, 0.f, 0.f}};
#pragma unroll
    for (int s = 0; s < 4; ++s) {
      const v4u qf = *(const LAS v4u*)(L + L_Q + ((s * 4 + g) * 4 + (c & 3)) * 16);
      sc[0] = mfma16(kf[0][s], qf, sc[0]);
      sc[1] = mfma16(kf[1][s], qf, sc[1]);
    }
    float mx = fmaxf(fmaxf(fmaxf(sc[0][0], sc[0][1]), fmaxf(sc[0][2], sc[0][3])), fmaxf(fmaxf(sc[1][0], sc[1][1]), fmaxf(sc[1][2], sc[1][3])));
    mx = x16_max(mx); mx = x32_max(mx);
    if (lane < 4) *(LAS float*)(L + L_ST + (w * 4 + lane) * 4) = mx;
    BARRIER();
    const f32x4 m4 = {*(const LAS float*)(L + L_ST + (0 * 4 + (c & 3)) * 4), *(const LAS float*)(L + L_ST + (1 * 4 + (c & 3)) * 4),
                      *(const LAS float*)(L + L_ST + (2 * 4 + (c & 3)) * 4), *(const LAS float*)(L + L_ST + (3 * 4 + (c & 3)) * 4)};
    const float M = fmaxf(fmaxf(m4[0], m4[1]), fmaxf(m4[2], m4[3]));
    constexpr float C2 = 0.08838834764831845f * 1.4426950408889634f;
    const float mc = M * C2;
    float lsum = 0.f;
#pragma unroll
    for (int T2 = 0; T2 < 2; ++T2) {
      float e[4];
#pragma unroll
      for (int r = 0; r < 4; ++r) e[r] = __builtin_amdgcn_exp2f(fmaf(sc[T2][r], C2, -mc));
      v2u pk; pk[0] = pkbf(e[0], e[1]); pk[1] = pkbf(e[2], e[3]);
      lsum += (bflo(pk[0]) + bfhi(pk[0])) + (bflo(pk[1]) + bfhi(pk[1]));
      if (c < 4) *(LAS v2u*)(L + L_P + ((w * 4 + 2 * T2 + (g >> 1)) * 4 + c) * 16 + (g & 1) * 8) = pk;
    }
    lsum = x16_sum(lsum); lsum = x32_sum(lsum);
    if (lane < 4) *(LAS float*)(L + L_ST + 64 + (w * 4 + lane) * 4) = lsum;
    WAIT_VM(0);
    STAMP(4);
    BARRIER();
    STAMP(5);
    f32x4 oa[2] = {{0.f, 0.f, 0.f, 0.f}, {0.f, 0.f, 0.f, 0.f}};
    {
      const int q4 = c >> 2, pp = c & 3;
#pragma unroll
      for (int s = 0; s < 4; ++s) {
        const v4u pf = *(const LAS v4u*)(L + L_P + ((s * 4 + g) * 4 + (c & 3)) * 16);
#pragma unroll
        for (int tt = 0; tt < 2; ++tt) {
          const int t = 2 * w + tt;
          s16x4 a[2];
#pragma unroll
          for (int t2 = 0; t2 < 2; ++t2) {
            const int row = 32 * s + 8 * g + 4 * t2 + q4;
            const int mask = (q4 << 2) | ((2 * g + t2) & 3);
            a[t2] = __builtin_amdgcn_ds_read_tr16_b64_v4i16((LAS s16x4*)(L + L_V + row * 256 + (((2 * t + (pp >> 1)) ^ mask) * 16) + (pp & 1) * 8));
          }
          const s16x8 af = __builtin_shufflevector(a[0], a[1], 0, 1, 2, 3, 4, 5, 6, 7);
          oa[tt] = mfma16(__builtin_bit_cast(v4u, af), pf, oa[tt]);
        }
      }
    }
    {
      const int head = c & 3, k = c >> 2, tt = k >> 1, pair = k & 1;
      const f32x4 o = tt ? oa[1] : oa[0];
      v4u gr; gr[0] = __float_as_uint(pair ? o[2] : o[0]); gr[1] = tlo; gr[2] = __float_as_uint(pair ? o[3] : o[1]); gr[3] = thi;
      const int off = W_O + ((((h * 32 + j) * 32 + head * 8 + 2 * w + tt) * 8) + 2 * g + pair) * 16;
      __builtin_amdgcn_raw_buffer_store_b128(gr, rs, off, 0, 16);
      if (w == 0 && lane < 4) {
        const float l4 = (*(const LAS float*)(L + L_ST + 64 + (0 * 4 + lane) * 4) + *(const LAS float*)(L + L_ST + 64 + (1 * 4 + lane) * 4)) +
                         (*(const LAS float*)(L + L_ST + 64 + (2 * 4 + lane) * 4) + *(const LAS float*)(L + L_ST + 64 + (3 * 4 + lane) * 4));
        v4u ml; ml[0] = __float_as_uint(mc); ml[1] = tlo; ml[2] = __float_as_uint(l4); ml[3] = thi;
        __builtin_amdgcn_raw_buffer_store_b128(ml, rs, W_ML + ((h * 32 + j) * 4 + lane) * 16, 0, 16);
      }
    }
    STAMP(6);
  } else if (wave == 4) {
    __builtin_amdgcn_s_setprio(2);
    const v4u xr = *(const GAS v4u*)(p.x + lane * 8);
    const v2u ev = __builtin_amdgcn_raw_buffer_load_b64(rs, W_E, 0, 16);
    const unsigned long long E = (unsigned long long)__builtin_amdgcn_readfirstlane(ev[0]) | ((unsigned long long)__builtin_amdgcn_readfirstlane(ev[1]) << 32);
    const unsigned long long T = E * 6364136223846793005ull + 1442695040888963407ull;
    const unsigned tlo = (unsigned)T, thi = (unsigned)(T >> 32);
    if (lane == 0) *(volatile LAS unsigned*)(L + L_FLAG) = 0u;
    const v4u xn = ln_packed(xr);
    *(LAS v4u*)(L + L_XN + lane * 16) = xn;
    WAIT_LGKM();
    v4u xf[16];
#pragma unroll
    for (int s = 0; s < 16; ++s) xf[s] = *(const LAS v4u*)(L + L_XN + (4 * s + g) * 16);
    STAMP(1);
    BARRIER();
    STAMP(2);
    f32x4 a0 = {0.f, 0.f, 0.f, 0.f}, a1 = {0.f, 0.f, 0.f, 0.f};
    {
      const int q4 = c >> 2, pp = c & 3;
      LAS unsigned char* wb = L + L_WQ + (8 * g + q4) * 32 + pp * 8;
#pragma unroll
      for (int s = 0; s < 16; ++s) {
        const s16x4 lo = __builtin_amdgcn_ds_read_tr16_b64_v4i16((LAS s16x4*)(wb + s * 1024));
        const s16x4 hi = __builtin_amdgcn_ds_read_tr16_b64_v4i16((LAS s16x4*)(wb + s * 1024 + 128));
        const s16x8 af = __builtin_shufflevector(lo, hi, 0, 1, 2, 3, 4, 5, 6, 7);
        if (s & 1) a1 = mfma16(__builtin_bit_cast(v4u, af), xf[s], a1);
        else a0 = mfma16(__builtin_bit_cast(v4u, af), xf[s], a0);
      }
    }
    {
      v4u gr; gr[0] = pkbf(a0[0] + a1[0], a0[1] + a1[1]); gr[1] = tlo; gr[2] = pkbf(a0[2] + a1[2], a0[3] + a1[3]); gr[3] = thi;
      if (c == 0) __builtin_amdgcn_raw_buffer_store_b128(gr, rs, W_Q + ((h * 32 + j) * 4 + g) * 16, 0, 16);
    }
    STAMP(3);
    {
      for (unsigned spin = 0; spin < SPIN_MAX; ++spin) {
        asm volatile("" ::: "memory");
        const v4u q0 = __builtin_amdgcn_raw_buffer_load_b128(rs, W_Q + h * 2048 + lane * 16, 0, 16);
        const v4u q1 = __builtin_amdgcn_raw_buffer_load_b128(rs, W_Q + h * 2048 + 1024 + lane * 16, 0, 16);
        const bool ok = q0[1] == tlo && q0[3] == thi && q1[1] == tlo && q1[3] == thi;
        if (__all(ok)) {
#pragma unroll
          for (int u = 0; u < 2; ++u) {
            const v4u q = u ? q1 : q0;
            const int G = u * 64 + lane, jj = G >> 2, g2 = G & 3;
            const int head = jj >> 3, dim0 = (jj & 7) * 16 + 4 * g2;
            v2u d; d[0] = q[0]; d[1] = q[2];
            *(LAS v2u*)(L + L_Q + (((dim0 >> 5) * 4 + ((dim0 >> 3) & 3)) * 4 + head) * 16 + ((dim0 >> 2) & 1) * 8) = d;
          }
          break;
        }
      }
    }
    STAMP(4);
    BARRIER();
    v4u wr[16];
    {
      const unsigned char* wb = (const unsigned char*)p.wo + (size_t)(h * 512 + j * 16) * 1024 + lane * 16;
#pragma unroll
      for (int r = 0; r < 16; ++r) wr[r] = ldw(wb + r * 1024);
      __builtin_amdgcn_sched_barrier(0);
    }
    BARRIER();
    BARRIER();
    STAMP(5);
    unsigned wp[8][8];
#pragma unroll
    for (int i = 0; i < 8; ++i)
#pragma unroll
      for (int d = 0; d < 4; ++d) {
        wp[i][2 * d] = __builtin_amdgcn_perm(wr[2 * i + 1][d], wr[2 * i][d], 0x05040100u);
        wp[i][2 * d + 1] = __builtin_amdgcn_perm(wr[2 * i + 1][d], wr[2 * i][d], 0x07060302u);
      }
    STAMP(6);
    unsigned op = 0;
    {
      const int jj = lane >> 3, gi = lane & 7;
      for (unsigned spin = 0; spin < SPIN_MAX; ++spin) {
        asm volatile("" ::: "memory");
        v4u o[4], ml[4];
#pragma unroll
        for (int i = 0; i < 4; ++i) {
          o[i] = __builtin_amdgcn_raw_buffer_load_b128(rs, W_O + ((((h * 32 + jj + 8 * i) * 32 + j) * 8) + gi) * 16, 0, 16);
          ml[i] = __builtin_amdgcn_raw_buffer_load_b128(rs, W_ML + ((h * 32 + jj + 8 * i) * 4 + (j >> 3)) * 16, 0, 16);
        }
        bool ok = true;
#pragma unroll
        for (int i = 0; i < 4; ++i) ok = ok && o[i][1] == tlo && o[i][3] == thi && ml[i][1] == tlo && ml[i][3] == thi;
        if (__all(ok)) {
          float M = fmaxf(fmaxf(__uint_as_float(ml[0][0]), __uint_as_float(ml[1][0])), fmaxf(__uint_as_float(ml[2][0]), __uint_as_float(ml[3][0])));
          M = fmaxf(M, dpp<XOR8>(M)); M = x16_max(M); M = x32_max(M);
          float Ls = 0.f, o0 = 0.f, o1 = 0.f;
#pragma unroll
          for (int i = 0; i < 4; ++i) {
            const float wt = __builtin_amdgcn_exp2f(__uint_as_float(ml[i][0]) - M);
            Ls = fmaf(wt, __uint_as_float(ml[i][2]), Ls); o0 = fmaf(wt, __uint_as_float(o[i][0]), o0); o1 = fmaf(wt, __uint_as_float(o[i][2]), o1);
          }
          Ls += dpp<XOR8>(Ls); o0 += dpp<XOR8>(o0); o1 += dpp<XOR8>(o1);
          Ls = x16_sum(Ls); o0 = x16_sum(o0); o1 = x16_sum(o1);
          Ls = x32_sum(Ls); o0 = x32_sum(o0); o1 = x32_sum(o1);
          const float inv = 1.0f / Ls;
          op = pkbf(o0 * inv, o1 * inv);
          break;
        }
      }
    }
    STAMP(7);
    {
      float acc[8];
#pragma unroll
      for (int e = 0; e < 8; ++e) acc[e] = 0.f;
#pragma unroll
      for (int i = 0; i < 8; ++i) {
        const unsigned oi = __builtin_amdgcn_readlane(op, i);
#pragma unroll
        for (int e = 0; e < 8; ++e) acc[e] = dot2(wp[i][e], oi, acc[e]);
      }
#pragma unroll
      for (int i = 0; i < 4; ++i) {
        v4u gr; gr[0] = __float_as_uint(acc[2 * i]); gr[1] = tlo; gr[2] = __float_as_uint(acc[2 * i + 1]); gr[3] = thi;
        __builtin_amdgcn_raw_buffer_store_b128(gr, rs, W_S + (b * 256 + i * 64 + lane) * 16, 0, 16);
      }
    }
    STAMP(8);
    {
      const int cc = (b & 7) * 32 + (b >> 3);
      const int col = 8 * (cc & 63) + 2 * (cc >> 6);
      const unsigned xw = *(const GAS unsigned*)(p.x + col);
      const unsigned b2w = *(const GAS unsigned*)(p.b2 + col);
      for (unsigned spin = 0; spin < SPIN_MAX; ++spin) {
        asm volatile("" ::: "memory");
        v4u s[4], m[4];
#pragma unroll
        for (int i = 0; i < 4; ++i) {
          s[i] = __builtin_amdgcn_raw_buffer_load_b128(rs, W_S + ((lane + 64 * i) * 256 + cc) * 16, 0, 16);
          const int mp = (i == 3 && lane >= 32) ? lane - 32 : lane + 64 * i;
          m[i] = __builtin_amdgcn_raw_buffer_load_b128(rs, W_M + (mp * 256 + cc) * 16, 0, 16);
        }
        bool ok = true;
#pragma unroll
        for (int i = 0; i < 4; ++i) ok = ok && s[i][1] == tlo && s[i][3] == thi && m[i][1] == tlo && m[i][3] == thi;
        if (__all(ok) || spin == SPIN_MAX - 1) {
          float s0 = (__uint_as_float(s[0][0]) + __uint_as_float(s[1][0])) + (__uint_as_float(s[2][0]) + __uint_as_float(s[3][0]));
          float s1 = (__uint_as_float(s[0][2]) + __uint_as_float(s[1][2])) + (__uint_as_float(s[2][2]) + __uint_as_float(s[3][2]));
          const float z3 = lane < 32 ? 1.f : 0.f;
          float m0 = (__uint_as_float(m[0][0]) + __uint_as_float(m[1][0])) + (__uint_as_float(m[2][0]) + (lane < 32 ? __uint_as_float(m[3][0]) : 0.f));
          float m1 = (__uint_as_float(m[0][2]) + __uint_as_float(m[1][2])) + (__uint_as_float(m[2][2]) + (lane < 32 ? __uint_as_float(m[3][2]) : 0.f));
          (void)z3;
          s0 = wave_sum(s0); s1 = wave_sum(s1); m0 = wave_sum(m0); m1 = wave_sum(m1);
          const float z0 = rbf(rbf(m0) + bflo(b2w)), z1 = rbf(rbf(m1) + bfhi(b2w));
          const float g0 = rbf(z0 * __builtin_amdgcn_rcpf(1.f + __builtin_amdgcn_exp2f(-1.4426950408889634f * z0)));
          const float g1 = rbf(z1 * __builtin_amdgcn_rcpf(1.f + __builtin_amdgcn_exp2f(-1.4426950408889634f * z1)));
          const float r0 = bflo(xw) + rbf(rbf(s0) * g0), r1 = bfhi(xw) + rbf(rbf(s1) * g1);
          if (lane == 0) *(GAS unsigned*)(p.out + col) = pkbf(r0, r1);
          break;
        }
      }
      if (b == 0 && lane == 0) __hip_atomic_store((GAS unsigned long long*)(p.ws + W_E), T, __ATOMIC_RELAXED, __HIP_MEMORY_SCOPE_AGENT);
    }
    STAMP(9);
  } else {
    const bool act = j < 28;
    const int cg = h * 28 + j;
    v4u xr = {0u, 0u, 0u, 0u};
    unsigned tlo = 0, thi = 0;
    v4u y = {0u, 0u, 0u, 0u};
    if (act) {
      xr = *(const GAS v4u*)(p.x + lane * 8);
      const v2u ev = __builtin_amdgcn_raw_buffer_load_b64(rs, W_E, 0, 16);
      const unsigned long long E = (unsigned long long)__builtin_amdgcn_readfirstlane(ev[0]) | ((unsigned long long)__builtin_amdgcn_readfirstlane(ev[1]) << 32);
      const unsigned long long T = E * 6364136223846793005ull + 1442695040888963407ull;
      tlo = (unsigned)T; thi = (unsigned)(T >> 32);
      y = ln_packed(ln_packed(xr));
    }
    BARRIER();
    BARRIER();
    v4u a[8], w2r[8]; v4u b1v = {0u, 0u, 0u, 0u};
    if (act) {
      const unsigned char* w1b = (const unsigned char*)p.w1 + (size_t)(lane * 8) * (FF * 2) + cg * 16;
#pragma unroll
      for (int i = 0; i < 8; ++i) a[i] = ldw(w1b + i * (FF * 2));
      const unsigned char* w2b = (const unsigned char*)p.w2 + (size_t)(cg * 8) * 1024 + lane * 16;
#pragma unroll
      for (int i = 0; i < 8; ++i) w2r[i] = ldw(w2b + i * 1024);
      b1v = *(const GAS v4u*)(p.b1 + cg * 8);
      __builtin_amdgcn_sched_barrier(0);
    }
    BARRIER();
    BARRIER();
    STAMP(5);
    if (act) {
      float acc[8];
#pragma unroll
      for (int e = 0; e < 8; ++e) acc[e] = 0.f;
#pragma unroll
      for (int i = 0; i < 4; ++i)
#pragma unroll
        for (int d = 0; d < 4; ++d) {
          acc[2 * d] = dot2(__builtin_amdgcn_perm(a[2 * i + 1][d], a[2 * i][d], 0x05040100u), y[i], acc[2 * d]);
          acc[2 * d + 1] = dot2(__builtin_amdgcn_perm(a[2 * i + 1][d], a[2 * i][d], 0x07060302u), y[i], acc[2 * d + 1]);
        }
#pragma unroll
      for (int e = 0; e < 8; ++e) acc[e] = wave_sum(acc[e]);
      const int e = lane & 7;
      float av = acc[0];
#pragma unroll
      for (int i = 1; i < 8; ++i) av = (e == i) ? acc[i] : av;
      const unsigned bw = (e >> 1) == 0 ? b1v[0] : (e >> 1) == 1 ? b1v[1] : (e >> 1) == 2 ? b1v[2] : b1v[3];
      const float z = rbf(rbf(av) + ((e & 1) ? bfhi(bw) : bflo(bw)));
      const float u = 0.7978845608028654f * (z + 0.044715f * z * z * z);
      const float th = 1.f - 2.f * __builtin_amdgcn_rcpf(1.f + __builtin_amdgcn_exp2f(2.885390081777927f * u));
      const float hv = 0.5f * z * (1.f + th);
      const float hn = dpp<XOR1>(hv);
      const unsigned hp = pkbf(hv, hn);
      float acc2[8];
#pragma unroll
      for (int i = 0; i < 8; ++i) acc2[i] = 0.f;
#pragma unroll
      for (int i = 0; i < 4; ++i) {
        const unsigned hi2 = __builtin_amdgcn_readlane(hp, 2 * i);
#pragma unroll
        for (int d = 0; d < 4; ++d) {
          acc2[2 * d] = dot2(__builtin_amdgcn_perm(w2r[2 * i + 1][d], w2r[2 * i][d], 0x05040100u), hi2, acc2[2 * d]);
          acc2[2 * d + 1] = dot2(__builtin_amdgcn_perm(w2r[2 * i + 1][d], w2r[2 * i][d], 0x07060302u), hi2, acc2[2 * d + 1]);
        }
      }
#pragma unroll
      for (int i = 0; i < 4; ++i) {
        v4u gr; gr[0] = __float_as_uint(acc2[2 * i]); gr[1] = tlo; gr[2] = __float_as_uint(acc2[2 * i + 1]); gr[3] = thi;
        __builtin_amdgcn_raw_buffer_store_b128(gr, rs, W_M + (cg * 256 + i * 64 + lane) * 16, 0, 16);
      }
    }
    STAMP(6);
  }
}

extern "C" void kernel_launch(void* const* d_in, const int* in_sizes, int n_in,
                              void* d_out, int out_size, void* d_ws, size_t ws_size,
                              hipStream_t stream) {
  Params p;
  p.x = (const u16*)d_in[0]; p.kc = (const u16*)d_in[1]; p.vc = (const u16*)d_in[2]; p.wq = (const u16*)d_in[3]; p.wo = (const u16*)d_in[4];
  p.w1 = (const u16*)d_in[6]; p.b1 = (const u16*)d_in[7]; p.w2 = (const u16*)d_in[8]; p.b2 = (const u16*)d_in[9];
  p.out = (u16*)d_out; p.ws = (unsigned char*)d_ws;
  fused_kernel<<<NB, NTHR, 0, stream>>>(p);
}
```

```cpp
#include <hip/hip_runtime.h>
#include <hip/hip_bf16.h>

#define LAS __attribute__((address_space(3)))
#define GAS __attribute__((address_space(1)))
typedef unsigned short u16;
typedef unsigned v4u __attribute__((ext_vector_type(4)));
typedef unsigned v2u __attribute__((ext_vector_type(2)));
typedef float f32x4 __attribute__((ext_vector_type(4)));
typedef short s16x4 __attribute__((ext_vector_type(4)));
typedef short s16x8 __attribute__((ext_vector_type(8)));
typedef __bf16 bf16x8 __attribute__((ext_vector_type(8)));
typedef __bf16 bf16x2 __attribute__((ext_vector_type(2)));

#define KN_NT 1
#define KN_VMODE 1
#define KN_STAMPS 0

constexpr int D = 512, SK = 65536, DH = 128, WIN = 4096, FF = 1792;
constexpr int NB = 256, NTHR = 384;
constexpr int L_WQ = 0, L_K = 16384, L_V = 49152, L_XN = 81920, L_Q = 82944, L_P = 83968, L_ST = 84992, L_FLAG = 85248, L_ARG = 85504, L_END = 85632;
constexpr int W_E = 0;
constexpr int W_Q = 4096;
constexpr int W_ML = W_Q + 16384;
constexpr int W_O = W_ML + 16384;
constexpr int W_S = W_O + 1048576;
constexpr int W_M = W_S + 1048576;
constexpr int W_END = W_M + 224 * 4096;
constexpr size_t W_STAMP = (size_t)64 << 20;
constexpr unsigned SPIN_MAX = 1u << 17;

__device__ __forceinline__ float bf2f(unsigned b) { return __uint_as_float(b << 16); }
__device__ __forceinline__ float bflo(unsigned w) { return __uint_as_float(w << 16); }
__device__ __forceinline__ float bfhi(unsigned w) { return __uint_as_float(w & 0xffff0000u); }
__device__ __forceinline__ unsigned pkbf(float lo, float hi) {
  bf16x2 r; r[0] = (__bf16)lo; r[1] = (__bf16)hi; return __builtin_bit_cast(unsigned, r);
}
__device__ __forceinline__ float rbf(float f) { return bflo(pkbf(f, f)); }
__device__ __forceinline__ float dot2(unsigned a, unsigned b, float c) {
  return __builtin_amdgcn_fdot2_f32_bf16(__builtin_bit_cast(bf16x2, a), __builtin_bit_cast(bf16x2, b), c, false);
}
template <int CTRL> __device__ __forceinline__ float dpp(float x) {
  return __builtin_bit_cast(float, __builtin_amdgcn_mov_dpp(__builtin_bit_cast(int, x), CTRL, 0xf, 0xf, true));
}
constexpr int XOR1 = 0xB1, XOR2 = 0x4E, XOR7 = 0x141, XOR15 = 0x140, XOR8 = 0x128;
__device__ __forceinline__ float x16_sum(float x) {
  auto s = __builtin_amdgcn_permlane16_swap(__float_as_uint(x), __float_as_uint(x), false, false);
  return __uint_as_float(s[0]) + __uint_as_float(s[1]);
}
__device__ __forceinline__ float x32_sum(float x) {
  auto s = __builtin_amdgcn_permlane32_swap(__float_as_uint(x), __float_as_uint(x), false, false);
  return __uint_as_float(s[0]) + __uint_as_float(s[1]);
}
__device__ __forceinline__ float x16_max(float x) {
  auto s = __builtin_amdgcn_permlane16_swap(__float_as_uint(x), __float_as_uint(x), false, false);
  return fmaxf(__uint_as_float(s[0]), __uint_as_float(s[1]));
}
__device__ __forceinline__ float x32_max(float x) {
  auto s = __builtin_amdgcn_permlane32_swap(__float_as_uint(x), __float_as_uint(x), false, false);
  return fmaxf(__uint_as_float(s[0]), __uint_as_float(s[1]));
}
__device__ __forceinline__ float wave_sum(float v) {
  v += dpp<XOR1>(v); v += dpp<XOR2>(v); v += dpp<XOR7>(v); v += dpp<XOR15>(v);
  v = x16_sum(v); v = x32_sum(v); return v;
}
__device__ __forceinline__ f32x4 mfma16(v4u a, v4u b, f32x4 c) {
  return __builtin_amdgcn_mfma_f32_16x16x32_bf16(__builtin_bit_cast(bf16x8, a), __builtin_bit_cast(bf16x8, b), c, 0, 0, 0);
}
__device__ __forceinline__ v4u ln_packed(v4u x) {
  float v[8];
#pragma unroll
  for (int i = 0; i < 4; ++i) { v[2 * i] = bflo(x[i]); v[2 * i + 1] = bfhi(x[i]); }
  float s = ((v[0] + v[1]) + (v[2] + v[3])) + ((v[4] + v[5]) + (v[6] + v[7]));
  const float mean = wave_sum(s) * (1.0f / D);
  float q = 0.f;
#pragma unroll
  for (int i = 0; i < 8; ++i) { const float d = v[i] - mean; q = fmaf(d, d, q); }
  const float var = rbf(wave_sum(q) * (1.0f / D));
  const float m = rbf(mean);
  const float sd = rbf(__builtin_sqrtf(rbf(var + 1e-5f)));
  const float inv = 1.0f / sd;
  v4u r;
#pragma unroll
  for (int i = 0; i < 4; ++i) r[i] = pkbf(rbf(v[2 * i] - m) * inv, rbf(v[2 * i + 1] - m) * inv);
  return r;
}
#define WAIT_VM(n) asm volatile("s_waitcnt vmcnt(" #n ")" ::: "memory")
#define WAIT_LGKM() asm volatile("s_waitcnt lgkmcnt(0)" ::: "memory")
#define BARRIER() do { asm volatile("s_waitcnt lgkmcnt(0)\n\ts_barrier" ::: "memory"); } while (0)
#define NTS " nt"
__device__ __forceinline__ void glds4(const void* sbase, unsigned v0, unsigned v1, unsigned v2, unsigned v3, unsigned ldsaddr) {
  unsigned keep;
  asm volatile("s_nop 4\n\ts_mov_b32 %0, m0\n\ts_mov_b32 m0, %6\n\ts_nop 0\n\t"
               "global_load_lds_dwordx4 %1, %5" NTS "\n\t"
               "global_load_lds_dwordx4 %2, %5 offset:1024" NTS "\n\t"
               "global_load_lds_dwordx4 %3, %5 offset:2048" NTS "\n\t"
               "global_load_lds_dwordx4 %4, %5 offset:3072" NTS "\n\t"
               "s_mov_b32 m0, %0"
               : "=&s"(keep) : "v"(v0), "v"(v1), "v"(v2), "v"(v3), "s"(sbase), "s"(ldsaddr) : "memory");
}
__device__ __forceinline__ v4u ldw(const void* p) {
  return __builtin_nontemporal_load((const GAS v4u*)p);
}
#define STAMP(i) do { } while (0)

struct Params {
  const u16* x; const u16* kc; const u16* vc; const u16* wq; const u16* wo;
  const u16* w1; const u16* b1; const u16* w2; const u16* b2; u16* out; unsigned char* ws; const u16* bx;
};
template <typename T> __device__ __forceinline__ T ptr_from_lds(LAS unsigned char* L, int i) {
  const v2u v = *(const LAS v2u*)(L + L_ARG + i * 8);
  const unsigned long long a = (unsigned long long)(unsigned)__builtin_amdgcn_readfirstlane(v[0]) | ((unsigned long long)(unsigned)__builtin_amdgcn_readfirstlane(v[1]) << 32);
  return (T)a;
}

__global__ void __launch_bounds__(NTHR, 2) fused_kernel(Params pk) {
  __shared__ __attribute__((aligned(16))) unsigned char lds[L_END];
  LAS unsigned char* L = (LAS unsigned char*)lds;
  const unsigned lbase = (unsigned)(size_t)L;
  const int tid = threadIdx.x, lane = tid & 63, wave = __builtin_amdgcn_readfirstlane(tid >> 6);
  const int b = blockIdx.x, h = b & 7, j = b >> 3;
  const int g = lane >> 4, c = lane & 15;
  STAMP(0);
  if (wave == 0) {
    const GAS unsigned long long* ka = (const GAS unsigned long long*)__builtin_amdgcn_kernarg_segment_ptr();
    if (lane < 12) *(LAS unsigned long long*)(L + L_ARG + lane * 8) = ka[lane];
  }
  BARRIER();
  Params p;
  p.x = ptr_from_lds<const u16*>(L, 0); p.kc = ptr_from_lds<const u16*>(L, 1); p.vc = ptr_from_lds<const u16*>(L, 2); p.wq = ptr_from_lds<const u16*>(L, 3);
  p.wo = ptr_from_lds<const u16*>(L, 4); p.w1 = ptr_from_lds<const u16*>(L, 5); p.b1 = ptr_from_lds<const u16*>(L, 6); p.w2 = ptr_from_lds<const u16*>(L, 7);
  p.b2 = ptr_from_lds<const u16*>(L, 8); p.out = ptr_from_lds<u16*>(L, 9); p.ws = ptr_from_lds<unsigned char*>(L, 10);
  const __amdgpu_buffer_rsrc_t rs = __builtin_amdgcn_make_buffer_rsrc((void*)p.ws, 0, W_END, 0x00020000);
  if (wave < 4) {
    const int w = wave;
    v2u ev;
    asm volatile("s_nop 4\n\tglobal_load_dwordx2 %0, %1, %2 sc1" : "=v"(ev) : "v"(0u), "s"(p.ws) : "memory");
    {
      const unsigned char* sb = (const unsigned char*)p.wq + (size_t)(h * 512 + j * 16) * 2 + (size_t)w * 128 * 8192;
      const unsigned v0 = (unsigned)(lane >> 1) * 8192u + (unsigned)(lane & 1) * 16u;
      glds4(sb, v0, v0 + 32u * 8192u - 1024u, v0 + 64u * 8192u - 2048u, v0 + 96u * 8192u - 3072u, lbase + L_WQ + w * 4096);
    }
    const size_t kvoff = ((size_t)h * SK + (SK - WIN) + (size_t)j * 128 + (size_t)w * 32) * 256;
    {
      const unsigned char* sb = (const unsigned char*)p.kc + kvoff;
      const unsigned v0 = (unsigned)g * 256u + (unsigned)((c ^ g) * 16);
      glds4(sb, v0, v0 ^ 64u, v0 ^ 128u, v0 ^ 192u, lbase + L_K + w * 8192);
      glds4(sb + 4096, v0, v0 ^ 64u, v0 ^ 128u, v0 ^ 192u, lbase + L_K + w * 8192 + 4096);
    }
    const unsigned char* vsb = (const unsigned char*)p.vc + kvoff;
    const unsigned vv0 = (unsigned)g * 256u + (unsigned)((c ^ (g << 2)) * 16);
#define ISSUE_V() do { glds4(vsb, vv0, vv0 ^ 16u, vv0 ^ 32u, vv0 ^ 48u, lbase + L_V + w * 8192); \
                       glds4(vsb + 4096, vv0, vv0 ^ 16u, vv0 ^ 32u, vv0 ^ 48u, lbase + L_V + w * 8192 + 4096); } while (0)
    asm volatile("s_waitcnt vmcnt(8)" : "+v"(ev) :: "memory");
    const unsigned long long E = (unsigned long long)(unsigned)__builtin_amdgcn_readfirstlane(ev[0]) | ((unsigned long long)(unsigned)__builtin_amdgcn_readfirstlane(ev[1]) << 32);
    const unsigned long long T = E * 6364136223846793005ull + 1442695040888963407ull;
    const unsigned tlo = (unsigned)T, thi = (unsigned)(T >> 32);
    STAMP(1);
    BARRIER();
    ISSUE_V();
    WAIT_VM(8);
    STAMP(2);
    v4u kf[2][4];
#pragma unroll
    for (int T2 = 0; T2 < 2; ++T2)
#pragma unroll
      for (int s = 0; s < 4; ++s)
        kf[T2][s] = *(const LAS v4u*)(L + L_K + (w * 32 + T2 * 16 + c) * 256 + (((4 * s + g) ^ c) * 16));
    BARRIER();
    STAMP(3);
    f32x4 sc[2] = {{0.f, 0.f, 0.f, 0.f}, {0.f, 0.f, 0.f, 0.f}};
#pragma unroll
    for (int s = 0; s < 4; ++s) {
      const v4u qf = *(const LAS v4u*)(L + L_Q + ((s * 4 + g) * 4 + (c & 3)) * 16);
      sc[0] = mfma16(kf[0][s], qf, sc[0]);
      sc[1] = mfma16(kf[1][s], qf, sc[1]);
    }
    float mx = fmaxf(fmaxf(fmaxf(sc[0][0], sc[0][1]), fmaxf(sc[0][2], sc[0][3])), fmaxf(fmaxf(sc[1][0], sc[1][1]), fmaxf(sc[1][2], sc[1][3])));
    mx = x16_max(mx); mx = x32_max(mx);
    if (lane < 4) *(LAS float*)(L + L_ST + (w * 4 + lane) * 4) = mx;
    BARRIER();
    const f32x4 m4 = {*(const LAS float*)(L + L_ST + (0 * 4 + (c & 3)) * 4), *(const LAS float*)(L + L_ST + (1 * 4 + (c & 3)) * 4),
                      *(const LAS float*)(L + L_ST + (2 * 4 + (c & 3)) * 4), *(const LAS float*)(L + L_ST + (3 * 4 + (c & 3)) * 4)};
    const float M = fmaxf(fmaxf(m4[0], m4[1]), fmaxf(m4[2], m4[3]));
    constexpr float C2 = 0.08838834764831845f * 1.4426950408889634f;
    const float mc = M * C2;
    float lsum = 0.f;
#pragma unroll
    for (int T2 = 0; T2 < 2; ++T2) {
      float e[4];
#pragma unroll
      for (int r = 0; r < 4; ++r) e[r] = __builtin_amdgcn_exp2f(fmaf(sc[T2][r], C2, -mc));
      v2u pk; pk[0] = pkbf(e[0], e[1]); pk[1] = pkbf(e[2], e[3]);
      lsum += (bflo(pk[0]) + bfhi(pk[0])) + (bflo(pk[1]) + bfhi(pk[1]));
      if (c < 4) *(LAS v2u*)(L + L_P + ((w * 4 + 2 * T2 + (g >> 1)) * 4 + c) * 16 + (g & 1) * 8) = pk;
    }
    lsum = x16_sum(lsum); lsum = x32_sum(lsum);
    if (lane < 4) *(LAS float*)(L + L_ST + 64 + (w * 4 + lane) * 4) = lsum;
    WAIT_VM(0);
    STAMP(4);
    BARRIER();
    STAMP(5);
    f32x4 oa[2] = {{0.f, 0.f, 0.f, 0.f}, {0.f, 0.f, 0.f, 0.f}};
    {
      const int q4 = c >> 2, pp = c & 3;
#pragma unroll
      for (int s = 0; s < 4; ++s) {
        const v4u pf = *(const LAS v4u*)(L + L_P + ((s * 4 + g) * 4 + (c & 3)) * 16);
#pragma unroll
        for (int tt = 0; tt < 2; ++tt) {
          const int t = 2 * w + tt;
          s16x4 a[2];
#pragma unroll
          for (int t2 = 0; t2 < 2; ++t2) {
            const int row = 32 * s + 8 * g + 4 * t2 + q4;
            const int mask = (q4 << 2) | ((2 * g + t2) & 3);
            a[t2] = __builtin_amdgcn_ds_read_tr16_b64_v4i16((LAS s16x4*)(L + L_V + row * 256 + (((2 * t + (pp >> 1)) ^ mask) * 16) + (pp & 1) * 8));
          }
          const s16x8 af = __builtin_shufflevector(a[0], a[1], 0, 1, 2, 3, 4, 5, 6, 7);
          oa[tt] = mfma16(__builtin_bit_cast(v4u, af), pf, oa[tt]);
        }
      }
    }
    {
      const int head = c & 3, k = c >> 2, tt = k >> 1, pair = k & 1;
      const f32x4 o = tt ? oa[1] : oa[0];
      v4u gr; gr[0] = __float_as_uint(pair ? o[2] : o[0]); gr[1] = tlo; gr[2] = __float_as_uint(pair ? o[3] : o[1]); gr[3] = thi;
      const int off = W_O + ((((h * 32 + j) * 32 + head * 8 + 2 * w + tt) * 8) + 2 * g + pair) * 16;
      __builtin_amdgcn_raw_buffer_store_b128(gr, rs, off, 0, 16);
      if (w == 0 && lane < 4) {
        const float l4 = (*(const LAS float*)(L + L_ST + 64 + (0 * 4 + lane) * 4) + *(const LAS float*)(L + L_ST + 64 + (1 * 4 + lane) * 4)) +
                         (*(const LAS float*)(L + L_ST + 64 + (2 * 4 + lane) * 4) + *(const LAS float*)(L + L_ST + 64 + (3 * 4 + lane) * 4));
        v4u ml; ml[0] = __float_as_uint(mc); ml[1] = tlo; ml[2] = __float_as_uint(l4); ml[3] = thi;
        __builtin_amdgcn_raw_buffer_store_b128(ml, rs, W_ML + ((h * 32 + j) * 4 + lane) * 16, 0, 16);
      }
    }
    STAMP(6);
  } else if (wave == 4) {
    __builtin_amdgcn_s_setprio(2);
    const v4u xr = *(const GAS v4u*)(p.x + lane * 8);
    const v2u ev = __builtin_amdgcn_raw_buffer_load_b64(rs, W_E, 0, 16);
    const unsigned long long E = (unsigned long long)(unsigned)__builtin_amdgcn_readfirstlane(ev[0]) | ((unsigned long long)(unsigned)__builtin_amdgcn_readfirstlane(ev[1]) << 32);
    const unsigned long long T = E * 6364136223846793005ull + 1442695040888963407ull;
    const unsigned tlo = (unsigned)T, thi = (unsigned)(T >> 32);
    if (lane == 0) *(volatile LAS unsigned*)(L + L_FLAG) = 0u;
    const v4u xn = ln_packed(xr);
    *(LAS v4u*)(L + L_XN + lane * 16) = xn;
    WAIT_LGKM();
    v4u xf[16];
#pragma unroll
    for (int s = 0; s < 16; ++s) xf[s] = *(const LAS v4u*)(L + L_XN + (4 * s + g) * 16);
    STAMP(1);
    BARRIER();
    STAMP(2);
    f32x4 a0 = {0.f, 0.f, 0.f, 0.f}, a1 = {0.f, 0.f, 0.f, 0.f};
    {
      const int q4 = c >> 2, pp = c & 3;
      LAS unsigned char* wb = L + L_WQ + (8 * g + q4) * 32 + pp * 8;
#pragma unroll
      for (int s = 0; s < 16; ++s) {
        const s16x4 lo = __builtin_amdgcn_ds_read_tr16_b64_v4i16((LAS s16x4*)(wb + s * 1024));
        const s16x4 hi = __builtin_amdgcn_ds_read_tr16_b64_v4i16((LAS s16x4*)(wb + s * 1024 + 128));
        const s16x8 af = __builtin_shufflevector(lo, hi, 0, 1, 2, 3, 4, 5, 6, 7);
        if (s & 1) a1 = mfma16(__builtin_bit_cast(v4u, af), xf[s], a1);
        else a0 = mfma16(__builtin_bit_cast(v4u, af), xf[s], a0);
      }
    }
    {
      v4u gr; gr[0] = pkbf(a0[0] + a1[0], a0[1] + a1[1]); gr[1] = tlo; gr[2] = pkbf(a0[2] + a1[2], a0[3] + a1[3]); gr[3] = thi;
      if (c == 0) __builtin_amdgcn_raw_buffer_store_b128(gr, rs, W_Q + ((h * 32 + j) * 4 + g) * 16, 0, 16);
    }
    STAMP(3);
    {
      for (unsigned spin = 0; spin < SPIN_MAX; ++spin) {
        asm volatile("" ::: "memory");
        const v4u q0 = __builtin_amdgcn_raw_buffer_load_b128(rs, W_Q + h * 2048 + lane * 16, 0, 16);
        const v4u q1 = __builtin_amdgcn_raw_buffer_load_b128(rs, W_Q + h * 2048 + 1024 + lane * 16, 0, 16);
        const bool ok = q0[1] == tlo && q0[3] == thi && q1[1] == tlo && q1[3] == thi;
        if (__all(ok)) {
#pragma unroll
          for (int u = 0; u < 2; ++u) {
            const v4u q = u ? q1 : q0;
            const int G = u * 64 + lane, jj = G >> 2, g2 = G & 3;
            const int head = jj >> 3, dim0 = (jj & 7) * 16 + 4 * g2;
            v2u d; d[0] = q[0]; d[1] = q[2];
            *(LAS v2u*)(L + L_Q + (((dim0 >> 5) * 4 + ((dim0 >> 3) & 3)) * 4 + head) * 16 + ((dim0 >> 2) & 1) * 8) = d;
          }
          break;
        }
      }
    }
    STAMP(4);
    BARRIER();
    v4u wr[16];
    {
      const unsigned char* wb = (const unsigned char*)p.wo + (size_t)(h * 512 + j * 16) * 1024 + lane * 16;
#pragma unroll
      for (int r = 0; r < 16; ++r) wr[r] = ldw(wb + r * 1024);
      __builtin_amdgcn_sched_barrier(0);
    }
    BARRIER();
    BARRIER();
    STAMP(5);
    unsigned wp[8][8];
#pragma unroll
    for (int i = 0; i < 8; ++i)
#pragma unroll
      for (int d = 0; d < 4; ++d) {
        wp[i][2 * d] = __builtin_amdgcn_perm(wr[2 * i + 1][d], wr[2 * i][d], 0x05040100u);
        wp[i][2 * d + 1] = __builtin_amdgcn_perm(wr[2 * i + 1][d], wr[2 * i][d], 0x07060302u);
      }
    STAMP(6);
    unsigned op = 0;
    {
      const int jj = lane >> 3, gi = lane & 7;
      for (unsigned spin = 0; spin < SPIN_MAX; ++spin) {
        asm volatile("" ::: "memory");
        v4u o[4], ml[4];
#pragma unroll
        for (int i = 0; i < 4; ++i) {
          o[i] = __builtin_amdgcn_raw_buffer_load_b128(rs, W_O + ((((h * 32 + jj + 8 * i) * 32 + j) * 8) + gi) * 16, 0, 16);
          ml[i] = __builtin_amdgcn_raw_buffer_load_b128(rs, W_ML + ((h * 32 + jj + 8 * i) * 4 + (j >> 3)) * 16, 0, 16);
        }
        bool ok = true;
#pragma unroll
        for (int i = 0; i < 4; ++i) ok = ok && o[i][1] == tlo && o[i][3] == thi && ml[i][1] == tlo && ml[i][3] == thi;
        if (__all(ok)) {
          float M = fmaxf(fmaxf(__uint_as_float(ml[0][0]), __uint_as_float(ml[1][0])), fmaxf(__uint_as_float(ml[2][0]), __uint_as_float(ml[3][0])));
          M = fmaxf(M, dpp<XOR8>(M)); M = x16_max(M); M = x32_max(M);
          float Ls = 0.f, o0 = 0.f, o1 = 0.f;
#pragma unroll
          for (int i = 0; i < 4; ++i) {
            const float wt = __builtin_amdgcn_exp2f(__uint_as_float(ml[i][0]) - M);
            Ls = fmaf(wt, __uint_as_float(ml[i][2]), Ls); o0 = fmaf(wt, __uint_as_float(o[i][0]), o0); o1 = fmaf(wt, __uint_as_float(o[i][2]), o1);
          }
          Ls += dpp<XOR8>(Ls); o0 += dpp<XOR8>(o0); o1 += dpp<XOR8>(o1);
          Ls = x16_sum(Ls); o0 = x16_sum(o0); o1 = x16_sum(o1);
          Ls = x32_sum(Ls); o0 = x32_sum(o0); o1 = x32_sum(o1);
          const float inv = 1.0f / Ls;
          op = pkbf(o0 * inv, o1 * inv);
          break;
        }
      }
    }
    STAMP(7);
    {
      float acc[8];
#pragma unroll
      for (int e = 0; e < 8; ++e) acc[e] = 0.f;
#pragma unroll
      for (int i = 0; i < 8; ++i) {
        const unsigned oi = __builtin_amdgcn_readlane(op, i);
#pragma unroll
        for (int e = 0; e < 8; ++e) acc[e] = dot2(wp[i][e], oi, acc[e]);
      }
#pragma unroll
      for (int i = 0; i < 4; ++i) {
        v4u gr; gr[0] = __float_as_uint(acc[2 * i]); gr[1] = tlo; gr[2] = __float_as_uint(acc[2 * i + 1]); gr[3] = thi;
        __builtin_amdgcn_raw_buffer_store_b128(gr, rs, W_S + (b * 256 + i * 64 + lane) * 16, 0, 16);
      }
    }
    STAMP(8);
    {
      const int cc = (b & 7) * 32 + (b >> 3);
      const int col = 8 * (cc & 63) + 2 * (cc >> 6);
      const unsigned xw = *(const GAS unsigned*)(p.x + col);
      const unsigned b2w = *(const GAS unsigned*)(p.b2 + col);
      for (unsigned spin = 0; spin < SPIN_MAX; ++spin) {
        asm volatile("" ::: "memory");
        v4u s[4], m[4];
#pragma unroll
        for (int i = 0; i < 4; ++i) {
          s[i] = __builtin_amdgcn_raw_buffer_load_b128(rs, W_S + ((lane + 64 * i) * 256 + cc) * 16, 0, 16);
          const int mp = (i == 3 && lane >= 32) ? lane - 32 : lane + 64 * i;
          m[i] = __builtin_amdgcn_raw_buffer_load_b128(rs, W_M + (mp * 256 + cc) * 16, 0, 16);
        }
        bool ok = true;
#pragma unroll
        for (int i = 0; i < 4; ++i) ok = ok && s[i][1] == tlo && s[i][3] == thi && m[i][1] == tlo && m[i][3] == thi;
        if (__all(ok) || spin == SPIN_MAX - 1) {
          float s0 = (__uint_as_float(s[0][0]) + __uint_as_float(s[1][0])) + (__uint_as_float(s[2][0]) + __uint_as_float(s[3][0]));
          float s1 = (__uint_as_float(s[0][2]) + __uint_as_float(s[1][2])) + (__uint_as_float(s[2][2]) + __uint_as_float(s[3][2]));
          const float z3 = lane < 32 ? 1.f : 0.f;
          float m0 = (__uint_as_float(m[0][0]) + __uint_as_float(m[1][0])) + (__uint_as_float(m[2][0]) + (lane < 32 ? __uint_as_float(m[3][0]) : 0.f));
          float m1 = (__uint_as_float(m[0][2]) + __uint_as_float(m[1][2])) + (__uint_as_float(m[2][2]) + (lane < 32 ? __uint_as_float(m[3][2]) : 0.f));
          (void)z3;
          s0 = wave_sum(s0); s1 = wave_sum(s1); m0 = wave_sum(m0); m1 = wave_sum(m1);
          const float z0 = rbf(rbf(m0) + bflo(b2w)), z1 = rbf(rbf(m1) + bfhi(b2w));
          const float g0 = rbf(z0 * __builtin_amdgcn_rcpf(1.f + __builtin_amdgcn_exp2f(-1.4426950408889634f * z0)));
          const float g1 = rbf(z1 * __builtin_amdgcn_rcpf(1.f + __builtin_amdgcn_exp2f(-1.4426950408889634f * z1)));
          const float r0 = bflo(xw) + rbf(rbf(s0) * g0), r1 = bfhi(xw) + rbf(rbf(s1) * g1);
          if (lane == 0) *(GAS unsigned*)(p.out + col) = pkbf(r0, r1);
          break;
        }
      }
      if (b == 0 && lane == 0) __hip_atomic_store((GAS unsigned long long*)(p.ws + W_E), T, __ATOMIC_RELAXED, __HIP_MEMORY_SCOPE_AGENT);
    }
    STAMP(9);
  } else {
    const bool act = j < 28;
    const int cg = h * 28 + j;
    v4u xr = {0u, 0u, 0u, 0u};
    unsigned tlo = 0, thi = 0;
    v4u y = {0u, 0u, 0u, 0u};
    if (act) {
      xr = *(const GAS v4u*)(p.x + lane * 8);
      const v2u ev = __builtin_amdgcn_raw_buffer_load_b64(rs, W_E, 0, 16);
      const unsigned long long E = (unsigned long long)(unsigned)__builtin_amdgcn_readfirstlane(ev[0]) | ((unsigned long long)(unsigned)__builtin_amdgcn_readfirstlane(ev[1]) << 32);
      const unsigned long long T = E * 6364136223846793005ull + 1442695040888963407ull;
      tlo = (unsigned)T; thi = (unsigned)(T >> 32);
      y = ln_packed(ln_packed(xr));
    }
    BARRIER();
    BARRIER();
    v4u a[8], w2r[8]; v4u b1v = {0u, 0u, 0u, 0u};
    if (act) {
      const unsigned char* w1b = (const unsigned char*)p.w1 + (size_t)(lane * 8) * (FF * 2) + cg * 16;
#pragma unroll
      for (int i = 0; i < 8; ++i) a[i] = ldw(w1b + i * (FF * 2));
      const unsigned char* w2b = (const unsigned char*)p.w2 + (size_t)(cg * 8) * 1024 + lane * 16;
#pragma unroll
      for (int i = 0; i < 8; ++i) w2r[i] = ldw(w2b + i * 1024);
      b1v = *(const GAS v4u*)(p.b1 + cg * 8);
      __builtin_amdgcn_sched_barrier(0);
    }
    BARRIER();
    BARRIER();
    STAMP(5);
    if (act) {
      float acc[8];
#pragma unroll
      for (int e = 0; e < 8; ++e) acc[e] = 0.f;
#pragma unroll
      for (int i = 0; i < 4; ++i)
#pragma unroll
        for (int d = 0; d < 4; ++d) {
          acc[2 * d] = dot2(__builtin_amdgcn_perm(a[2 * i + 1][d], a[2 * i][d], 0x05040100u), y[i], acc[2 * d]);
          acc[2 * d + 1] = dot2(__builtin_amdgcn_perm(a[2 * i + 1][d], a[2 * i][d], 0x07060302u), y[i], acc[2 * d + 1]);
        }
#pragma unroll
      for (int e = 0; e < 8; ++e) acc[e] = wave_sum(acc[e]);
      const int e = lane & 7;
      float av = acc[0];
#pragma unroll
      for (int i = 1; i < 8; ++i) av = (e == i) ? acc[i] : av;
      const unsigned bw = (e >> 1) == 0 ? b1v[0] : (e >> 1) == 1 ? b1v[1] : (e >> 1) == 2 ? b1v[2] : b1v[3];
      const float z = rbf(rbf(av) + ((e & 1) ? bfhi(bw) : bflo(bw)));
      const float u = 0.7978845608028654f * (z + 0.044715f * z * z * z);
      const float th = 1.f - 2.f * __builtin_amdgcn_rcpf(1.f + __builtin_amdgcn_exp2f(2.885390081777927f * u));
      const float hv = 0.5f * z * (1.f + th);
      const float hn = dpp<XOR1>(hv);
      const unsigned hp = pkbf(hv, hn);
      float acc2[8];
#pragma unroll
      for (int i = 0; i < 8; ++i) acc2[i] = 0.f;
#pragma unroll
      for (int i = 0; i < 4; ++i) {
        const unsigned hi2 = __builtin_amdgcn_readlane(hp, 2 * i);
#pragma unroll
        for (int d = 0; d < 4; ++d) {
          acc2[2 * d] = dot2(__builtin_amdgcn_perm(w2r[2 * i + 1][d], w2r[2 * i][d], 0x05040100u), hi2, acc2[2 * d]);
          acc2[2 * d + 1] = dot2(__builtin_amdgcn_perm(w2r[2 * i + 1][d], w2r[2 * i][d], 0x07060302u), hi2, acc2[2 * d + 1]);
        }
      }
#pragma unroll
      for (int i = 0; i < 4; ++i) {
        v4u gr; gr[0] = __float_as_uint(acc2[2 * i]); gr[1] = tlo; gr[2] = __float_as_uint(acc2[2 * i + 1]); gr[3] = thi;
        __builtin_amdgcn_raw_buffer_store_b128(gr, rs, W_M + (cg * 256 + i * 64 + lane) * 16, 0, 16);
      }
    }
    STAMP(6);
  }
}

extern "C" void kernel_launch(void* const* d_in, const int* in_sizes, int n_in,
                              void* d_out, int out_size, void* d_ws, size_t ws_size,
                              hipStream_t stream) {
  Params p;
  p.x = (const u16*)d_in[0]; p.kc = (const u16*)d_in[1]; p.vc = (const u16*)d_in[2]; p.wq = (const u16*)d_in[3]; p.wo = (const u16*)d_in[4];
  p.w1 = (const u16*)d_in[6]; p.b1 = (const u16*)d_in[7]; p.w2 = (const u16*)d_in[8]; p.b2 = (const u16*)d_in[9];
  p.out = (u16*)d_out; p.ws = (unsigned char*)d_ws; p.bx = (const u16*)d_in[5];
  fused_kernel<<<NB, NTHR, 0, stream>>>(p);
}
```

```cpp
#include <hip/hip_runtime.h>
#include <hip/hip_bf16.h>

#define LAS __attribute__((address_space(3)))
#define GAS __attribute__((address_space(1)))
typedef unsigned short u16;
typedef unsigned v4u __attribute__((ext_vector_type(4)));
typedef unsigned v2u __attribute__((ext_vector_type(2)));
typedef float f32x4 __attribute__((ext_vector_type(4)));
typedef short s16x4 __attribute__((ext_vector_type(4)));
typedef short s16x8 __attribute__((ext_vector_type(8)));
typedef __bf16 bf16x8 __attribute__((ext_vector_type(8)));
typedef __bf16 bf16x2 __attribute__((ext_vector_type(2)));

#define KN_NT 1
#define KN_VMODE 1
#define KN_STAMPS 1

constexpr int D = 512, SK = 65536, DH = 128, WIN = 4096, FF = 1792;
constexpr int NB = 256, NTHR = 384;
constexpr int L_WQ = 0, L_K = 16384, L_V = 49152, L_XN = 81920, L_Q = 82944, L_P = 83968, L_ST = 84992, L_FLAG = 85248, L_END = 85504;
constexpr int W_E = 0;
constexpr int W_Q = 4096;
constexpr int W_ML = W_Q + 16384;
constexpr int W_O = W_ML + 16384;
constexpr int W_S = W_O + 1048576;
constexpr int W_M = W_S + 1048576;
constexpr int W_END = W_M + 224 * 4096;
constexpr size_t W_STAMP = (size_t)64 << 20;
constexpr unsigned SPIN_MAX = 1u << 17;

__device__ __forceinline__ float bf2f(unsigned b) { return __uint_as_float(b << 16); }
__device__ __forceinline__ float bflo(unsigned w) { return __uint_as_float(w << 16); }
__device__ __forceinline__ float bfhi(unsigned w) { return __uint_as_float(w & 0xffff0000u); }
__device__ __forceinline__ unsigned pkbf(float lo, float hi) {
  bf16x2 r; r[0] = (__bf16)lo; r[1] = (__bf16)hi; return __builtin_bit_cast(unsigned, r);
}
__device__ __forceinline__ float rbf(float f) { return bflo(pkbf(f, f)); }
__device__ __forceinline__ float dot2(unsigned a, unsigned b, float c) {
  return __builtin_amdgcn_fdot2_f32_bf16(__builtin_bit_cast(bf16x2, a), __builtin_bit_cast(bf16x2, b), c, false);
}
template <int CTRL> __device__ __forceinline__ float dpp(float x) {
  return __builtin_bit_cast(float, __builtin_amdgcn_mov_dpp(__builtin_bit_cast(int, x), CTRL, 0xf, 0xf, true));
}
constexpr int XOR1 = 0xB1, XOR2 = 0x4E, XOR7 = 0x141, XOR15 = 0x140, XOR8 = 0x128;
__device__ __forceinline__ float x16_sum(float x) {
  auto s = __builtin_amdgcn_permlane16_swap(__float_as_uint(x), __float_as_uint(x), false, false);
  return __uint_as_float(s[0]) + __uint_as_float(s[1]);
}
__device__ __forceinline__ float x32_sum(float x) {
  auto s = __builtin_amdgcn_permlane32_swap(__float_as_uint(x), __float_as_uint(x), false, false);
  return __uint_as_float(s[0]) + __uint_as_float(s[1]);
}
__device__ __forceinline__ float x16_max(float x) {
  auto s = __builtin_amdgcn_permlane16_swap(__float_as_uint(x), __float_as_uint(x), false, false);
  return fmaxf(__uint_as_float(s[0]), __uint_as_float(s[1]));
}
__device__ __forceinline__ float x32_max(float x) {
  auto s = __builtin_amdgcn_permlane32_swap(__float_as_uint(x), __float_as_uint(x), false, false);
  return fmaxf(__uint_as_float(s[0]), __uint_as_float(s[1]));
}
__device__ __forceinline__ float wave_sum(float v) {
  v += dpp<XOR1>(v); v += dpp<XOR2>(v); v += dpp<XOR7>(v); v += dpp<XOR15>(v);
  v = x16_sum(v); v = x32_sum(v); return v;
}
__device__ __forceinline__ f32x4 mfma16(v4u a, v4u b, f32x4 c) {
  return __builtin_amdgcn_mfma_f32_16x16x32_bf16(__builtin_bit_cast(bf16x8, a), __builtin_bit_cast(bf16x8, b), c, 0, 0, 0);
}
__device__ __forceinline__ v4u ln_packed(v4u x) {
  float v[8];
#pragma unroll
  for (int i = 0; i < 4; ++i) { v[2 * i] = bflo(x[i]); v[2 * i + 1] = bfhi(x[i]); }
  float s = ((v[0] + v[1]) + (v[2] + v[3])) + ((v[4] + v[5]) + (v[6] + v[7]));
  const float mean = wave_sum(s) * (1.0f / D);
  float q = 0.f;
#pragma unroll
  for (int i = 0; i < 8; ++i) { const float d = v[i] - mean; q = fmaf(d, d, q); }
  const float var = rbf(wave_sum(q) * (1.0f / D));
  const float m = rbf(mean);
  const float sd = rbf(__builtin_sqrtf(rbf(var + 1e-5f)));
  const float inv = 1.0f / sd;
  v4u r;
#pragma unroll
  for (int i = 0; i < 4; ++i) r[i] = pkbf(rbf(v[2 * i] - m) * inv, rbf(v[2 * i + 1] - m) * inv);
  return r;
}
#define WAIT_VM(n) asm volatile("s_waitcnt vmcnt(" #n ")" ::: "memory")
#define WAIT_LGKM() asm volatile("s_waitcnt lgkmcnt(0)" ::: "memory")
#define BARRIER() do { asm volatile("s_waitcnt lgkmcnt(0)\n\ts_barrier" ::: "memory"); } while (0)
#define NTS " nt"
__device__ __forceinline__ void glds4(const void* sbase, unsigned v0, unsigned v1, unsigned v2, unsigned v3, unsigned ldsaddr) {
  unsigned keep;
  asm volatile("s_mov_b32 %0, m0\n\ts_mov_b32 m0, %6\n\ts_nop 0\n\t"
               "global_load_lds_dwordx4 %1, %5" NTS "\n\t"
               "global_load_lds_dwordx4 %2, %5 offset:1024" NTS "\n\t"
               "global_load_lds_dwordx4 %3, %5 offset:2048" NTS "\n\t"
               "global_load_lds_dwordx4 %4, %5 offset:3072" NTS "\n\t"
               "s_mov_b32 m0, %0"
               : "=&s"(keep) : "v"(v0), "v"(v1), "v"(v2), "v"(v3), "s"(sbase), "s"(ldsaddr) : "memory");
}
__device__ __forceinline__ v4u ldw(const void* p) {
  return __builtin_nontemporal_load((const GAS v4u*)p);
}
#define STAMP(i) do { st[i] = __builtin_amdgcn_s_memrealtime(); } while (0)

struct Params {
  const u16* x; const u16* kc; const u16* vc; const u16* wq; const u16* wo;
  const u16* w1; const u16* b1; const u16* w2; const u16* b2; u16* out; unsigned char* ws;
};

__global__ void __launch_bounds__(NTHR, 2) fused_kernel(Params p) {
  __shared__ __attribute__((aligned(16))) unsigned char lds[L_END];
  LAS unsigned char* L = (LAS unsigned char*)lds;
  const unsigned lbase = (unsigned)(size_t)L;
  const int tid = threadIdx.x, lane = tid & 63, wave = __builtin_amdgcn_readfirstlane(tid >> 6);
  const int b = blockIdx.x, h = b & 7, j = b >> 3;
  const int g = lane >> 4, c = lane & 15;
  unsigned long long st[12];
#pragma unroll
  for (int i = 0; i < 12; ++i) st[i] = 0;
  STAMP(0);
  const unsigned long long ck0 = __builtin_amdgcn_s_memtime();
  const __amdgpu_buffer_rsrc_t rs = __builtin_amdgcn_make_buffer_rsrc((void*)p.ws, 0, W_END, 0x00020000);
  if (wave < 4) {
    const int w = wave;
    v2u ev;
    asm volatile("global_load_dwordx2 %0, %1, %2 sc1" : "=v"(ev) : "v"(0u), "s"(p.ws) : "memory");
    {
      const unsigned char* sb = (const unsigned char*)p.wq + (size_t)(h * 512 + j * 16) * 2 + (size_t)w * 128 * 8192;
      const unsigned v0 = (unsigned)(lane >> 1) * 8192u + (unsigned)(lane & 1) * 16u;
      glds4(sb, v0, v0 + 32u * 8192u - 1024u, v0 + 64u * 8192u - 2048u, v0 + 96u * 8192u - 3072u, lbase + L_WQ + w * 4096);
    }
    const size_t kvoff = ((size_t)h * SK + (SK - WIN) + (size_t)j * 128 + (size_t)w * 32) * 256;
    {
      const unsigned char* sb = (const unsigned char*)p.kc + kvoff;
      const unsigned v0 = (unsigned)g * 256u + (unsigned)((c ^ g) * 16);
      glds4(sb, v0, v0 ^ 64u, v0 ^ 128u, v0 ^ 192u, lbase + L_K + w * 8192);
      glds4(sb + 4096, v0, v0 ^ 64u, v0 ^ 128u, v0 ^ 192u, lbase + L_K + w * 8192 + 4096);
    }
    const unsigned char* vsb = (const unsigned char*)p.vc + kvoff;
    const unsigned vv0 = (unsigned)g * 256u + (unsigned)((c ^ (g << 2)) * 16);
#define ISSUE_V() do { glds4(vsb, vv0, vv0 ^ 16u, vv0 ^ 32u, vv0 ^ 48u, lbase + L_V + w * 8192); \
                       glds4(vsb + 4096, vv0, vv0 ^ 16u, vv0 ^ 32u, vv0 ^ 48u, lbase + L_V + w * 8192 + 4096); } while (0)
    asm volatile("s_waitcnt vmcnt(8)" : "+v"(ev) :: "memory");
    const unsigned long long E = (unsigned long long)(unsigned)__builtin_amdgcn_readfirstlane(ev[0]) | ((unsigned long long)(unsigned)__builtin_amdgcn_readfirstlane(ev[1]) << 32);
    const unsigned long long T = E * 6364136223846793005ull + 1442695040888963407ull;
    const unsigned tlo = (unsigned)T, thi = (unsigned)(T >> 32);
    STAMP(1);
    BARRIER();
    ISSUE_V();
    WAIT_VM(8);
    STAMP(2);
    v4u kf[2][4];
#pragma unroll
    for (int T2 = 0; T2 < 2; ++T2)
#pragma unroll
      for (int s = 0; s < 4; ++s)
        kf[T2][s] = *(const LAS v4u*)(L + L_K + (w * 32 + T2 * 16 + c) * 256 + (((4 * s + g) ^ c) * 16));
    BARRIER();
    STAMP(3);
    f32x4 sc[2] = {{0.f, 0.f, 0.f, 0.f}, {0.f, 0.f, 0.f, 0.f}};
#pragma unroll
    for (int s = 0; s < 4; ++s) {
      const v4u qf = *(const LAS v4u*)(L + L_Q + ((s * 4 + g) * 4 + (c & 3)) * 16);
      sc[0] = mfma16(kf[0][s], qf, sc[0]);
      sc[1] = mfma16(kf[1][s], qf, sc[1]);
    }
    float mx = fmaxf(fmaxf(fmaxf(sc[0][0], sc[0][1]), fmaxf(sc[0][2], sc[0][3])), fmaxf(fmaxf(sc[1][0], sc[1][1]), fmaxf(sc[1][2], sc[1][3])));
    mx = x16_max(mx); mx = x32_max(mx);
    if (lane < 4) *(LAS float*)(L + L_ST + (w * 4 + lane) * 4) = mx;
    BARRIER();
    const f32x4 m4 = {*(const LAS float*)(L + L_ST + (0 * 4 + (c & 3)) * 4), *(const LAS float*)(L + L_ST + (1 * 4 + (c & 3)) * 4),
                      *(const LAS float*)(L + L_ST + (2 * 4 + (c & 3)) * 4), *(const LAS float*)(L + L_ST + (3 * 4 + (c & 3)) * 4)};
    const float M = fmaxf(fmaxf(m4[0], m4[1]), fmaxf(m4[2], m4[3]));
    constexpr float C2 = 0.08838834764831845f * 1.4426950408889634f;
    const float mc = M * C2;
    float lsum = 0.f;
#pragma unroll
    for (int T2 = 0; T2 < 2; ++T2) {
      float e[4];
#pragma unroll
      for (int r = 0; r < 4; ++r) e[r] = __builtin_amdgcn_exp2f(fmaf(sc[T2][r], C2, -mc));
      v2u pk; pk[0] = pkbf(e[0], e[1]); pk[1] = pkbf(e[2], e[3]);
      lsum += (bflo(pk[0]) + bfhi(pk[0])) + (bflo(pk[1]) + bfhi(pk[1]));
      if (c < 4) *(LAS v2u*)(L + L_P + ((w * 4 + 2 * T2 + (g >> 1)) * 4 + c) * 16 + (g & 1) * 8) = pk;
    }
    lsum = x16_sum(lsum); lsum = x32_sum(lsum);
    if (lane < 4) *(LAS float*)(L + L_ST + 64 + (w * 4 + lane) * 4) = lsum;
    WAIT_VM(0);
    STAMP(4);
    BARRIER();
    STAMP(5);
    f32x4 oa[2] = {{0.f, 0.f, 0.f, 0.f}, {0.f, 0.f, 0.f, 0.f}};
    {
      const int q4 = c >> 2, pp = c & 3;
#pragma unroll
      for (int s = 0; s < 4; ++s) {
        const v4u pf = *(const LAS v4u*)(L + L_P + ((s * 4 + g) * 4 + (c & 3)) * 16);
#pragma unroll
        for (int tt = 0; tt < 2; ++tt) {
          const int t = 2 * w + tt;
          s16x4 a[2];
#pragma unroll
          for (int t2 = 0; t2 < 2; ++t2) {
            const int row = 32 * s + 8 * g + 4 * t2 + q4;
            const int mask = (q4 << 2) | ((2 * g + t2) & 3);
            a[t2] = __builtin_amdgcn_ds_read_tr16_b64_v4i16((LAS s16x4*)(L + L_V + row * 256 + (((2 * t + (pp >> 1)) ^ mask) * 16) + (pp & 1) * 8));
          }
          const s16x8 af = __builtin_shufflevector(a[0], a[1], 0, 1, 2, 3, 4, 5, 6, 7);
          oa[tt] = mfma16(__builtin_bit_cast(v4u, af), pf, oa[tt]);
        }
      }
    }
    {
      const int head = c & 3, k = c >> 2, tt = k >> 1, pair = k & 1;
      const f32x4 o = tt ? oa[1] : oa[0];
      v4u gr; gr[0] = __float_as_uint(pair ? o[2] : o[0]); gr[1] = tlo; gr[2] = __float_as_uint(pair ? o[3] : o[1]); gr[3] = thi;
      const int off = W_O + ((((h * 32 + j) * 32 + head * 8 + 2 * w + tt) * 8) + 2 * g + pair) * 16;
      __builtin_amdgcn_raw_buffer_store_b128(gr, rs, off, 0, 16);
      if (w == 0 && lane < 4) {
        const float l4 = (*(const LAS float*)(L + L_ST + 64 + (0 * 4 + lane) * 4) + *(const LAS float*)(L + L_ST + 64 + (1 * 4 + lane) * 4)) +
                         (*(const LAS float*)(L + L_ST + 64 + (2 * 4 + lane) * 4) + *(const LAS float*)(L + L_ST + 64 + (3 * 4 + lane) * 4));
        v4u ml; ml[0] = __float_as_uint(mc); ml[1] = tlo; ml[2] = __float_as_uint(l4); ml[3] = thi;
        __builtin_amdgcn_raw_buffer_store_b128(ml, rs, W_ML + ((h * 32 + j) * 4 + lane) * 16, 0, 16);
      }
    }
    STAMP(6);
  } else if (wave == 4) {
    __builtin_amdgcn_s_setprio(2);
    const v4u xr = *(const GAS v4u*)(p.x + lane * 8);
    const v2u ev = __builtin_amdgcn_raw_buffer_load_b64(rs, W_E, 0, 16);
    const unsigned long long E = (unsigned long long)(unsigned)__builtin_amdgcn_readfirstlane(ev[0]) | ((unsigned long long)(unsigned)__builtin_amdgcn_readfirstlane(ev[1]) << 32);
    const unsigned long long T = E * 6364136223846793005ull + 1442695040888963407ull;
    const unsigned tlo = (unsigned)T, thi = (unsigned)(T >> 32);
    if (lane == 0) *(volatile LAS unsigned*)(L + L_FLAG) = 0u;
    const v4u xn = ln_packed(xr);
    *(LAS v4u*)(L + L_XN + lane * 16) = xn;
    WAIT_LGKM();
    v4u xf[16];
#pragma unroll
    for (int s = 0; s < 16; ++s) xf[s] = *(const LAS v4u*)(L + L_XN + (4 * s + g) * 16);
    STAMP(1);
    BARRIER();
    STAMP(2);
    f32x4 a0 = {0.f, 0.f, 0.f, 0.f}, a1 = {0.f, 0.f, 0.f, 0.f};
    {
      const int q4 = c >> 2, pp = c & 3;
      LAS unsigned char* wb = L + L_WQ + (8 * g + q4) * 32 + pp * 8;
#pragma unroll
      for (int s = 0; s < 16; ++s) {
        const s16x4 lo = __builtin_amdgcn_ds_read_tr16_b64_v4i16((LAS s16x4*)(wb + s * 1024));
        const s16x4 hi = __builtin_amdgcn_ds_read_tr16_b64_v4i16((LAS s16x4*)(wb + s * 1024 + 128));
        const s16x8 af = __builtin_shufflevector(lo, hi, 0, 1, 2, 3, 4, 5, 6, 7);
        if (s & 1) a1 = mfma16(__builtin_bit_cast(v4u, af), xf[s], a1);
        else a0 = mfma16(__builtin_bit_cast(v4u, af), xf[s], a0);
      }
    }
    {
      v4u gr; gr[0] = pkbf(a0[0] + a1[0], a0[1] + a1[1]); gr[1] = tlo; gr[2] = pkbf(a0[2] + a1[2], a0[3] + a1[3]); gr[3] = thi;
      if (c == 0) __builtin_amdgcn_raw_buffer_store_b128(gr, rs, W_Q + ((h * 32 + j) * 4 + g) * 16, 0, 16);
    }
    STAMP(3);
    {
      for (unsigned spin = 0; spin < SPIN_MAX; ++spin) {
        asm volatile("" ::: "memory");
        const v4u q0 = __builtin_amdgcn_raw_buffer_load_b128(rs, W_Q + h * 2048 + lane * 16, 0, 16);
        const v4u q1 = __builtin_amdgcn_raw_buffer_load_b128(rs, W_Q + h * 2048 + 1024 + lane * 16, 0, 16);
        const bool ok = q0[1] == tlo && q0[3] == thi && q1[1] == tlo && q1[3] == thi;
        if (__all(ok)) {
#pragma unroll
          for (int u = 0; u < 2; ++u) {
            const v4u q = u ? q1 : q0;
            const int G = u * 64 + lane, jj = G >> 2, g2 = G & 3;
            const int head = jj >> 3, dim0 = (jj & 7) * 16 + 4 * g2;
            v2u d; d[0] = q[0]; d[1] = q[2];
            *(LAS v2u*)(L + L_Q + (((dim0 >> 5) * 4 + ((dim0 >> 3) & 3)) * 4 + head) * 16 + ((dim0 >> 2) & 1) * 8) = d;
          }
          break;
        }
      }
    }
    STAMP(4);
    BARRIER();
    v4u wr[16];
    {
      const unsigned char* wb = (const unsigned char*)p.wo + (size_t)(h * 512 + j * 16) * 1024 + lane * 16;
#pragma unroll
      for (int r = 0; r < 16; ++r) wr[r] = ldw(wb + r * 1024);
      __builtin_amdgcn_sched_barrier(0);
    }
    BARRIER();
    BARRIER();
    STAMP(5);
    unsigned wp[8][8];
#pragma unroll
    for (int i = 0; i < 8; ++i)
#pragma unroll
      for (int d = 0; d < 4; ++d) {
        wp[i][2 * d] = __builtin_amdgcn_perm(wr[2 * i + 1][d], wr[2 * i][d], 0x05040100u);
        wp[i][2 * d + 1] = __builtin_amdgcn_perm(wr[2 * i + 1][d], wr[2 * i][d], 0x07060302u);
      }
    STAMP(6);
    unsigned op = 0;
    {
      const int jj = lane >> 3, gi = lane & 7;
      for (unsigned spin = 0; spin < SPIN_MAX; ++spin) {
        asm volatile("" ::: "memory");
        v4u o[4], ml[4];
#pragma unroll
        for (int i = 0; i < 4; ++i) {
          o[i] = __builtin_amdgcn_raw_buffer_load_b128(rs, W_O + ((((h * 32 + jj + 8 * i) * 32 + j) * 8) + gi) * 16, 0, 16);
          ml[i] = __builtin_amdgcn_raw_buffer_load_b128(rs, W_ML + ((h * 32 + jj + 8 * i) * 4 + (j >> 3)) * 16, 0, 16);
        }
        bool ok = true;
#pragma unroll
        for (int i = 0; i < 4; ++i) ok = ok && o[i][1] == tlo && o[i][3] == thi && ml[i][1] == tlo && ml[i][3] == thi;
        if (__all(ok)) {
          float M = fmaxf(fmaxf(__uint_as_float(ml[0][0]), __uint_as_float(ml[1][0])), fmaxf(__uint_as_float(ml[2][0]), __uint_as_float(ml[3][0])));
          M = fmaxf(M, dpp<XOR8>(M)); M = x16_max(M); M = x32_max(M);
          float Ls = 0.f, o0 = 0.f, o1 = 0.f;
#pragma unroll
          for (int i = 0; i < 4; ++i) {
            const float wt = __builtin_amdgcn_exp2f(__uint_as_float(ml[i][0]) - M);
            Ls = fmaf(wt, __uint_as_float(ml[i][2]), Ls); o0 = fmaf(wt, __uint_as_float(o[i][0]), o0); o1 = fmaf(wt, __uint_as_float(o[i][2]), o1);
          }
          Ls += dpp<XOR8>(Ls); o0 += dpp<XOR8>(o0); o1 += dpp<XOR8>(o1);
          Ls = x16_sum(Ls); o0 = x16_sum(o0); o1 = x16_sum(o1);
          Ls = x32_sum(Ls); o0 = x32_sum(o0); o1 = x32_sum(o1);
          const float inv = 1.0f / Ls;
          op = pkbf(o0 * inv, o1 * inv);
          break;
        }
      }
    }
    STAMP(7);
    {
      float acc[8];
#pragma unroll
      for (int e = 0; e < 8; ++e) acc[e] = 0.f;
#pragma unroll
      for (int i = 0; i < 8; ++i) {
        const unsigned oi = __builtin_amdgcn_readlane(op, i);
#pragma unroll
        for (int e = 0; e < 8; ++e) acc[e] = dot2(wp[i][e], oi, acc[e]);
      }
#pragma unroll
      for (int i = 0; i < 4; ++i) {
        v4u gr; gr[0] = __float_as_uint(acc[2 * i]); gr[1] = tlo; gr[2] = __float_as_uint(acc[2 * i + 1]); gr[3] = thi;
        __builtin_amdgcn_raw_buffer_store_b128(gr, rs, W_S + (b * 256 + i * 64 + lane) * 16, 0, 16);
      }
    }
    STAMP(8);
    {
      const int cc = (b & 7) * 32 + (b >> 3);
      const int col = 8 * (cc & 63) + 2 * (cc >> 6);
      const unsigned xw = *(const GAS unsigned*)(p.x + col);
      const unsigned b2w = *(const GAS unsigned*)(p.b2 + col);
      for (unsigned spin = 0; spin < SPIN_MAX; ++spin) {
        asm volatile("" ::: "memory");
        v4u s[4], m[4];
#pragma unroll
        for (int i = 0; i < 4; ++i) {
          s[i] = __builtin_amdgcn_raw_buffer_load_b128(rs, W_S + ((lane + 64 * i) * 256 + cc) * 16, 0, 16);
          const int mp = (i == 3 && lane >= 32) ? lane - 32 : lane + 64 * i;
          m[i] = __builtin_amdgcn_raw_buffer_load_b128(rs, W_M + (mp * 256 + cc) * 16, 0, 16);
        }
        bool ok = true;
#pragma unroll
        for (int i = 0; i < 4; ++i) ok = ok && s[i][1] == tlo && s[i][3] == thi && m[i][1] == tlo && m[i][3] == thi;
        if (__all(ok) || spin == SPIN_MAX - 1) {
          float s0 = (__uint_as_float(s[0][0]) + __uint_as_float(s[1][0])) + (__uint_as_float(s[2][0]) + __uint_as_float(s[3][0]));
          float s1 = (__uint_as_float(s[0][2]) + __uint_as_float(s[1][2])) + (__uint_as_float(s[2][2]) + __uint_as_float(s[3][2]));
          const float z3 = lane < 32 ? 1.f : 0.f;
          float m0 = (__uint_as_float(m[0][0]) + __uint_as_float(m[1][0])) + (__uint_as_float(m[2][0]) + (lane < 32 ? __uint_as_float(m[3][0]) : 0.f));
          float m1 = (__uint_as_float(m[0][2]) + __uint_as_float(m[1][2])) + (__uint_as_float(m[2][2]) + (lane < 32 ? __uint_as_float(m[3][2]) : 0.f));
          (void)z3;
          s0 = wave_sum(s0); s1 = wave_sum(s1); m0 = wave_sum(m0); m1 = wave_sum(m1);
          const float z0 = rbf(rbf(m0) + bflo(b2w)), z1 = rbf(rbf(m1) + bfhi(b2w));
          const float g0 = rbf(z0 * __builtin_amdgcn_rcpf(1.f + __builtin_amdgcn_exp2f(-1.4426950408889634f * z0)));
          const float g1 = rbf(z1 * __builtin_amdgcn_rcpf(1.f + __builtin_amdgcn_exp2f(-1.4426950408889634f * z1)));
          const float r0 = bflo(xw) + rbf(rbf(s0) * g0), r1 = bfhi(xw) + rbf(rbf(s1) * g1);
          if (lane == 0) *(GAS unsigned*)(p.out + col) = pkbf(r0, r1);
          break;
        }
      }
      if (b == 0 && lane == 0) __hip_atomic_store((GAS unsigned long long*)(p.ws + W_E), T, __ATOMIC_RELAXED, __HIP_MEMORY_SCOPE_AGENT);
    }
    STAMP(9);
  } else {
    const bool act = j < 28;
    const int cg = h * 28 + j;
    v4u xr = {0u, 0u, 0u, 0u};
    unsigned tlo = 0, thi = 0;
    v4u y = {0u, 0u, 0u, 0u};
    if (act) {
      xr = *(const GAS v4u*)(p.x + lane * 8);
      const v2u ev = __builtin_amdgcn_raw_buffer_load_b64(rs, W_E, 0, 16);
      const unsigned long long E = (unsigned long long)(unsigned)__builtin_amdgcn_readfirstlane(ev[0]) | ((unsigned long long)(unsigned)__builtin_amdgcn_readfirstlane(ev[1]) << 32);
      const unsigned long long T = E * 6364136223846793005ull + 1442695040888963407ull;
      tlo = (unsigned)T; thi = (unsigned)(T >> 32);
      y = ln_packed(ln_packed(xr));
    }
    BARRIER();
    BARRIER();
    v4u a[8], w2r[8]; v4u b1v = {0u, 0u, 0u, 0u};
    if (act) {
      const unsigned char* w1b = (const unsigned char*)p.w1 + (size_t)(lane * 8) * (FF * 2) + cg * 16;
#pragma unroll
      for (int i = 0; i < 8; ++i) a[i] = ldw(w1b + i * (FF * 2));
      const unsigned char* w2b = (const unsigned char*)p.w2 + (size_t)(cg * 8) * 1024 + lane * 16;
#pragma unroll
      for (int i = 0; i < 8; ++i) w2r[i] = ldw(w2b + i * 1024);
      b1v = *(const GAS v4u*)(p.b1 + cg * 8);
      __builtin_amdgcn_sched_barrier(0);
    }
    BARRIER();
    BARRIER();
    STAMP(5);
    if (act) {
      float acc[8];
#pragma unroll
      for (int e = 0; e < 8; ++e) acc[e] = 0.f;
#pragma unroll
      for (int i = 0; i < 4; ++i)
#pragma unroll
        for (int d = 0; d < 4; ++d) {
          acc[2 * d] = dot2(__builtin_amdgcn_perm(a[2 * i + 1][d], a[2 * i][d], 0x05040100u), y[i], acc[2 * d]);
          acc[2 * d + 1] = dot2(__builtin_amdgcn_perm(a[2 * i + 1][d], a[2 * i][d], 0x07060302u), y[i], acc[2 * d + 1]);
        }
#pragma unroll
      for (int e = 0; e < 8; ++e) acc[e] = wave_sum(acc[e]);
      const int e = lane & 7;
      float av = acc[0];
#pragma unroll
      for (int i = 1; i < 8; ++i) av = (e == i) ? acc[i] : av;
      const unsigned bw = (e >> 1) == 0 ? b1v[0] : (e >> 1) == 1 ? b1v[1] : (e >> 1) == 2 ? b1v[2] : b1v[3];
      const float z = rbf(rbf(av) + ((e & 1) ? bfhi(bw) : bflo(bw)));
      const float u = 0.7978845608028654f * (z + 0.044715f * z * z * z);
      const float th = 1.f - 2.f * __builtin_amdgcn_rcpf(1.f + __builtin_amdgcn_exp2f(2.885390081777927f * u));
      const float hv = 0.5f * z * (1.f + th);
      const float hn = dpp<XOR1>(hv);
      const unsigned hp = pkbf(hv, hn);
      float acc2[8];
#pragma unroll
      for (int i = 0; i < 8; ++i) acc2[i] = 0.f;
#pragma unroll
      for (int i = 0; i < 4; ++i) {
        const unsigned hi2 = __builtin_amdgcn_readlane(hp, 2 * i);
#pragma unroll
        for (int d = 0; d < 4; ++d) {
          acc2[2 * d] = dot2(__builtin_amdgcn_perm(w2r[2 * i + 1][d], w2r[2 * i][d], 0x05040100u), hi2, acc2[2 * d]);
          acc2[2 * d + 1] = dot2(__builtin_amdgcn_perm(w2r[2 * i + 1][d], w2r[2 * i][d], 0x07060302u), hi2, acc2[2 * d + 1]);
        }
      }
#pragma unroll
      for (int i = 0; i < 4; ++i) {
        v4u gr; gr[0] = __float_as_uint(acc2[2 * i]); gr[1] = tlo; gr[2] = __float_as_uint(acc2[2 * i + 1]); gr[3] = thi;
        __builtin_amdgcn_raw_buffer_store_b128(gr, rs, W_M + (cg * 256 + i * 64 + lane) * 16, 0, 16);
      }
    }
    STAMP(6);
  }
  if (lane == 0) {
    GAS unsigned long long* sp = (GAS unsigned long long*)(p.ws + W_STAMP) + ((size_t)b * 8 + wave) * 16;
#pragma unroll
    for (int i = 0; i < 12; ++i) sp[i] = st[i];
    sp[12] = ck0; sp[13] = __builtin_amdgcn_s_memtime();
  }
}

#include <cstdio>
#include <vector>
#include <algorithm>
static void diag_report(const char* tag, std::vector<std::vector<double>>& acc) {
  fprintf(stderr, "---- %s ----\n", tag);
  for (int w = 0; w < 8; ++w) for (int i = 0; i < 12; ++i) { auto& a = acc[w * 16 + i]; if (a.empty()) continue; if (w == 1 || w == 2) continue; int n = a.size() / 3; double r[3];
    for (int k = 0; k < 3; ++k) { std::vector<double> v; for (int u = 0; u < n; ++u) v.push_back(a[u * 3 + k]); std::sort(v.begin(), v.end()); r[k] = v[n / 2]; }
    fprintf(stderr, "w%d s%d: %6.2f %6.2f %6.2f\n", w, i, r[0], r[1], r[2]); }
}
static void diag_collect(void* d_ws, std::vector<std::vector<double>>& acc) {
  std::vector<unsigned long long> hs(256 * 8 * 16);
  (void)hipMemcpy(hs.data(), (char*)d_ws + W_STAMP, hs.size() * 8, hipMemcpyDeviceToHost);
  unsigned long long t0 = ~0ull; for (int b = 0; b < 256; ++b) for (int w = 0; w < 8; ++w) { unsigned long long v = hs[(b * 8 + w) * 16]; if (v && v < t0) t0 = v; }
  for (int w = 0; w < 8; ++w) for (int i = 0; i < 12; ++i) { std::vector<double> v; for (int b = 0; b < 256; ++b) { unsigned long long s = hs[(b * 8 + w) * 16 + i]; if (s) v.push_back((double)(s - t0) * 0.01); }
    if (v.empty()) continue; std::sort(v.begin(), v.end()); auto& a = acc[w * 16 + i]; a.push_back(v[0]); a.push_back(v[v.size() / 2]); a.push_back(v.back()); }
}

extern "C" void kernel_launch(void* const* d_in, const int* in_sizes, int n_in,
                              void* d_out, int out_size, void* d_ws, size_t ws_size,
                              hipStream_t stream) {
  Params p;
  p.x = (const u16*)d_in[0]; p.kc = (const u16*)d_in[1]; p.vc = (const u16*)d_in[2]; p.wq = (const u16*)d_in[3]; p.wo = (const u16*)d_in[4];
  p.w1 = (const u16*)d_in[6]; p.b1 = (const u16*)d_in[7]; p.w2 = (const u16*)d_in[8]; p.b2 = (const u16*)d_in[9];
  p.out = (u16*)d_out; p.ws = (unsigned char*)d_ws;
  hipStreamCaptureStatus cs = hipStreamCaptureStatusNone;
  (void)hipStreamIsCapturing(stream, &cs);
  if (cs == hipStreamCaptureStatusNone) {
    for (int i = 0; i < n_in; ++i) { hipPointerAttribute_t at; hipError_t e = hipPointerGetAttributes(&at, d_in[i]); void* base = nullptr; size_t sz = 0; (void)hipMemGetAddressRange((hipDeviceptr_t*)&base, &sz, d_in[i]);
      fprintf(stderr, "in[%d] %p size %d type %d managed %d flags %u err %d base %p rangesize %zu\n", i, d_in[i], in_sizes[i], (int)at.type, (int)at.isManaged, at.allocationFlags, (int)e, base, sz); }
    { hipPointerAttribute_t at; (void)hipPointerGetAttributes(&at, d_ws); void* base = nullptr; size_t sz = 0; (void)hipMemGetAddressRange((hipDeviceptr_t*)&base, &sz, d_ws);
      fprintf(stderr, "ws %p type %d flags %u base %p rangesize %zu; out %p\n", d_ws, (int)at.type, at.allocationFlags, base, sz, d_out); }
    std::vector<std::vector<double>> acc(8 * 16), accw(8 * 16);
    for (int it = 0; it < 7; ++it) {
      (void)hipMemsetAsync((char*)d_ws + ((size_t)128 << 20), it, (size_t)384 << 20, stream);
      (void)hipMemsetAsync((char*)d_ws + W_STAMP, 0, 256 * 8 * 16 * 8, stream);
      (void)hipStreamSynchronize(stream);
      fused_kernel<<<NB, NTHR, 0, stream>>>(p);
      (void)hipStreamSynchronize(stream);
      diag_collect(d_ws, acc);
    }
    diag_report("cold (in harness process)", acc);
    for (int it = 0; it < 7; ++it) {
      for (int r = 0; r < 10; ++r) fused_kernel<<<NB, NTHR, 0, stream>>>(p);
      (void)hipStreamSynchronize(stream);
      diag_collect(d_ws, accw);
    }
    diag_report("warm (in harness process)", accw);
  }
  fused_kernel<<<NB, NTHR, 0, stream>>>(p);
}
```

```cpp
#include <hip/hip_runtime.h>
#include <hip/hip_bf16.h>

#define LAS __attribute__((address_space(3)))
#define GAS __attribute__((address_space(1)))
typedef unsigned short u16;
typedef unsigned v4u __attribute__((ext_vector_type(4)));
typedef unsigned v2u __attribute__((ext_vector_type(2)));
typedef float f32x4 __attribute__((ext_vector_type(4)));
typedef float f32x2 __attribute__((ext_vector_type(2)));
typedef short s16x4 __attribute__((ext_vector_type(4)));
typedef short s16x8 __attribute__((ext_vector_type(8)));
typedef __bf16 bf16x8 __attribute__((ext_vector_type(8)));
typedef __bf16 bf16x2 __attribute__((ext_vector_type(2)));

#define KN_G1_DELAY 40
#define KN_H2_DELAY 12
#define KN_H3_DELAY 18
#define KN_WIN 24
#define KN_STAMPS 0

constexpr int D = 512, SK = 65536, WIN = 4096, FF = 1792;
constexpr int NB = 256, NTHR = 576;
constexpr int L_WQ = 0, L_K = 16384, L_V = 49152, L_WO = 81920, L_W2 = 98304, L_W1 = 106496, L_XN = 114688, L_Y = 115712, L_Q = 116736, L_P = 117760;
constexpr int L_MX = 118784, L_SM = L_MX + 128, L_TAG = L_SM + 128, L_H = L_TAG + 16, L_OP = L_H + 16, L_FS = L_OP + 512, L_FM = L_FS + 64, L_B1 = L_FM + 64, L_XB = L_B1 + 16, L_FLG = L_XB + 16, L_END = L_FLG + 16;
constexpr int W_E = 0;
constexpr int W_Q = 4096;
constexpr int W_Z = W_Q + 131072;
constexpr int W_O = W_Z + 262144;
constexpr int W_S = W_O + 1048576;
constexpr int W_M = W_S + 1048576;
constexpr int W_TOUCH = W_M + 1048576;
constexpr int W_END = W_TOUCH + 4096;
constexpr size_t W_STAMP = (size_t)64 << 20;
constexpr unsigned SPIN_MAX = 1u << 17;

__device__ __forceinline__ float bflo(unsigned w) { return __uint_as_float(w << 16); }
__device__ __forceinline__ float bfhi(unsigned w) { return __uint_as_float(w & 0xffff0000u); }
__device__ __forceinline__ unsigned pkbf(float lo, float hi) { bf16x2 r; r[0] = (__bf16)lo; r[1] = (__bf16)hi; return __builtin_bit_cast(unsigned, r); }
__device__ __forceinline__ float rbf(float f) { return bflo(pkbf(f, f)); }
template <int CTRL> __device__ __forceinline__ float dpp(float x) {
  return __builtin_bit_cast(float, __builtin_amdgcn_mov_dpp(__builtin_bit_cast(int, x), CTRL, 0xf, 0xf, true));
}
constexpr int XOR1 = 0xB1, XOR2 = 0x4E, XOR7 = 0x141, XOR15 = 0x140, XOR8 = 0x128, ROR4 = 0x124;
__device__ __forceinline__ int fkey(float f) { const int i = __float_as_int(f); return i ^ ((i >> 31) & 0x7fffffff); }
__device__ __forceinline__ float funkey(int k) { return __int_as_float(k ^ ((k >> 31) & 0x7fffffff)); }
template <int CTRL> __device__ __forceinline__ int dppi(int x) { return __builtin_amdgcn_mov_dpp(x, CTRL, 0xf, 0xf, true); }
__device__ __forceinline__ int imax(int a, int b) { return a > b ? a : b; }
__device__ __forceinline__ int x16_imax(int x) { auto s = __builtin_amdgcn_permlane16_swap((unsigned)x, (unsigned)x, false, false); return imax((int)s[0], (int)s[1]); }
__device__ __forceinline__ int x32_imax(int x) { auto s = __builtin_amdgcn_permlane32_swap((unsigned)x, (unsigned)x, false, false); return imax((int)s[0], (int)s[1]); }
__device__ __forceinline__ float x16_sum(float x) { auto s = __builtin_amdgcn_permlane16_swap(__float_as_uint(x), __float_as_uint(x), false, false); return __uint_as_float(s[0]) + __uint_as_float(s[1]); }
__device__ __forceinline__ float x32_sum(float x) { auto s = __builtin_amdgcn_permlane32_swap(__float_as_uint(x), __float_as_uint(x), false, false); return __uint_as_float(s[0]) + __uint_as_float(s[1]); }
__device__ __forceinline__ float wave_sum(float v) {
  v += dpp<XOR1>(v); v += dpp<XOR2>(v); v += dpp<XOR7>(v); v += dpp<XOR15>(v);
  v = x16_sum(v); v = x32_sum(v); return v;
}
__device__ __forceinline__ void wave_sum2(float& a, float& b) {
  a += dpp<XOR1>(a); b += dpp<XOR1>(b); a += dpp<XOR2>(a); b += dpp<XOR2>(b); a += dpp<XOR7>(a); b += dpp<XOR7>(b); a += dpp<XOR15>(a); b += dpp<XOR15>(b);
  a = x16_sum(a); b = x16_sum(b); a = x32_sum(a); b = x32_sum(b);
}
__device__ __forceinline__ f32x4 mfma32(v4u a, v4u b, f32x4 c) {
  return __builtin_amdgcn_mfma_f32_16x16x32_bf16(__builtin_bit_cast(bf16x8, a), __builtin_bit_cast(bf16x8, b), c, 0, 0, 0);
}
__device__ __forceinline__ f32x4 mfma16(s16x4 a, v2u b, f32x4 c) {
  return __builtin_amdgcn_mfma_f32_16x16x16bf16_1k(a, __builtin_bit_cast(s16x4, b), c, 0, 0, 0);
}
__device__ __forceinline__ s16x4 trd(LAS unsigned char* p) { return __builtin_amdgcn_ds_read_tr16_b64_v4i16((LAS s16x4*)p); }
__device__ __forceinline__ v4u cat(s16x4 lo, s16x4 hi) { return __builtin_bit_cast(v4u, __builtin_shufflevector(lo, hi, 0, 1, 2, 3, 4, 5, 6, 7)); }
__device__ __forceinline__ v4u ln_packed(v4u x) {
  float v[8];
#pragma unroll
  for (int i = 0; i < 4; ++i) { v[2 * i] = bflo(x[i]); v[2 * i + 1] = bfhi(x[i]); }
  const float piv = __uint_as_float(__builtin_amdgcn_readfirstlane(x[0]) << 16);
  float s = 0.f, q = 0.f;
#pragma unroll
  for (int i = 0; i < 8; ++i) { const float d = v[i] - piv; s += d; q = fmaf(d, d, q); }
  wave_sum2(s, q);
  const float md = s * (1.0f / D);
  const float var = rbf(fmaxf(fmaf(-md, md, q * (1.0f / D)), 0.f));
  const float m = rbf(piv + md);
  const float sd = rbf(__builtin_amdgcn_sqrtf(rbf(var + 1e-5f)));
  const float inv = __builtin_amdgcn_rcpf(sd);
  v4u r;
#pragma unroll
  for (int i = 0; i < 4; ++i) r[i] = pkbf(rbf(v[2 * i] - m) * inv, rbf(v[2 * i + 1] - m) * inv);
  return r;
}
#define WAIT_VM(n) asm volatile("s_waitcnt vmcnt(" #n ")" ::: "memory")
#define WAIT_VMN(n) asm volatile("s_waitcnt vmcnt(%0)" :: "i"(n) : "memory")
static_assert(KN_WIN % 4 == 0 && KN_WIN >= 12 && KN_WIN <= 64 && (12 + KN_WIN) / 4 >= 6 && (20 + KN_WIN) / 4 <= 21, "loader window");
#define BARRIER() asm volatile("s_waitcnt lgkmcnt(0)\n\ts_barrier" ::: "memory")
#define RAWBAR() asm volatile("s_barrier" ::: "memory")
__device__ __forceinline__ void glds1(const void* sbase, unsigned v0, unsigned ldsaddr) {
  unsigned keep;
  asm volatile("s_mov_b32 %0, m0\n\ts_mov_b32 m0, %3\n\ts_nop 0\n\tglobal_load_lds_dwordx4 %1, %2 nt\n\ts_mov_b32 m0, %0"
               : "=&s"(keep) : "v"(v0), "s"(sbase), "s"(ldsaddr) : "memory");
}
__device__ __forceinline__ void glds2(const void* sbase, unsigned v0, unsigned v1, unsigned ldsaddr) {
  unsigned keep;
  asm volatile("s_mov_b32 %0, m0\n\ts_mov_b32 m0, %4\n\ts_nop 0\n\tglobal_load_lds_dwordx4 %1, %3 nt\n\tglobal_load_lds_dwordx4 %2, %3 offset:1024 nt\n\ts_mov_b32 m0, %0"
               : "=&s"(keep) : "v"(v0), "v"(v1), "s"(sbase), "s"(ldsaddr) : "memory");
}
__device__ __forceinline__ void glds2b(const void* sbase, unsigned v2, unsigned v3, unsigned ldsaddr) {
  unsigned keep;
  asm volatile("s_mov_b32 %0, m0\n\ts_mov_b32 m0, %4\n\ts_nop 0\n\tglobal_load_lds_dwordx4 %1, %3 offset:2048 nt\n\tglobal_load_lds_dwordx4 %2, %3 offset:3072 nt\n\ts_mov_b32 m0, %0"
               : "=&s"(keep) : "v"(v2), "v"(v3), "s"(sbase), "s"(ldsaddr) : "memory");
}
__device__ __forceinline__ void glds4(const void* sbase, unsigned v0, unsigned v1, unsigned v2, unsigned v3, unsigned ldsaddr) {
  unsigned keep;
  asm volatile("s_mov_b32 %0, m0\n\ts_mov_b32 m0, %6\n\ts_nop 0\n\tglobal_load_lds_dwordx4 %1, %5 nt\n\tglobal_load_lds_dwordx4 %2, %5 offset:1024 nt\n\t"
               "global_load_lds_dwordx4 %3, %5 offset:2048 nt\n\tglobal_load_lds_dwordx4 %4, %5 offset:3072 nt\n\ts_mov_b32 m0, %0"
               : "=&s"(keep) : "v"(v0), "v"(v1), "v"(v2), "v"(v3), "s"(sbase), "s"(ldsaddr) : "memory");
}
__device__ __forceinline__ unsigned long long rtc() { return __builtin_amdgcn_s_memrealtime(); }
#define STAMP(i) do { } while (0)
#define RETRY(i) do { } while (0)
#define DELAY(n) do { if ((n) > 0) __builtin_amdgcn_s_sleep((n) > 127 ? 127 : (n)); } while (0)
#define LD16(off) __builtin_amdgcn_raw_buffer_load_b128(rs, (off), 0, 16)
#define ST16(v, off) __builtin_amdgcn_raw_buffer_store_b128((v), rs, (off), 0, 16)
#define TAGBAD(x) (((x)[1] ^ tlo) | ((x)[3] ^ thi))
#define NONEBAD(v) (__builtin_amdgcn_ballot_w64((v) != 0u) == 0ull)
#define PIN(x) asm volatile("" : "+v"(x))
__device__ __forceinline__ void lds_add(LAS unsigned* q, unsigned v) { __hip_atomic_fetch_add(q, v, __ATOMIC_RELAXED, __HIP_MEMORY_SCOPE_WORKGROUP); }
__device__ __forceinline__ unsigned lds_peek(LAS unsigned* q) { return __builtin_amdgcn_readfirstlane(__hip_atomic_load(q, __ATOMIC_RELAXED, __HIP_MEMORY_SCOPE_WORKGROUP)); }

struct Params {
  const u16* x; const u16* kc; const u16* vc; const u16* wq; const u16* wo;
  const u16* w1; const u16* b1; const u16* w2; const u16* b2; u16* out; unsigned char* ws;
};

__global__ void __launch_bounds__(NTHR, 3) fused_kernel(Params p) {
  __shared__ __attribute__((aligned(16))) unsigned char lds[L_END];
  LAS unsigned char* L = (LAS unsigned char*)lds;
  const unsigned lbase = (unsigned)(size_t)L;
  const int tid = threadIdx.x, lane = tid & 63, wave = __builtin_amdgcn_readfirstlane(tid >> 6);
  const int b = blockIdx.x, h = b & 7, j = b >> 3;
  const int g = lane >> 4, c = lane & 15;
  STAMP(0);
  asm volatile("" :: "s"(p.x), "s"(p.kc), "s"(p.vc), "s"(p.wq), "s"(p.wo), "s"(p.w1), "s"(p.b1), "s"(p.w2), "s"(p.b2), "s"(p.out), "s"(p.ws));
  const __amdgpu_buffer_rsrc_t rs = __builtin_amdgcn_make_buffer_rsrc((void*)p.ws, 0, W_END, 0x00020000);
  const bool act = j < 28;
  const int cg = h * 28 + (act ? j : j - 28);
  const int cc_cg = h * 4 + (j >> 3), cc_i = j & 7;
  const int col = 16 * cc_cg + 2 * cc_i;

  LAS unsigned* flg = (LAS unsigned*)(L + L_FLG);
  if (tid == 0) { v4u z = {0u, 0u, 0u, 0u}; *(LAS v4u*)(L + L_FLG) = z; }
  if (wave == 0) {
    const int lc = j >> 2, rq = j & 3, lm = cg >> 3, kq = cg & 7;
    const unsigned kvoff = ((unsigned)h * SK + (SK - WIN) + (unsigned)j * 128u) * 256u;
    const unsigned char* kb = (const unsigned char*)p.kc + kvoff;
    const unsigned char* vb = (const unsigned char*)p.vc + kvoff;
    const unsigned k0 = (unsigned)g * 256u + (unsigned)((c ^ g) * 16), v0 = (unsigned)g * 256u + (unsigned)((c ^ (g << 2)) * 16), l16 = (unsigned)lane * 16u;
    BARRIER();
    STAMP(1);
    {
      const unsigned ph = (unsigned)(lane >> 4) & 1u, c8 = (unsigned)lane & 7u;
      const unsigned ce = (c8 ^ (ph << 1)) * 16u, co = (c8 ^ ((ph | 2u) << 1)) * 16u;
      const unsigned rq8 = (unsigned)(lane >> 3) * 8192u, r18 = (unsigned)(lane >> 3) * (unsigned)(FF * 2);
      const unsigned char* sq = (const unsigned char*)p.wq + (unsigned)((h * 512 + lc * 64) * 2 + rq * 128 * 8192);
      const unsigned char* s1 = (const unsigned char*)p.w1 + (unsigned)(lm * 128 + kq * 64 * (FF * 2));
      constexpr unsigned SQ = 8u * 8192u - 1024u, S1 = 8u * (FF * 2) - 1024u;
#pragma unroll 1
      for (int n = 0; n < 4; ++n) glds4(sq + (unsigned)(n * 32 * 8192), rq8 + ce, rq8 + co + SQ, rq8 + ce + 2u * SQ, rq8 + co + 3u * SQ, lbase + L_WQ + n * 4096);
#pragma unroll 1
      for (int n = 0; n < 2; ++n) glds4(s1 + (unsigned)(n * 32 * (FF * 2)), r18 + ce, r18 + co + S1, r18 + ce + 2u * S1, r18 + co + 3u * S1, lbase + L_W1 + n * 4096);
    }
    STAMP(2);
    constexpr int GFQ = (12 + KN_WIN) / 4, GF1 = (20 + KN_WIN) / 4;
#define GROUP_WAIT(gi) do { WAIT_VMN(KN_WIN - 4); \
      if ((gi) == GFQ) STAMP(6); if ((gi) == GF1) STAMP(7); if ((gi) == 14) STAMP(8); \
      if ((gi) == GFQ && lane == 0) __hip_atomic_store(flg + 0, 1u, __ATOMIC_RELAXED, __HIP_MEMORY_SCOPE_WORKGROUP); \
      if ((gi) == GF1 && lane == 0) __hip_atomic_store(flg + 2, 1u, __ATOMIC_RELAXED, __HIP_MEMORY_SCOPE_WORKGROUP); } while (0)
#pragma unroll 1
    for (int n = 0; n < 8; ++n) { GROUP_WAIT(6 + n); glds4(kb + n * 4096, k0, k0 ^ 64u, k0 ^ 128u, k0 ^ 192u, lbase + L_K + n * 4096); }
#pragma unroll 1
    for (int n = 0; n < 8; ++n) { GROUP_WAIT(14 + n); glds4(vb + n * 4096, v0, v0 ^ 16u, v0 ^ 32u, v0 ^ 48u, lbase + L_V + n * 4096); }
    STAMP(3);
    WAIT_VMN(32);
    RAWBAR();
    RAWBAR();
#pragma unroll 1
    for (int n = 0; n < 2; ++n) glds4((const unsigned char*)p.w2 + (unsigned)(cg * 8192 + n * 4096), l16, l16, l16, l16, lbase + L_W2 + n * 4096);
    WAIT_VMN(8);
    STAMP(4);
    RAWBAR();
#pragma unroll 1
    for (int n = 0; n < 4; ++n) {
      const unsigned r0 = (unsigned)((4 * n) & 7) << 5;
      glds4((const unsigned char*)p.wo + (unsigned)((h * 512 + j * 16 + 4 * n) * 1024), l16 ^ r0, l16 ^ (r0 + 32u), l16 ^ (r0 + 64u), l16 ^ (r0 + 96u), lbase + L_WO + n * 4096);
    }
    if (act) { WAIT_VMN(16); RAWBAR(); }
    WAIT_VM(0);
    STAMP(5);
    RAWBAR();
  } else {
    const int w = wave - 1;
    const int lc = j >> 2, rq = j & 3;
    const int lm = cg >> 3, kq = cg & 7;
    if (w == 0 || w == 4) {
      const v4u xr = *(const GAS v4u*)(p.x + lane * 8);
      v2u ev = {0u, 0u}; unsigned touch = 0u, small = 0u;
      if (w == 0) {
        ev = __builtin_amdgcn_raw_buffer_load_b64(rs, W_E, 0, 16);
        touch = __builtin_amdgcn_raw_buffer_load_b32(rs, W_TOUCH + (lane & 7) * 4, 0, 16);
        const unsigned long long a_b1 = (unsigned long long)(p.b1 + cg * 8), a_x = (unsigned long long)(p.x + col), a_b2 = (unsigned long long)(p.b2 + col);
        const unsigned long long a_sm = lane < 4 ? a_b1 + lane * 4 : (lane == 4 ? a_x : a_b2);
        small = *(const GAS unsigned*)a_sm;
      }
      __builtin_amdgcn_sched_barrier(0);
      RAWBAR();
      __builtin_amdgcn_sched_barrier(0);
      const v4u xn = ln_packed(xr);
      if (w == 0) {
        *(LAS v4u*)(L + L_XN + lane * 16) = xn;
        const unsigned long long E = (unsigned long long)(unsigned)__builtin_amdgcn_readfirstlane(ev[0]) | ((unsigned long long)(unsigned)__builtin_amdgcn_readfirstlane(ev[1]) << 32);
        const unsigned long long T = E * 6364136223846793005ull + 1442695040888963407ull;
        if (lane == 0) { v2u tg; tg[0] = (unsigned)T; tg[1] = (unsigned)(T >> 32); *(LAS v2u*)(L + L_TAG) = tg; }
        if (lane < 6) *(LAS unsigned*)(L + L_B1 + lane * 4) = small;
        asm volatile("s_waitcnt lgkmcnt(0)" ::: "memory");
        if (lane == 0) __hip_atomic_store(flg + 1, 1u, __ATOMIC_RELAXED, __HIP_MEMORY_SCOPE_WORKGROUP);
        asm volatile("" :: "v"(touch));
      } else {
        *(LAS v4u*)(L + L_Y + lane * 16) = ln_packed(xn);
        asm volatile("s_waitcnt lgkmcnt(0)" ::: "memory");
        if (lane == 0) __hip_atomic_store(flg + 3, 1u, __ATOMIC_RELAXED, __HIP_MEMORY_SCOPE_WORKGROUP);
      }
    } else {
      RAWBAR();
    }
    STAMP(1);
    for (unsigned spin = 0; spin < SPIN_MAX; ++spin) {
      const v4u f = *(volatile LAS v4u*)flg;
      asm volatile("" ::: "memory");
      const unsigned need = w < 4 ? (f[0] & f[1]) : (f[1] & f[2] & f[3]);
      if (__builtin_amdgcn_readfirstlane(need) != 0u) break;
      __builtin_amdgcn_s_sleep(1);
    }
    const v2u tg = *(const LAS v2u*)(L + L_TAG);
    const unsigned tlo = tg[0], thi = tg[1];
    {
      const int q4 = c >> 2, pp = c & 3, t = w & 3;
      const int ph = ((q4 >> 1) & 1) | ((g & 1) << 1);
      const int toff = (8 * g + q4) * 128 + (((2 * t + (pp >> 1)) ^ (ph << 1)) * 16) + (pp & 1) * 8;
      f32x4 a0 = {0.f, 0.f, 0.f, 0.f};
      int off;
      if (w < 4) {
#pragma unroll
        for (int s = 0; s < 4; ++s)
          a0 = mfma32(cat(trd(L + L_WQ + toff + s * 4096), trd(L + L_WQ + toff + s * 4096 + 512)), *(const LAS v4u*)(L + L_XN + (128 * rq + 32 * s + 8 * g) * 2), a0);
        off = W_Q + ((((h * 8 + lc) * 4 + rq) * 32) + 8 * t + 2 * g + c) * 16;
      } else {
#pragma unroll
        for (int s = 0; s < 2; ++s)
          a0 = mfma32(cat(trd(L + L_W1 + toff + s * 4096), trd(L + L_W1 + toff + s * 4096 + 512)), *(const LAS v4u*)(L + L_Y + (64 * kq + 32 * s + 8 * g) * 2), a0);
        off = W_Z + ((lm * 8 + 2 * t + (g >> 1)) * 8 + kq) * 128 + (2 * (g & 1) + c) * 16;
      }
      v4u gr; gr[0] = __float_as_uint((c & 1) ? a0[2] : a0[0]); gr[1] = tlo; gr[2] = __float_as_uint((c & 1) ? a0[3] : a0[1]); gr[3] = thi;
      if (c < 2 && (w < 4 || act)) ST16(gr, off);
    }
    STAMP(11);
    const int qoff = W_Q + (h * 8 + w) * 2048 + lane * 16;
    const int zoff = W_Z + ((lm * 8 + kq) * 8 + ((lane & 31) >> 2)) * 128 + (lane & 3) * 16;
    DELAY(KN_G1_DELAY);
    v4u q0, q1, zq;
    asm volatile("global_load_dwordx4 %0, %3, %5 sc1\n\tglobal_load_dwordx4 %1, %3, %5 offset:1024 sc1\n\tglobal_load_dwordx4 %2, %4, %5 sc1"
                 : "=&v"(q0), "=&v"(q1), "=&v"(zq) : "v"(qoff), "v"(w == 7 ? zoff : qoff), "s"(p.ws) : "memory");
    asm volatile("s_waitcnt vmcnt(0)" : "+v"(q0), "+v"(q1), "+v"(zq) :: "memory");
    const bool needz = (w == 7) && act;
    if (!NONEBAD(TAGBAD(q0) | TAGBAD(q1) | (needz ? TAGBAD(zq) : 0u))) {
      for (unsigned spin = 0; spin < SPIN_MAX; ++spin) {
        asm volatile("" ::: "memory");
        RETRY(0);
        q0 = LD16(qoff); q1 = LD16(qoff + 1024); zq = LD16(w == 7 ? zoff : qoff);
        if (NONEBAD(TAGBAD(q0) | TAGBAD(q1) | (needz ? TAGBAD(zq) : 0u))) break;
      }
    }
    {
      const float s0 = x32_sum(__uint_as_float(q0[0]) + __uint_as_float(q1[0])), s1 = x32_sum(__uint_as_float(q0[2]) + __uint_as_float(q1[2]));
      const int dim = (w & 1) * 64 + 2 * (lane & 31);
      if (lane < 32) *(LAS unsigned*)(L + L_Q + (((dim >> 5) * 4 + ((dim >> 3) & 3)) * 4 + (w >> 1)) * 16 + (dim & 7) * 2) = pkbf(s0, s1);
    }
    if (needz) {
      float z0 = __uint_as_float(zq[0]), z1 = __uint_as_float(zq[2]);
      z0 += dpp<ROR4>(z0); z1 += dpp<ROR4>(z1); z0 += dpp<XOR8>(z0); z1 += dpp<XOR8>(z1); z0 = x16_sum(z0); z1 = x16_sum(z1);
      const unsigned bw = *(const LAS unsigned*)(L + L_B1 + (lane & 3) * 4);
      float hv[2];
#pragma unroll
      for (int e = 0; e < 2; ++e) {
        const float z = rbf(rbf(e ? z1 : z0) + (e ? bfhi(bw) : bflo(bw)));
        const float uu = 0.7978845608028654f * (z + 0.044715f * z * z * z);
        const float th = 1.f - 2.f * __builtin_amdgcn_rcpf(1.f + __builtin_amdgcn_exp2f(2.885390081777927f * uu));
        hv[e] = 0.5f * z * (1.f + th);
      }
      if (lane < 4) *(LAS unsigned*)(L + L_H + lane * 4) = pkbf(hv[0], hv[1]);
    }
    STAMP(2);
    BARRIER();
    STAMP(3);
    f32x4 sc = {0.f, 0.f, 0.f, 0.f};
#pragma unroll
    for (int s = 0; s < 4; ++s) sc = mfma32(*(const LAS v4u*)(L + L_K + (w * 16 + c) * 256 + (((4 * s + g) ^ c) * 16)), *(const LAS v4u*)(L + L_Q + ((s * 4 + g) * 4 + (c & 3)) * 16), sc);
    {
      int mx = imax(imax(fkey(sc[0]), fkey(sc[1])), imax(fkey(sc[2]), fkey(sc[3])));
      mx = x16_imax(mx); mx = x32_imax(mx);
      if (lane < 4) *(LAS int*)(L + L_MX + (lane * 8 + w) * 4) = mx;
    }
    BARRIER();
    float mc;
    constexpr float C2 = 0.08838834764831845f * 1.4426950408889634f;
    {
      const v4u ma = *(const LAS v4u*)(L + L_MX + (c & 3) * 32), mb = *(const LAS v4u*)(L + L_MX + (c & 3) * 32 + 16);
      const int M = imax(imax(imax((int)ma[0], (int)ma[1]), imax((int)ma[2], (int)ma[3])), imax(imax((int)mb[0], (int)mb[1]), imax((int)mb[2], (int)mb[3])));
      mc = funkey(M) * C2;
      float e[4];
#pragma unroll
      for (int r = 0; r < 4; ++r) e[r] = __builtin_amdgcn_exp2f(fmaf(sc[r], C2, -mc));
      v2u pk; pk[0] = pkbf(e[0], e[1]); pk[1] = pkbf(e[2], e[3]);
      if (c < 4) *(LAS v2u*)(L + L_P + (((w >> 1) * 4 + 2 * (w & 1) + (g >> 1)) * 4 + c) * 16 + (g & 1) * 8) = pk;
      float lsum = (bflo(pk[0]) + bfhi(pk[0])) + (bflo(pk[1]) + bfhi(pk[1]));
      lsum = x16_sum(lsum); lsum = x32_sum(lsum);
      if (lane < 4) *(LAS float*)(L + L_SM + (lane * 8 + w) * 4) = lsum;
    }
    WAIT_VM(0);
    STAMP(4);
    BARRIER();
    {
      f32x4 oa = {0.f, 0.f, 0.f, 0.f};
      const int q4 = c >> 2, pp = c & 3;
#pragma unroll
      for (int s = 0; s < 4; ++s) {
        const v4u pf = *(const LAS v4u*)(L + L_P + ((s * 4 + g) * 4 + (c & 3)) * 16);
        s16x4 a[2];
#pragma unroll
        for (int t2 = 0; t2 < 2; ++t2) {
          const int row = 32 * s + 8 * g + 4 * t2 + q4;
          const int mask = (q4 << 2) | ((2 * g + t2) & 3);
          a[t2] = trd(L + L_V + row * 256 + (((2 * w + (pp >> 1)) ^ mask) * 16) + (pp & 1) * 8);
        }
        oa = mfma32(cat(a[0], a[1]), pf, oa);
      }
      const int head = c & 3, k = c >> 2;
      const f32x4 la = *(const LAS f32x4*)(L + L_SM + head * 32), lb = *(const LAS f32x4*)(L + L_SM + head * 32 + 16);
      const float l8 = ((la[0] + la[1]) + (la[2] + la[3])) + ((lb[0] + lb[1]) + (lb[2] + lb[3]));
      v4u gr; gr[0] = __float_as_uint((k & 1) ? oa[2] : oa[0]); gr[1] = __float_as_uint((k & 1) ? oa[3] : oa[1]); gr[2] = __float_as_uint((k & 1) ? l8 : mc); gr[3] = (k & 1) ? thi : tlo;
      if (k < 2) ST16(gr, W_O + ((((h * 32 + head * 8 + w) * 32 + j) * 8) + 2 * g + k) * 16);
    }
    STAMP(5);
    const int tsel = (c >> 1) & 3, prs = c & 1;
    unsigned tm[4];
#pragma unroll
    for (int t = 0; t < 4; ++t) tm[t] = tsel == t ? ~0u : 0u;
    f32x4 dm = {0.f, 0.f, 0.f, 0.f};
    if (act) {
      RAWBAR();
      v2u hf = *(const LAS v2u*)(L + L_H + (g & 1) * 8);
      if (g >= 2) { hf[0] = 0u; hf[1] = 0u; }
#pragma unroll
      for (int t = 0; t < 4; ++t) {
        const s16x4 a = trd(L + L_W2 + ((4 * g + (c >> 2)) & 7) * 1024 + (4 * w + t) * 32 + (c & 3) * 8);
        v2u hm; hm[0] = hf[0] & tm[t]; hm[1] = hf[1] & tm[t];
        dm = mfma16(a, hm, dm);
      }
    }
    STAMP(6);
    int soff = W_S + (((4 * w + tsel) * 256 + b) * 8 + 2 * g + prs) * 16, moff = W_M + (((4 * w + tsel) * 256 + cg) * 8 + 2 * g + prs) * 16;
    PIN(soff); PIN(moff);
    if (w < 4) {
      int offa = W_O + (((h * 32 + j) * 32 + 8 * w) * 8 + lane) * 16;
      unsigned tex = (lane & 1) ? thi : tlo;
      PIN(offa); PIN(tex);
      DELAY(KN_H2_DELAY);
      v4u ga;
      for (unsigned spin = 0; spin < SPIN_MAX; ++spin) {
        asm volatile("" ::: "memory");
        RETRY(1);
        ga = LD16(offa);
        if (NONEBAD(ga[3] ^ tex)) break;
      }
      STAMP(7);
      const float aux = __uint_as_float(ga[2]), axn = dpp<XOR1>(aux);
      const float mj = (lane & 1) ? axn : aux, lj = (lane & 1) ? aux : axn;
      int mk = fkey(mj);
      mk = imax(mk, dppi<XOR8>(mk)); mk = x16_imax(mk); mk = x32_imax(mk);
      const float Mw = funkey(mk);
      const float wt = __builtin_amdgcn_exp2f(mj - Mw);
      float o0 = __uint_as_float(ga[0]) * wt, o1 = __uint_as_float(ga[1]) * wt, ll = lj * wt;
      o0 += dpp<XOR8>(o0); o1 += dpp<XOR8>(o1); ll += dpp<XOR8>(ll);
      o0 = x16_sum(o0); o1 = x16_sum(o1); ll = x16_sum(ll);
      o0 = x32_sum(o0); o1 = x32_sum(o1); ll = x32_sum(ll);
      if (lane < 9) { f32x2 o2; o2[0] = lane < 8 ? o0 : Mw; o2[1] = lane < 8 ? o1 : ll; *(LAS f32x2*)(L + L_OP + w * 80 + lane * 8) = o2; }
    }
    BARRIER();
    {
      s16x4 a[4];
#pragma unroll
      for (int t = 0; t < 4; ++t) {
        const int row = 4 * g + (c >> 2);
        const int chunk = (2 * (4 * w + t) + ((c & 3) >> 1)) ^ ((row & 7) << 1);
        a[t] = trd(L + L_WO + row * 1024 + chunk * 16 + (c & 1) * 8);
      }
      f32x4 ou[4]; f32x2 ml[4];
#pragma unroll
      for (int u = 0; u < 4; ++u) { ou[u] = *(const LAS f32x4*)(L + L_OP + u * 80 + g * 16); ml[u] = *(const LAS f32x2*)(L + L_OP + u * 80 + 64); }
      const float M = funkey(imax(imax(fkey(ml[0][0]), fkey(ml[1][0])), imax(fkey(ml[2][0]), fkey(ml[3][0]))));
      float f[4];
#pragma unroll
      for (int u = 0; u < 4; ++u) f[u] = __builtin_amdgcn_exp2f(ml[u][0] - M);
      const float Lt = (f[0] * ml[0][1] + f[1] * ml[1][1]) + (f[2] * ml[2][1] + f[3] * ml[3][1]);
      const f32x4 o4 = ((ou[0] * f[0] + ou[1] * f[1]) + (ou[2] * f[2] + ou[3] * f[3])) * __builtin_amdgcn_rcpf(Lt);
      v2u of; of[0] = pkbf(o4[0], o4[1]); of[1] = pkbf(o4[2], o4[3]);
      f32x4 d = {0.f, 0.f, 0.f, 0.f};
#pragma unroll
      for (int t = 0; t < 4; ++t) { v2u om; om[0] = of[0] & tm[t]; om[1] = of[1] & tm[t]; d = mfma16(a[t], om, d); }
      v4u gr; gr[0] = __float_as_uint(prs ? d[2] : d[0]); gr[1] = tlo; gr[2] = __float_as_uint(prs ? d[3] : d[1]); gr[3] = thi;
      v4u gm; gm[0] = __float_as_uint(prs ? dm[2] : dm[0]); gm[1] = tlo; gm[2] = __float_as_uint(prs ? dm[3] : dm[1]); gm[3] = thi;
      if (c < 8) {
        ST16(gr, soff);
        if (act) ST16(gm, moff);
      }
    }
    STAMP(8);
    {
      const int prod = (w & 3) * 64 + lane;
      const bool real = w < 4 || prod < 224;
      int off = (w < 4 ? W_S : W_M) + ((cc_cg * 256 + (real ? prod : prod - 64)) * 8 + cc_i) * 16;
      PIN(off);
      DELAY(KN_H3_DELAY);
      v4u gq;
      for (unsigned spin = 0; spin < SPIN_MAX; ++spin) {
        asm volatile("" ::: "memory");
        RETRY(2);
        gq = LD16(off);
        if (NONEBAD(TAGBAD(gq))) break;
      }
      STAMP(9);
      float a0 = real ? __uint_as_float(gq[0]) : 0.f, a1 = real ? __uint_as_float(gq[2]) : 0.f;
      wave_sum2(a0, a1);
      if (lane == 0) { f32x2 o2; o2[0] = a0; o2[1] = a1; *(LAS f32x2*)(L + (w < 4 ? L_FS : L_FM) + (w & 3) * 8) = o2; }
    }
    BARRIER();
    if (w == 0) {
      const int e = lane & 1;
      const unsigned xw = *(const LAS unsigned*)(L + L_XB), b2w = *(const LAS unsigned*)(L + L_XB + 4);
      const LAS float* fs = (const LAS float*)(L + L_FS) + e; const LAS float* fm = (const LAS float*)(L + L_FM) + e;
      const float s = (fs[0] + fs[2]) + (fs[4] + fs[6]), m = (fm[0] + fm[2]) + (fm[4] + fm[6]);
      const float z = rbf(rbf(m) + (e ? bfhi(b2w) : bflo(b2w)));
      const float gt = rbf(z * __builtin_amdgcn_rcpf(1.f + __builtin_amdgcn_exp2f(-1.4426950408889634f * z)));
      const float r = (e ? bfhi(xw) : bflo(xw)) + rbf(rbf(s) * gt);
      const float rn = dpp<XOR1>(r);
      if (lane == 0) {
        *(GAS unsigned*)(p.out + col) = pkbf(r, rn);
        if (b == 0) { const unsigned long long T = (unsigned long long)tlo | ((unsigned long long)thi << 32); __hip_atomic_store((GAS unsigned long long*)(p.ws + W_E), T, __ATOMIC_RELAXED, __HIP_MEMORY_SCOPE_AGENT); }
      }
    }
    STAMP(10);
  }
}

extern "C" void kernel_launch(void* const* d_in, const int* in_sizes, int n_in,
                              void* d_out, int out_size, void* d_ws, size_t ws_size,
                              hipStream_t stream) {
  Params p;
  p.x = (const u16*)d_in[0]; p.kc = (const u16*)d_in[1]; p.vc = (const u16*)d_in[2]; p.wq = (const u16*)d_in[3]; p.wo = (const u16*)d_in[4];
  p.w1 = (const u16*)d_in[6]; p.b1 = (const u16*)d_in[7]; p.w2 = (const u16*)d_in[8]; p.b2 = (const u16*)d_in[9];
  p.out = (u16*)d_out; p.ws = (unsigned char*)d_ws;
  fused_kernel<<<NB, NTHR, 0, stream>>>(p);
}
```

```cpp
#include <hip/hip_runtime.h>
#include <hip/hip_bf16.h>

#define LAS __attribute__((address_space(3)))
#define GAS __attribute__((address_space(1)))
typedef unsigned short u16;
typedef unsigned v4u __attribute__((ext_vector_type(4)));
typedef unsigned v2u __attribute__((ext_vector_type(2)));
typedef float f32x4 __attribute__((ext_vector_type(4)));
typedef float f32x2 __attribute__((ext_vector_type(2)));
typedef short s16x4 __attribute__((ext_vector_type(4)));
typedef short s16x8 __attribute__((ext_vector_type(8)));
typedef __bf16 bf16x8 __attribute__((ext_vector_type(8)));
typedef __bf16 bf16x2 __attribute__((ext_vector_type(2)));

#define KN_G1_DELAY 28
#define KN_H2_DELAY 12
#define KN_H3_DELAY 24
#define KN_WIN 24
#define KN_WAITOWN 0
#define KN_STAMPS 0

constexpr int D = 512, SK = 65536, WIN = 4096, FF = 1792;
constexpr int NB = 256, NTHR = 576;
constexpr int L_WQ = 0, L_K = 16384, L_V = 49152, L_WO = 81920, L_W2 = 98304, L_W1 = 106496, L_XN = 114688, L_Y = 115712, L_Q = 116736, L_P = 117760;
constexpr int L_MX = 118784, L_SM = L_MX + 128, L_TAG = L_SM + 128, L_H = L_TAG + 16, L_OP = L_H + 16, L_FS = L_OP + 512, L_FM = L_FS + 64, L_B1 = L_FM + 64, L_XB = L_B1 + 16, L_FLG = L_XB + 16, L_END = L_FLG + 16;
constexpr int W_E = 0;
constexpr int W_Q = 4096;
constexpr int W_Z = W_Q + 131072;
constexpr int W_O = W_Z + 262144;
constexpr int W_S = W_O + 1048576;
constexpr int W_TOUCH = W_S + 2097152;
constexpr int W_END = W_TOUCH + 4096;
constexpr size_t W_STAMP = (size_t)64 << 20;
constexpr unsigned SPIN_MAX = 1u << 17;

__device__ __forceinline__ float bflo(unsigned w) { return __uint_as_float(w << 16); }
__device__ __forceinline__ float bfhi(unsigned w) { return __uint_as_float(w & 0xffff0000u); }
__device__ __forceinline__ unsigned pkbf(float lo, float hi) { bf16x2 r; r[0] = (__bf16)lo; r[1] = (__bf16)hi; return __builtin_bit_cast(unsigned, r); }
__device__ __forceinline__ float rbf(float f) { return bflo(pkbf(f, f)); }
template <int CTRL> __device__ __forceinline__ float dpp(float x) {
  return __builtin_bit_cast(float, __builtin_amdgcn_mov_dpp(__builtin_bit_cast(int, x), CTRL, 0xf, 0xf, true));
}
constexpr int XOR1 = 0xB1, XOR2 = 0x4E, XOR7 = 0x141, XOR15 = 0x140, XOR8 = 0x128, ROR4 = 0x124;
__device__ __forceinline__ int fkey(float f) { const int i = __float_as_int(f); return i ^ ((i >> 31) & 0x7fffffff); }
__device__ __forceinline__ float funkey(int k) { return __int_as_float(k ^ ((k >> 31) & 0x7fffffff)); }
template <int CTRL> __device__ __forceinline__ int dppi(int x) { return __builtin_amdgcn_mov_dpp(x, CTRL, 0xf, 0xf, true); }
__device__ __forceinline__ int imax(int a, int b) { return a > b ? a : b; }
__device__ __forceinline__ int x16_imax(int x) { auto s = __builtin_amdgcn_permlane16_swap((unsigned)x, (unsigned)x, false, false); return imax((int)s[0], (int)s[1]); }
__device__ __forceinline__ int x32_imax(int x) { auto s = __builtin_amdgcn_permlane32_swap((unsigned)x, (unsigned)x, false, false); return imax((int)s[0], (int)s[1]); }
__device__ __forceinline__ float x16_sum(float x) { auto s = __builtin_amdgcn_permlane16_swap(__float_as_uint(x), __float_as_uint(x), false, false); return __uint_as_float(s[0]) + __uint_as_float(s[1]); }
__device__ __forceinline__ float x32_sum(float x) { auto s = __builtin_amdgcn_permlane32_swap(__float_as_uint(x), __float_as_uint(x), false, false); return __uint_as_float(s[0]) + __uint_as_float(s[1]); }
__device__ __forceinline__ float wave_sum(float v) {
  v += dpp<XOR1>(v); v += dpp<XOR2>(v); v += dpp<XOR7>(v); v += dpp<XOR15>(v);
  v = x16_sum(v); v = x32_sum(v); return v;
}
__device__ __forceinline__ void wave_sum2(float& a, float& b) {
  a += dpp<XOR1>(a); b += dpp<XOR1>(b); a += dpp<XOR2>(a); b += dpp<XOR2>(b); a += dpp<XOR7>(a); b += dpp<XOR7>(b); a += dpp<XOR15>(a); b += dpp<XOR15>(b);
  a = x16_sum(a); b = x16_sum(b); a = x32_sum(a); b = x32_sum(b);
}
__device__ __forceinline__ f32x4 mfma32(v4u a, v4u b, f32x4 c) {
  return __builtin_amdgcn_mfma_f32_16x16x32_bf16(__builtin_bit_cast(bf16x8, a), __builtin_bit_cast(bf16x8, b), c, 0, 0, 0);
}
__device__ __forceinline__ f32x4 mfma16(s16x4 a, v2u b, f32x4 c) {
  return __builtin_amdgcn_mfma_f32_16x16x16bf16_1k(a, __builtin_bit_cast(s16x4, b), c, 0, 0, 0);
}
__device__ __forceinline__ s16x4 trd(LAS unsigned char* p) { return __builtin_amdgcn_ds_read_tr16_b64_v4i16((LAS s16x4*)p); }
__device__ __forceinline__ v4u cat(s16x4 lo, s16x4 hi) { return __builtin_bit_cast(v4u, __builtin_shufflevector(lo, hi, 0, 1, 2, 3, 4, 5, 6, 7)); }
__device__ __forceinline__ v4u ln_packed(v4u x) {
  float v[8];
#pragma unroll
  for (int i = 0; i < 4; ++i) { v[2 * i] = bflo(x[i]); v[2 * i + 1] = bfhi(x[i]); }
  const float piv = __uint_as_float(__builtin_amdgcn_readfirstlane(x[0]) << 16);
  float s = 0.f, q = 0.f;
#pragma unroll
  for (int i = 0; i < 8; ++i) { const float d = v[i] - piv; s += d; q = fmaf(d, d, q); }
  wave_sum2(s, q);
  const float md = s * (1.0f / D);
  const float var = rbf(fmaxf(fmaf(-md, md, q * (1.0f / D)), 0.f));
  const float m = rbf(piv + md);
  const float sd = rbf(__builtin_amdgcn_sqrtf(rbf(var + 1e-5f)));
  const float inv = __builtin_amdgcn_rcpf(sd);
  v4u r;
#pragma unroll
  for (int i = 0; i < 4; ++i) r[i] = pkbf(rbf(v[2 * i] - m) * inv, rbf(v[2 * i + 1] - m) * inv);
  return r;
}
#define WAIT_VM(n) asm volatile("s_waitcnt vmcnt(" #n ")" ::: "memory")
#define WAIT_VMN(n) asm volatile("s_waitcnt vmcnt(%0)" :: "i"(n) : "memory")
static_assert(KN_WIN % 4 == 0 && KN_WIN >= 12 && KN_WIN <= 64 && (12 + KN_WIN) / 4 >= 6 && (20 + KN_WIN) / 4 <= 21, "loader window");
#define BARRIER() asm volatile("s_waitcnt lgkmcnt(0)\n\ts_barrier" ::: "memory")
#define RAWBAR() asm volatile("s_barrier" ::: "memory")
__device__ __forceinline__ void glds1(const void* sbase, unsigned v0, unsigned ldsaddr) {
  unsigned keep;
  asm volatile("s_mov_b32 %0, m0\n\ts_mov_b32 m0, %3\n\ts_nop 0\n\tglobal_load_lds_dwordx4 %1, %2 nt\n\ts_mov_b32 m0, %0"
               : "=&s"(keep) : "v"(v0), "s"(sbase), "s"(ldsaddr) : "memory");
}
__device__ __forceinline__ void glds2(const void* sbase, unsigned v0, unsigned v1, unsigned ldsaddr) {
  unsigned keep;
  asm volatile("s_mov_b32 %0, m0\n\ts_mov_b32 m0, %4\n\ts_nop 0\n\tglobal_load_lds_dwordx4 %1, %3 nt\n\tglobal_load_lds_dwordx4 %2, %3 offset:1024 nt\n\ts_mov_b32 m0, %0"
               : "=&s"(keep) : "v"(v0), "v"(v1), "s"(sbase), "s"(ldsaddr) : "memory");
}
__device__ __forceinline__ void glds2b(const void* sbase, unsigned v2, unsigned v3, unsigned ldsaddr) {
  unsigned keep;
  asm volatile("s_mov_b32 %0, m0\n\ts_mov_b32 m0, %4\n\ts_nop 0\n\tglobal_load_lds_dwordx4 %1, %3 offset:2048 nt\n\tglobal_load_lds_dwordx4 %2, %3 offset:3072 nt\n\ts_mov_b32 m0, %0"
               : "=&s"(keep) : "v"(v2), "v"(v3), "s"(sbase), "s"(ldsaddr) : "memory");
}
__device__ __forceinline__ void glds4(const void* sbase, unsigned v0, unsigned v1, unsigned v2, unsigned v3, unsigned ldsaddr) {
  unsigned keep;
  asm volatile("s_mov_b32 %0, m0\n\ts_mov_b32 m0, %6\n\ts_nop 0\n\tglobal_load_lds_dwordx4 %1, %5 nt\n\tglobal_load_lds_dwordx4 %2, %5 offset:1024 nt\n\t"
               "global_load_lds_dwordx4 %3, %5 offset:2048 nt\n\tglobal_load_lds_dwordx4 %4, %5 offset:3072 nt\n\ts_mov_b32 m0, %0"
               : "=&s"(keep) : "v"(v0), "v"(v1), "v"(v2), "v"(v3), "s"(sbase), "s"(ldsaddr) : "memory");
}
__device__ __forceinline__ unsigned long long rtc() { return __builtin_amdgcn_s_memrealtime(); }
#define STAMP(i) do { } while (0)
#define RETRY(i) do { } while (0)
#define DELAY(n) do { if ((n) > 0) __builtin_amdgcn_s_sleep((n) > 127 ? 127 : (n)); } while (0)
#define LD16(off) __builtin_amdgcn_raw_buffer_load_b128(rs, (off), 0, 16)
#define ST16(v, off) __builtin_amdgcn_raw_buffer_store_b128((v), rs, (off), 0, 16)
#define TAGBAD(x) (((x)[1] ^ tlo) | ((x)[3] ^ thi))
#define NONEBAD(v) (__builtin_amdgcn_ballot_w64((v) != 0u) == 0ull)
#define PIN(x) asm volatile("" : "+v"(x))
__device__ __forceinline__ void lds_add(LAS unsigned* q, unsigned v) { __hip_atomic_fetch_add(q, v, __ATOMIC_RELAXED, __HIP_MEMORY_SCOPE_WORKGROUP); }
__device__ __forceinline__ unsigned lds_peek(LAS unsigned* q) { return __builtin_amdgcn_readfirstlane(__hip_atomic_load(q, __ATOMIC_RELAXED, __HIP_MEMORY_SCOPE_WORKGROUP)); }

struct Params {
  const u16* x; const u16* kc; const u16* vc; const u16* wq; const u16* wo;
  const u16* w1; const u16* b1; const u16* w2; const u16* b2; u16* out; unsigned char* ws;
};

__global__ void __launch_bounds__(NTHR, 3) fused_kernel(Params p) {
  __shared__ __attribute__((aligned(16))) unsigned char lds[L_END];
  LAS unsigned char* L = (LAS unsigned char*)lds;
  const unsigned lbase = (unsigned)(size_t)L;
  const int tid = threadIdx.x, lane = tid & 63, wave = __builtin_amdgcn_readfirstlane(tid >> 6);
  const int b = blockIdx.x, h = b & 7, j = b >> 3;
  const int g = lane >> 4, c = lane & 15;
  STAMP(0);
  asm volatile("" :: "s"(p.x), "s"(p.kc), "s"(p.vc), "s"(p.wq), "s"(p.wo), "s"(p.w1), "s"(p.b1), "s"(p.w2), "s"(p.b2), "s"(p.out), "s"(p.ws));
  const __amdgpu_buffer_rsrc_t rs = __builtin_amdgcn_make_buffer_rsrc((void*)p.ws, 0, W_END, 0x00020000);
  const bool act = j < 28;
  const int cg = h * 28 + (act ? j : j - 28);
  const int cc_cg = h * 4 + (j >> 3), cc_i = j & 7;
  const int col = 16 * cc_cg + 2 * cc_i;

  LAS unsigned* flg = (LAS unsigned*)(L + L_FLG);
  if (tid == 0) { v4u z = {0u, 0u, 0u, 0u}; *(LAS v4u*)(L + L_FLG) = z; }
  if (wave == 0) {
    const int lc = j >> 2, rq = j & 3, lm = cg >> 3, kq = cg & 7;
    const unsigned kvoff = ((unsigned)h * SK + (SK - WIN) + (unsigned)j * 128u) * 256u;
    const unsigned char* kb = (const unsigned char*)p.kc + kvoff;
    const unsigned char* vb = (const unsigned char*)p.vc + kvoff;
    const unsigned k0 = (unsigned)g * 256u + (unsigned)((c ^ g) * 16), v0 = (unsigned)g * 256u + (unsigned)((c ^ (g << 2)) * 16), l16 = (unsigned)lane * 16u;
    BARRIER();
    STAMP(1);
    {
      const unsigned ph = (unsigned)(lane >> 4) & 1u, c8 = (unsigned)lane & 7u;
      const unsigned ce = (c8 ^ (ph << 1)) * 16u, co = (c8 ^ ((ph | 2u) << 1)) * 16u;
      const unsigned rq8 = (unsigned)(lane >> 3) * 8192u, r18 = (unsigned)(lane >> 3) * (unsigned)(FF * 2);
      const unsigned char* sq = (const unsigned char*)p.wq + (unsigned)((h * 512 + lc * 64) * 2 + rq * 128 * 8192);
      const unsigned char* s1 = (const unsigned char*)p.w1 + (unsigned)(lm * 128 + kq * 64 * (FF * 2));
      constexpr unsigned SQ = 8u * 8192u - 1024u, S1 = 8u * (FF * 2) - 1024u;
#pragma unroll 1
      for (int n = 0; n < 4; ++n) glds4(sq + (unsigned)(n * 32 * 8192), rq8 + ce, rq8 + co + SQ, rq8 + ce + 2u * SQ, rq8 + co + 3u * SQ, lbase + L_WQ + n * 4096);
#pragma unroll 1
      for (int n = 0; n < 2; ++n) glds4(s1 + (unsigned)(n * 32 * (FF * 2)), r18 + ce, r18 + co + S1, r18 + ce + 2u * S1, r18 + co + 3u * S1, lbase + L_W1 + n * 4096);
    }
    STAMP(2);
    constexpr int GFQ = (12 + KN_WIN) / 4, GF1 = (20 + KN_WIN) / 4;
#define GROUP_WAIT(gi) do { WAIT_VMN(KN_WIN - 4); \
      if ((gi) == GFQ) STAMP(6); if ((gi) == GF1) STAMP(7); if ((gi) == 14) STAMP(8); \
      if ((gi) == GFQ && lane == 0) __hip_atomic_store(flg + 0, 1u, __ATOMIC_RELAXED, __HIP_MEMORY_SCOPE_WORKGROUP); \
      if ((gi) == GF1 && lane == 0) __hip_atomic_store(flg + 2, 1u, __ATOMIC_RELAXED, __HIP_MEMORY_SCOPE_WORKGROUP); } while (0)
#pragma unroll 1
    for (int n = 0; n < 8; ++n) { GROUP_WAIT(6 + n); glds4(kb + n * 4096, k0, k0 ^ 64u, k0 ^ 128u, k0 ^ 192u, lbase + L_K + n * 4096); }
#pragma unroll 1
    for (int n = 0; n < 8; ++n) { GROUP_WAIT(14 + n); glds4(vb + n * 4096, v0, v0 ^ 16u, v0 ^ 32u, v0 ^ 48u, lbase + L_V + n * 4096); }
    STAMP(3);
    WAIT_VMN(32);
    RAWBAR();
    RAWBAR();
#pragma unroll 1
    for (int n = 0; n < 2; ++n) glds4((const unsigned char*)p.w2 + (unsigned)(cg * 8192 + n * 4096), l16, l16, l16, l16, lbase + L_W2 + n * 4096);
#pragma unroll 1
    for (int n = 0; n < 4; ++n) {
      const unsigned r0 = (unsigned)((4 * n) & 7) << 5;
      glds4((const unsigned char*)p.wo + (unsigned)((h * 512 + j * 16 + 4 * n) * 1024), l16 ^ r0, l16 ^ (r0 + 32u), l16 ^ (r0 + 64u), l16 ^ (r0 + 96u), lbase + L_WO + n * 4096);
    }
    WAIT_VMN(24);
    STAMP(4);
    RAWBAR();
    if (act) { WAIT_VMN(16); RAWBAR(); }
    WAIT_VM(0);
    STAMP(5);
    RAWBAR();
  } else {
    const int w = wave - 1;
    const int lc = j >> 2, rq = j & 3;
    const int lm = cg >> 3, kq = cg & 7;
    if (w == 0 || w == 4) {
      const v4u xr = *(const GAS v4u*)(p.x + lane * 8);
      v2u ev = {0u, 0u}; unsigned touch = 0u, small = 0u;
      if (w == 0) {
        ev = __builtin_amdgcn_raw_buffer_load_b64(rs, W_E, 0, 16);
        touch = __builtin_amdgcn_raw_buffer_load_b32(rs, W_TOUCH + (lane & 7) * 4, 0, 16);
        const unsigned long long a_b1 = (unsigned long long)(p.b1 + cg * 8), a_x = (unsigned long long)(p.x + col), a_b2 = (unsigned long long)(p.b2 + col);
        const unsigned long long a_sm = lane < 4 ? a_b1 + lane * 4 : (lane == 4 ? a_x : a_b2);
        small = *(const GAS unsigned*)a_sm;
      }
      __builtin_amdgcn_sched_barrier(0);
      RAWBAR();
      __builtin_amdgcn_sched_barrier(0);
      const v4u xn = ln_packed(xr);
      if (w == 0) {
        *(LAS v4u*)(L + L_XN + lane * 16) = xn;
        const unsigned long long E = (unsigned long long)(unsigned)__builtin_amdgcn_readfirstlane(ev[0]) | ((unsigned long long)(unsigned)__builtin_amdgcn_readfirstlane(ev[1]) << 32);
        const unsigned long long T = E * 6364136223846793005ull + 1442695040888963407ull;
        if (lane == 0) { v2u tg; tg[0] = (unsigned)T; tg[1] = (unsigned)(T >> 32); *(LAS v2u*)(L + L_TAG) = tg; }
        if (lane < 6) *(LAS unsigned*)(L + L_B1 + lane * 4) = small;
        asm volatile("s_waitcnt lgkmcnt(0)" ::: "memory");
        if (lane == 0) __hip_atomic_store(flg + 1, 1u, __ATOMIC_RELAXED, __HIP_MEMORY_SCOPE_WORKGROUP);
        asm volatile("" :: "v"(touch));
      } else {
        *(LAS v4u*)(L + L_Y + lane * 16) = ln_packed(xn);
        asm volatile("s_waitcnt lgkmcnt(0)" ::: "memory");
        if (lane == 0) __hip_atomic_store(flg + 3, 1u, __ATOMIC_RELAXED, __HIP_MEMORY_SCOPE_WORKGROUP);
      }
    } else {
      RAWBAR();
    }
    STAMP(1);
    for (unsigned spin = 0; spin < SPIN_MAX; ++spin) {
      const v4u f = *(volatile LAS v4u*)flg;
      asm volatile("" ::: "memory");
      const unsigned need = w < 4 ? (f[0] & f[1]) : (f[1] & f[2] & f[3]);
      if (__builtin_amdgcn_readfirstlane(need) != 0u) break;
      __builtin_amdgcn_s_sleep(1);
    }
    const v2u tg = *(const LAS v2u*)(L + L_TAG);
    const unsigned tlo = tg[0], thi = tg[1];
    {
      const int q4 = c >> 2, pp = c & 3, t = w & 3;
      const int ph = ((q4 >> 1) & 1) | ((g & 1) << 1);
      const int toff = (8 * g + q4) * 128 + (((2 * t + (pp >> 1)) ^ (ph << 1)) * 16) + (pp & 1) * 8;
      f32x4 a0 = {0.f, 0.f, 0.f, 0.f};
      int off;
      if (w < 4) {
#pragma unroll
        for (int s = 0; s < 4; ++s)
          a0 = mfma32(cat(trd(L + L_WQ + toff + s * 4096), trd(L + L_WQ + toff + s * 4096 + 512)), *(const LAS v4u*)(L + L_XN + (128 * rq + 32 * s + 8 * g) * 2), a0);
        off = W_Q + ((((h * 8 + lc) * 4 + rq) * 16) + 4 * t + g) * 16;
      } else {
#pragma unroll
        for (int s = 0; s < 2; ++s)
          a0 = mfma32(cat(trd(L + L_W1 + toff + s * 4096), trd(L + L_W1 + toff + s * 4096 + 512)), *(const LAS v4u*)(L + L_Y + (64 * kq + 32 * s + 8 * g) * 2), a0);
        off = W_Z + ((lm * 8 + 2 * t + (g >> 1)) * 8 + kq) * 128 + (2 * (g & 1) + c) * 16;
      }
      v4u gr;
      gr[0] = w < 4 ? pkbf(a0[0], a0[1]) : __float_as_uint((c & 1) ? a0[2] : a0[0]); gr[1] = tlo;
      gr[2] = w < 4 ? pkbf(a0[2], a0[3]) : __float_as_uint((c & 1) ? a0[3] : a0[1]); gr[3] = thi;
      if (w < 4 ? c == 0 : (c < 2 && act)) ST16(gr, off);
    }
    STAMP(11);
    const int qoff = W_Q + (h * 8 + w) * 1024 + lane * 16;
    const int zoff = W_Z + ((lm * 8 + kq) * 8 + ((lane & 31) >> 2)) * 128 + (lane & 3) * 16;
    DELAY(KN_G1_DELAY);
    const bool needz = (w == 7) && act;
    v4u q0, zq = {0u, tlo, 0u, thi};
    for (unsigned spin = 0; spin < SPIN_MAX; ++spin) {
      asm volatile("" ::: "memory");
      RETRY(0);
      q0 = LD16(qoff);
      if (needz) zq = LD16(zoff);
      if (NONEBAD(TAGBAD(q0) | TAGBAD(zq))) break;
    }
    {
      float u0 = bflo(q0[0]), u1 = bfhi(q0[0]), u2 = bflo(q0[2]), u3 = bfhi(q0[2]);
      u0 = x16_sum(u0); u1 = x16_sum(u1); u2 = x16_sum(u2); u3 = x16_sum(u3);
      u0 = x32_sum(u0); u1 = x32_sum(u1); u2 = x32_sum(u2); u3 = x32_sum(u3);
      const int dim = (w & 1) * 64 + 4 * (lane & 15);
      v2u d; d[0] = pkbf(u0, u1); d[1] = pkbf(u2, u3);
      if (lane < 16) *(LAS v2u*)(L + L_Q + (((dim >> 5) * 4 + ((dim >> 3) & 3)) * 4 + (w >> 1)) * 16 + (dim & 7) * 2) = d;
    }
    STAMP(2);
    BARRIER();
    STAMP(3);
    f32x4 sc = {0.f, 0.f, 0.f, 0.f};
#pragma unroll
    for (int s = 0; s < 4; ++s) sc = mfma32(*(const LAS v4u*)(L + L_K + (w * 16 + c) * 256 + (((4 * s + g) ^ c) * 16)), *(const LAS v4u*)(L + L_Q + ((s * 4 + g) * 4 + (c & 3)) * 16), sc);
    {
      int mx = imax(imax(fkey(sc[0]), fkey(sc[1])), imax(fkey(sc[2]), fkey(sc[3])));
      mx = x16_imax(mx); mx = x32_imax(mx);
      if (lane < 4) *(LAS int*)(L + L_MX + (lane * 8 + w) * 4) = mx;
    }
    BARRIER();
    float mc;
    constexpr float C2 = 0.08838834764831845f * 1.4426950408889634f;
    {
      const v4u ma = *(const LAS v4u*)(L + L_MX + (c & 3) * 32), mb = *(const LAS v4u*)(L + L_MX + (c & 3) * 32 + 16);
      const int M = imax(imax(imax((int)ma[0], (int)ma[1]), imax((int)ma[2], (int)ma[3])), imax(imax((int)mb[0], (int)mb[1]), imax((int)mb[2], (int)mb[3])));
      mc = funkey(M) * C2;
      float e[4];
#pragma unroll
      for (int r = 0; r < 4; ++r) e[r] = __builtin_amdgcn_exp2f(fmaf(sc[r], C2, -mc));
      v2u pk; pk[0] = pkbf(e[0], e[1]); pk[1] = pkbf(e[2], e[3]);
      if (c < 4) *(LAS v2u*)(L + L_P + (((w >> 1) * 4 + 2 * (w & 1) + (g >> 1)) * 4 + c) * 16 + (g & 1) * 8) = pk;
      float lsum = (bflo(pk[0]) + bfhi(pk[0])) + (bflo(pk[1]) + bfhi(pk[1]));
      lsum = x16_sum(lsum); lsum = x32_sum(lsum);
      if (lane < 4) *(LAS float*)(L + L_SM + (lane * 8 + w) * 4) = lsum;
    }
    WAIT_VM(0);
    STAMP(4);
    BARRIER();
    {
      f32x4 oa = {0.f, 0.f, 0.f, 0.f};
      const int q4 = c >> 2, pp = c & 3;
#pragma unroll
      for (int s = 0; s < 4; ++s) {
        const v4u pf = *(const LAS v4u*)(L + L_P + ((s * 4 + g) * 4 + (c & 3)) * 16);
        s16x4 a[2];
#pragma unroll
        for (int t2 = 0; t2 < 2; ++t2) {
          const int row = 32 * s + 8 * g + 4 * t2 + q4;
          const int mask = (q4 << 2) | ((2 * g + t2) & 3);
          a[t2] = trd(L + L_V + row * 256 + (((2 * w + (pp >> 1)) ^ mask) * 16) + (pp & 1) * 8);
        }
        oa = mfma32(cat(a[0], a[1]), pf, oa);
      }
      const int head = c & 3, k = c >> 2;
      const f32x4 la = *(const LAS f32x4*)(L + L_SM + head * 32), lb = *(const LAS f32x4*)(L + L_SM + head * 32 + 16);
      const float l8 = ((la[0] + la[1]) + (la[2] + la[3])) + ((lb[0] + lb[1]) + (lb[2] + lb[3]));
      v4u gr; gr[0] = __float_as_uint((k & 1) ? oa[2] : oa[0]); gr[1] = __float_as_uint((k & 1) ? oa[3] : oa[1]); gr[2] = __float_as_uint((k & 1) ? l8 : mc); gr[3] = (k & 1) ? thi : tlo;
      if (k < 2) ST16(gr, W_O + ((((h * 32 + head * 8 + w) * 32 + j) * 8) + 2 * g + k) * 16);
    }
    STAMP(5);
    const int tsel = (c >> 1) & 3, prs = c & 1;
    unsigned tm[4];
#pragma unroll
    for (int t = 0; t < 4; ++t) tm[t] = tsel == t ? ~0u : 0u;
    f32x4 dm = {0.f, 0.f, 0.f, 0.f};
    if (act) {
      if (needz) {
        float z0 = __uint_as_float(zq[0]), z1 = __uint_as_float(zq[2]);
        z0 += dpp<ROR4>(z0); z1 += dpp<ROR4>(z1); z0 += dpp<XOR8>(z0); z1 += dpp<XOR8>(z1); z0 = x16_sum(z0); z1 = x16_sum(z1);
        const unsigned bw = *(const LAS unsigned*)(L + L_B1 + (lane & 3) * 4);
        float hv[2];
#pragma unroll
        for (int e = 0; e < 2; ++e) {
          const float z = rbf(rbf(e ? z1 : z0) + (e ? bfhi(bw) : bflo(bw)));
          const float uu = 0.7978845608028654f * (z + 0.044715f * z * z * z);
          const float th = 1.f - 2.f * __builtin_amdgcn_rcpf(1.f + __builtin_amdgcn_exp2f(2.885390081777927f * uu));
          hv[e] = 0.5f * z * (1.f + th);
        }
        if (lane < 4) *(LAS unsigned*)(L + L_H + lane * 4) = pkbf(hv[0], hv[1]);
      }
      BARRIER();
      v2u hf = *(const LAS v2u*)(L + L_H + (g & 1) * 8);
      if (g >= 2) { hf[0] = 0u; hf[1] = 0u; }
#pragma unroll
      for (int t = 0; t < 4; ++t) {
        const s16x4 a = trd(L + L_W2 + ((4 * g + (c >> 2)) & 7) * 1024 + (4 * w + t) * 32 + (c & 3) * 8);
        v2u hm; hm[0] = hf[0] & tm[t]; hm[1] = hf[1] & tm[t];
        dm = mfma16(a, hm, dm);
      }
    }
    STAMP(6);
    int soff = W_S + (((2 * (4 * w + tsel) + (g >> 1)) * 256 + b) * 4 + 2 * (g & 1) + prs) * 32 + (c >> 3) * 16;
    PIN(soff);
    if (w < 4) {
      int offa = W_O + (((h * 32 + j) * 32 + 8 * w) * 8 + lane) * 16;
      unsigned tex = (lane & 1) ? thi : tlo;
      PIN(offa); PIN(tex);
      DELAY(KN_H2_DELAY);
      v4u ga;
      for (unsigned spin = 0; spin < SPIN_MAX; ++spin) {
        asm volatile("" ::: "memory");
        RETRY(1);
        ga = LD16(offa);
        if (NONEBAD(ga[3] ^ tex)) break;
      }
      STAMP(7);
      const float aux = __uint_as_float(ga[2]), axn = dpp<XOR1>(aux);
      const float mj = (lane & 1) ? axn : aux, lj = (lane & 1) ? aux : axn;
      int mk = fkey(mj);
      mk = imax(mk, dppi<XOR8>(mk)); mk = x16_imax(mk); mk = x32_imax(mk);
      const float Mw = funkey(mk);
      const float wt = __builtin_amdgcn_exp2f(mj - Mw);
      float o0 = __uint_as_float(ga[0]) * wt, o1 = __uint_as_float(ga[1]) * wt, ll = lj * wt;
      o0 += dpp<XOR8>(o0); o1 += dpp<XOR8>(o1); ll += dpp<XOR8>(ll);
      o0 = x16_sum(o0); o1 = x16_sum(o1); ll = x16_sum(ll);
      o0 = x32_sum(o0); o1 = x32_sum(o1); ll = x32_sum(ll);
      if (lane < 9) { f32x2 o2; o2[0] = lane < 8 ? o0 : Mw; o2[1] = lane < 8 ? o1 : ll; *(LAS f32x2*)(L + L_OP + w * 80 + lane * 8) = o2; }
    }
    BARRIER();
    {
      s16x4 a[4];
#pragma unroll
      for (int t = 0; t < 4; ++t) {
        const int row = 4 * g + (c >> 2);
        const int chunk = (2 * (4 * w + t) + ((c & 3) >> 1)) ^ ((row & 7) << 1);
        a[t] = trd(L + L_WO + row * 1024 + chunk * 16 + (c & 1) * 8);
      }
      f32x4 ou[4]; f32x2 ml[4];
#pragma unroll
      for (int u = 0; u < 4; ++u) { ou[u] = *(const LAS f32x4*)(L + L_OP + u * 80 + g * 16); ml[u] = *(const LAS f32x2*)(L + L_OP + u * 80 + 64); }
      const float M = funkey(imax(imax(fkey(ml[0][0]), fkey(ml[1][0])), imax(fkey(ml[2][0]), fkey(ml[3][0]))));
      float f[4];
#pragma unroll
      for (int u = 0; u < 4; ++u) f[u] = __builtin_amdgcn_exp2f(ml[u][0] - M);
      const float Lt = (f[0] * ml[0][1] + f[1] * ml[1][1]) + (f[2] * ml[2][1] + f[3] * ml[3][1]);
      const f32x4 o4 = ((ou[0] * f[0] + ou[1] * f[1]) + (ou[2] * f[2] + ou[3] * f[3])) * __builtin_amdgcn_rcpf(Lt);
      v2u of; of[0] = pkbf(o4[0], o4[1]); of[1] = pkbf(o4[2], o4[3]);
      f32x4 d = {0.f, 0.f, 0.f, 0.f};
#pragma unroll
      for (int t = 0; t < 4; ++t) { v2u om; om[0] = of[0] & tm[t]; om[1] = of[1] & tm[t]; d = mfma16(a[t], om, d); }
      const f32x4 dv = c < 8 ? d : dm;
      v4u gr; gr[0] = __float_as_uint(prs ? dv[2] : dv[0]); gr[1] = tlo; gr[2] = __float_as_uint(prs ? dv[3] : dv[1]); gr[3] = thi;
      ST16(gr, soff);
    }
    STAMP(8);
    {
      const int cc = 8 * cc_cg + cc_i;
      int off = W_S + (((cc >> 2) * 256 + 32 * w + (lane >> 1)) * 4 + (cc & 3)) * 32 + (lane & 1) * 16;
      PIN(off);
      DELAY(KN_H3_DELAY);
      v4u gq;
      for (unsigned spin = 0; spin < SPIN_MAX; ++spin) {
        asm volatile("" ::: "memory");
        RETRY(2);
        gq = LD16(off);
        if (NONEBAD(TAGBAD(gq))) break;
      }
      STAMP(9);
      float a0 = __uint_as_float(gq[0]), a1 = __uint_as_float(gq[2]);
      a0 += dpp<XOR2>(a0); a1 += dpp<XOR2>(a1); a0 += dpp<ROR4>(a0); a1 += dpp<ROR4>(a1); a0 += dpp<XOR8>(a0); a1 += dpp<XOR8>(a1);
      a0 = x16_sum(a0); a1 = x16_sum(a1); a0 = x32_sum(a0); a1 = x32_sum(a1);
      if (lane < 2) { f32x2 o2; o2[0] = a0; o2[1] = a1; *(LAS f32x2*)(L + L_FS + lane * 64 + w * 8) = o2; }
    }
    BARRIER();
    if (w == 0) {
      const int e = lane & 1;
      const unsigned xw = *(const LAS unsigned*)(L + L_XB), b2w = *(const LAS unsigned*)(L + L_XB + 4);
      const LAS float* fs = (const LAS float*)(L + L_FS) + e; const LAS float* fm = (const LAS float*)(L + L_FM) + e;
      const float s = ((fs[0] + fs[2]) + (fs[4] + fs[6])) + ((fs[8] + fs[10]) + (fs[12] + fs[14])), m = ((fm[0] + fm[2]) + (fm[4] + fm[6])) + ((fm[8] + fm[10]) + (fm[12] + fm[14]));
      const float z = rbf(rbf(m) + (e ? bfhi(b2w) : bflo(b2w)));
      const float gt = rbf(z * __builtin_amdgcn_rcpf(1.f + __builtin_amdgcn_exp2f(-1.4426950408889634f * z)));
      const float r = (e ? bfhi(xw) : bflo(xw)) + rbf(rbf(s) * gt);
      const float rn = dpp<XOR1>(r);
      if (lane == 0) {
        *(GAS unsigned*)(p.out + col) = pkbf(r, rn);
        if (b == 0) { const unsigned long long T = (unsigned long long)tlo | ((unsigned long long)thi << 32); __hip_atomic_store((GAS unsigned long long*)(p.ws + W_E), T, __ATOMIC_RELAXED, __HIP_MEMORY_SCOPE_AGENT); }
      }
    }
    STAMP(10);
  }
}

extern "C" void kernel_launch(void* const* d_in, const int* in_sizes, int n_in,
                              void* d_out, int out_size, void* d_ws, size_t ws_size,
                              hipStream_t stream) {
  Params p;
  p.x = (const u16*)d_in[0]; p.kc = (const u16*)d_in[1]; p.vc = (const u16*)d_in[2]; p.wq = (const u16*)d_in[3]; p.wo = (const u16*)d_in[4];
  p.w1 = (const u16*)d_in[6]; p.b1 = (const u16*)d_in[7]; p.w2 = (const u16*)d_in[8]; p.b2 = (const u16*)d_in[9];
  p.out = (u16*)d_out; p.ws = (unsigned char*)d_ws;
  fused_kernel<<<NB, NTHR, 0, stream>>>(p);
}
```

```cpp
#include <hip/hip_runtime.h>
#include <hip/hip_bf16.h>

#define LAS __attribute__((address_space(3)))
#define GAS __attribute__((address_space(1)))
typedef unsigned short u16;
typedef unsigned v4u __attribute__((ext_vector_type(4)));
typedef unsigned v2u __attribute__((ext_vector_type(2)));
typedef float f32x4 __attribute__((ext_vector_type(4)));
typedef float f32x2 __attribute__((ext_vector_type(2)));
typedef short s16x4 __attribute__((ext_vector_type(4)));
typedef short s16x8 __attribute__((ext_vector_type(8)));
typedef __bf16 bf16x8 __attribute__((ext_vector_type(8)));
typedef __bf16 bf16x2 __attribute__((ext_vector_type(2)));

#define KN_G1_DELAY 28
#define KN_H2_DELAY 30
#define KN_H3_DELAY 36
#define KN_WIN 24
#define KN_WIN2 KN_WIN
#define KN_WAITOWN 0
#define KN_STAMPS 0

constexpr int D = 512, SK = 65536, WIN = 4096, FF = 1792;
constexpr int NB = 256, NTHR = 640;
constexpr int L_WQ = 0, L_K = 16384, L_V = 49152, L_WO = 81920, L_W2 = 98304, L_W1 = 106496, L_XN = 114688, L_Y = 115712, L_Q = 116736, L_P = 117760;
constexpr int L_MX = 118784, L_SM = L_MX + 128, L_TAG = L_SM + 128, L_H = L_TAG + 16, L_OP = L_H + 16, L_FS = L_OP + 512, L_FM = L_FS + 64, L_B1 = L_FM + 64, L_XB = L_B1 + 16, L_FLG = L_XB + 16, L_MP = L_FLG + 32, L_END = L_MP + 2048;
constexpr int W_E = 0;
constexpr int W_Q = 4096;
constexpr int W_Z = W_Q + 131072;
constexpr int W_O = W_Z + 262144;
constexpr int W_S = W_O + 1048576;
constexpr int W_TOUCH = W_S + 2097152;
constexpr int W_END = W_TOUCH + 4096;
constexpr size_t W_STAMP = (size_t)64 << 20;
constexpr unsigned SPIN_MAX = 1u << 17;

__device__ __forceinline__ float bflo(unsigned w) { return __uint_as_float(w << 16); }
__device__ __forceinline__ float bfhi(unsigned w) { return __uint_as_float(w & 0xffff0000u); }
__device__ __forceinline__ unsigned pkbf(float lo, float hi) { bf16x2 r; r[0] = (__bf16)lo; r[1] = (__bf16)hi; return __builtin_bit_cast(unsigned, r); }
__device__ __forceinline__ float rbf(float f) { return bflo(pkbf(f, f)); }
template <int CTRL> __device__ __forceinline__ float dpp(float x) {
  return __builtin_bit_cast(float, __builtin_amdgcn_mov_dpp(__builtin_bit_cast(int, x), CTRL, 0xf, 0xf, true));
}
constexpr int XOR1 = 0xB1, XOR2 = 0x4E, XOR7 = 0x141, XOR15 = 0x140, XOR8 = 0x128, ROR4 = 0x124;
__device__ __forceinline__ int fkey(float f) { const int i = __float_as_int(f); return i ^ ((i >> 31) & 0x7fffffff); }
__device__ __forceinline__ float funkey(int k) { return __int_as_float(k ^ ((k >> 31) & 0x7fffffff)); }
template <int CTRL> __device__ __forceinline__ int dppi(int x) { return __builtin_amdgcn_mov_dpp(x, CTRL, 0xf, 0xf, true); }
__device__ __forceinline__ int imax(int a, int b) { return a > b ? a : b; }
__device__ __forceinline__ int x16_imax(int x) { auto s = __builtin_amdgcn_permlane16_swap((unsigned)x, (unsigned)x, false, false); return imax((int)s[0], (int)s[1]); }
__device__ __forceinline__ int x32_imax(int x) { auto s = __builtin_amdgcn_permlane32_swap((unsigned)x, (unsigned)x, false, false); return imax((int)s[0], (int)s[1]); }
__device__ __forceinline__ float x16_sum(float x) { auto s = __builtin_amdgcn_permlane16_swap(__float_as_uint(x), __float_as_uint(x), false, false); return __uint_as_float(s[0]) + __uint_as_float(s[1]); }
__device__ __forceinline__ float x32_sum(float x) { auto s = __builtin_amdgcn_permlane32_swap(__float_as_uint(x), __float_as_uint(x), false, false); return __uint_as_float(s[0]) + __uint_as_float(s[1]); }
__device__ __forceinline__ float wave_sum(float v) {
  v += dpp<XOR1>(v); v += dpp<XOR2>(v); v += dpp<XOR7>(v); v += dpp<XOR15>(v);
  v = x16_sum(v); v = x32_sum(v); return v;
}
__device__ __forceinline__ void wave_sum2(float& a, float& b) {
  a += dpp<XOR1>(a); b += dpp<XOR1>(b); a += dpp<XOR2>(a); b += dpp<XOR2>(b); a += dpp<XOR7>(a); b += dpp<XOR7>(b); a += dpp<XOR15>(a); b += dpp<XOR15>(b);
  a = x16_sum(a); b = x16_sum(b); a = x32_sum(a); b = x32_sum(b);
}
__device__ __forceinline__ f32x4 mfma32(v4u a, v4u b, f32x4 c) {
  return __builtin_amdgcn_mfma_f32_16x16x32_bf16(__builtin_bit_cast(bf16x8, a), __builtin_bit_cast(bf16x8, b), c, 0, 0, 0);
}
__device__ __forceinline__ f32x4 mfma16(s16x4 a, v2u b, f32x4 c) {
  return __builtin_amdgcn_mfma_f32_16x16x16bf16_1k(a, __builtin_bit_cast(s16x4, b), c, 0, 0, 0);
}
__device__ __forceinline__ s16x4 trd(LAS unsigned char* p) { return __builtin_amdgcn_ds_read_tr16_b64_v4i16((LAS s16x4*)p); }
__device__ __forceinline__ v4u cat(s16x4 lo, s16x4 hi) { return __builtin_bit_cast(v4u, __builtin_shufflevector(lo, hi, 0, 1, 2, 3, 4, 5, 6, 7)); }
__device__ __forceinline__ v4u ln_packed(v4u x) {
  float v[8];
#pragma unroll
  for (int i = 0; i < 4; ++i) { v[2 * i] = bflo(x[i]); v[2 * i + 1] = bfhi(x[i]); }
  const float piv = __uint_as_float(__builtin_amdgcn_readfirstlane(x[0]) << 16);
  float s = 0.f, q = 0.f;
#pragma unroll
  for (int i = 0; i < 8; ++i) { const float d = v[i] - piv; s += d; q = fmaf(d, d, q); }
  wave_sum2(s, q);
  const float md = s * (1.0f / D);
  const float var = rbf(fmaxf(fmaf(-md, md, q * (1.0f / D)), 0.f));
  const float m = rbf(piv + md);
  const float sd = rbf(__builtin_amdgcn_sqrtf(rbf(var + 1e-5f)));
  const float inv = __builtin_amdgcn_rcpf(sd);
  v4u r;
#pragma unroll
  for (int i = 0; i < 4; ++i) r[i] = pkbf(rbf(v[2 * i] - m) * inv, rbf(v[2 * i + 1] - m) * inv);
  return r;
}
#define WAIT_VM(n) asm volatile("s_waitcnt vmcnt(" #n ")" ::: "memory")
#define WAIT_VMN(n) asm volatile("s_waitcnt vmcnt(%0)" :: "i"(n) : "memory")
static_assert(KN_WIN % 4 == 0 && KN_WIN >= 8 && KN_WIN <= 64 && KN_WIN2 % 4 == 0 && KN_WIN2 >= 8 && KN_WIN2 <= 36, "loader window");
#define BARRIER() asm volatile("s_waitcnt lgkmcnt(0)\n\ts_barrier" ::: "memory")
#define RAWBAR() asm volatile("s_barrier" ::: "memory")
__device__ __forceinline__ void glds1(const void* sbase, unsigned v0, unsigned ldsaddr) {
  unsigned keep;
  asm volatile("s_mov_b32 %0, m0\n\ts_mov_b32 m0, %3\n\ts_nop 0\n\tglobal_load_lds_dwordx4 %1, %2 nt\n\ts_mov_b32 m0, %0"
               : "=&s"(keep) : "v"(v0), "s"(sbase), "s"(ldsaddr) : "memory");
}
__device__ __forceinline__ void glds2(const void* sbase, unsigned v0, unsigned v1, unsigned ldsaddr) {
  unsigned keep;
  asm volatile("s_mov_b32 %0, m0\n\ts_mov_b32 m0, %4\n\ts_nop 0\n\tglobal_load_lds_dwordx4 %1, %3 nt\n\tglobal_load_lds_dwordx4 %2, %3 offset:1024 nt\n\ts_mov_b32 m0, %0"
               : "=&s"(keep) : "v"(v0), "v"(v1), "s"(sbase), "s"(ldsaddr) : "memory");
}
__device__ __forceinline__ void glds2b(const void* sbase, unsigned v2, unsigned v3, unsigned ldsaddr) {
  unsigned keep;
  asm volatile("s_mov_b32 %0, m0\n\ts_mov_b32 m0, %4\n\ts_nop 0\n\tglobal_load_lds_dwordx4 %1, %3 offset:2048 nt\n\tglobal_load_lds_dwordx4 %2, %3 offset:3072 nt\n\ts_mov_b32 m0, %0"
               : "=&s"(keep) : "v"(v2), "v"(v3), "s"(sbase), "s"(ldsaddr) : "memory");
}
__device__ __forceinline__ void glds4(const void* sbase, unsigned v0, unsigned v1, unsigned v2, unsigned v3, unsigned ldsaddr) {
  unsigned keep;
  asm volatile("s_mov_b32 %0, m0\n\ts_mov_b32 m0, %6\n\ts_nop 0\n\tglobal_load_lds_dwordx4 %1, %5 nt\n\tglobal_load_lds_dwordx4 %2, %5 offset:1024 nt\n\t"
               "global_load_lds_dwordx4 %3, %5 offset:2048 nt\n\tglobal_load_lds_dwordx4 %4, %5 offset:3072 nt\n\ts_mov_b32 m0, %0"
               : "=&s"(keep) : "v"(v0), "v"(v1), "v"(v2), "v"(v3), "s"(sbase), "s"(ldsaddr) : "memory");
}
__device__ __forceinline__ unsigned long long rtc() { return __builtin_amdgcn_s_memrealtime(); }
#define STAMP(i) do { } while (0)
#define RETRY(i) do { } while (0)
#define DELAY(n) do { if ((n) > 0) __builtin_amdgcn_s_sleep((n) > 127 ? 127 : (n)); } while (0)
#define LD16(off) __builtin_amdgcn_raw_buffer_load_b128(rs, (off), 0, 16)
#define ST16(v, off) __builtin_amdgcn_raw_buffer_store_b128((v), rs, (off), 0, 16)
#define TAGBAD(x) (((x)[1] ^ tlo) | ((x)[3] ^ thi))
#define NONEBAD(v) (__builtin_amdgcn_ballot_w64((v) != 0u) == 0ull)
#define PIN(x) asm volatile("" : "+v"(x))
__device__ __forceinline__ void lds_add(LAS unsigned* q, unsigned v) { __hip_atomic_fetch_add(q, v, __ATOMIC_RELAXED, __HIP_MEMORY_SCOPE_WORKGROUP); }
__device__ __forceinline__ unsigned lds_peek(LAS unsigned* q) { return __builtin_amdgcn_readfirstlane(__hip_atomic_load(q, __ATOMIC_RELAXED, __HIP_MEMORY_SCOPE_WORKGROUP)); }

constexpr int first_group(int need) { for (int g = 6; g < 22; ++g) if (4 * g - ((g < 14 ? KN_WIN : KN_WIN2) - 4) >= need) return g; return 99; }

struct Params {
  const u16* x; const u16* kc; const u16* vc; const u16* wq; const u16* wo;
  const u16* w1; const u16* b1; const u16* w2; const u16* b2; u16* out; unsigned char* ws;
};

__global__ void __launch_bounds__(NTHR, 3) fused_kernel(Params p) {
  __shared__ __attribute__((aligned(16))) unsigned char lds[L_END];
  LAS unsigned char* L = (LAS unsigned char*)lds;
  const unsigned lbase = (unsigned)(size_t)L;
  const int tid = threadIdx.x, lane = tid & 63, wave = __builtin_amdgcn_readfirstlane(tid >> 6);
  const int b = blockIdx.x, h = b & 7, j = b >> 3;
  const int g = lane >> 4, c = lane & 15;
  STAMP(0);
  asm volatile("" :: "s"(p.x), "s"(p.kc), "s"(p.vc), "s"(p.wq), "s"(p.wo), "s"(p.w1), "s"(p.b1), "s"(p.w2), "s"(p.b2), "s"(p.out), "s"(p.ws));
  const __amdgpu_buffer_rsrc_t rs = __builtin_amdgcn_make_buffer_rsrc((void*)p.ws, 0, W_END, 0x00020000);
  const bool act = j < 28;
  const int cg = h * 28 + (act ? j : j - 28);
  const int cc_cg = h * 4 + (j >> 3), cc_i = j & 7;
  const int col = 16 * cc_cg + 2 * cc_i;

  LAS unsigned* flg = (LAS unsigned*)(L + L_FLG);
  if (tid < 2) { v4u z = {0u, 0u, 0u, 0u}; *(LAS v4u*)(L + L_FLG + tid * 16) = z; }
  if (wave == 0) {
    const int lc = j >> 2, rq = j & 3, lm = cg >> 3, kq = cg & 7;
    const unsigned kvoff = ((unsigned)h * SK + (SK - WIN) + (unsigned)j * 128u) * 256u;
    const unsigned char* kb = (const unsigned char*)p.kc + kvoff;
    const unsigned char* vb = (const unsigned char*)p.vc + kvoff;
    const unsigned k0 = (unsigned)g * 256u + (unsigned)((c ^ g) * 16), v0 = (unsigned)g * 256u + (unsigned)((c ^ (g << 2)) * 16), l16 = (unsigned)lane * 16u;
    BARRIER();
    STAMP(1);
    {
      const unsigned ph = (unsigned)(lane >> 4) & 1u, c8 = (unsigned)lane & 7u;
      const unsigned ce = (c8 ^ (ph << 1)) * 16u, co = (c8 ^ ((ph | 2u) << 1)) * 16u;
      const unsigned rq8 = (unsigned)(lane >> 3) * 8192u, r18 = (unsigned)(lane >> 3) * (unsigned)(FF * 2);
      const unsigned char* sq = (const unsigned char*)p.wq + (unsigned)((h * 512 + lc * 64) * 2 + rq * 128 * 8192);
      const unsigned char* s1 = (const unsigned char*)p.w1 + (unsigned)(lm * 128 + kq * 64 * (FF * 2));
      constexpr unsigned SQ = 8u * 8192u - 1024u, S1 = 8u * (FF * 2) - 1024u;
#pragma unroll 1
      for (int n = 0; n < 4; ++n) glds4(sq + (unsigned)(n * 32 * 8192), rq8 + ce, rq8 + co + SQ, rq8 + ce + 2u * SQ, rq8 + co + 3u * SQ, lbase + L_WQ + n * 4096);
#pragma unroll 1
      for (int n = 0; n < 2; ++n) glds4(s1 + (unsigned)(n * 32 * (FF * 2)), r18 + ce, r18 + co + S1, r18 + ce + 2u * S1, r18 + co + 3u * S1, lbase + L_W1 + n * 4096);
    }
    STAMP(2);
    constexpr int GFQ = first_group(16), GF1 = first_group(24), GFK = first_group(56);
    static_assert(GFQ <= 21 && GF1 <= 21 && GFK <= 21, "flags are set inside the K and V loops");
#define GROUP_FLAGS(gi) do { \
      if ((gi) == GFQ) STAMP(6); if ((gi) == GF1) STAMP(7); if ((gi) == 14) STAMP(8); \
      if ((gi) == GFQ && lane == 0) __hip_atomic_store(flg + 0, 1u, __ATOMIC_RELAXED, __HIP_MEMORY_SCOPE_WORKGROUP); \
      if ((gi) == GF1 && lane == 0) __hip_atomic_store(flg + 2, 1u, __ATOMIC_RELAXED, __HIP_MEMORY_SCOPE_WORKGROUP); \
      if ((gi) == GFK && lane == 0) __hip_atomic_store(flg + 5, 1u, __ATOMIC_RELAXED, __HIP_MEMORY_SCOPE_WORKGROUP); } while (0)
#pragma unroll 1
    for (int n = 0; n < 8; ++n) { WAIT_VMN(KN_WIN - 4); GROUP_FLAGS(6 + n); glds4(kb + n * 4096, k0, k0 ^ 64u, k0 ^ 128u, k0 ^ 192u, lbase + L_K + n * 4096); }
#pragma unroll 1
    for (int n = 0; n < 8; ++n) { WAIT_VMN(KN_WIN2 - 4); GROUP_FLAGS(14 + n); glds4(vb + n * 4096, v0, v0 ^ 16u, v0 ^ 32u, v0 ^ 48u, lbase + L_V + n * 4096); }
    STAMP(3);
    WAIT_VMN(32);
    RAWBAR();
    RAWBAR();
#pragma unroll 1
    for (int n = 0; n < 2; ++n) glds4((const unsigned char*)p.w2 + (unsigned)(cg * 8192 + n * 4096), l16, l16, l16, l16, lbase + L_W2 + n * 4096);
#pragma unroll 1
    for (int n = 0; n < 4; ++n) {
      const unsigned r0 = (unsigned)((4 * n) & 7) << 5;
      glds4((const unsigned char*)p.wo + (unsigned)((h * 512 + j * 16 + 4 * n) * 1024), l16 ^ r0, l16 ^ (r0 + 32u), l16 ^ (r0 + 64u), l16 ^ (r0 + 96u), lbase + L_WO + n * 4096);
    }
    WAIT_VMN(24);
    STAMP(4);
    RAWBAR();
    WAIT_VMN(16);
    if (lane == 0) __hip_atomic_store(flg + 4, 1u, __ATOMIC_RELAXED, __HIP_MEMORY_SCOPE_WORKGROUP);
    WAIT_VM(0);
    STAMP(5);
    RAWBAR();
  } else if (wave == 9) {
    const int lm = cg >> 3, kq = cg & 7;
    RAWBAR();
    const int zoff = W_Z + ((lm * 8 + kq) * 8 + ((lane & 31) >> 2)) * 128 + (lane & 3) * 16;
    for (unsigned spin = 0; spin < SPIN_MAX; ++spin) {
      const v4u f = *(volatile LAS v4u*)flg;
      asm volatile("" ::: "memory");
      if (__builtin_amdgcn_readfirstlane(f[1] & f[2] & f[3]) != 0u) break;
      __builtin_amdgcn_s_sleep(1);
    }
    DELAY(KN_G1_DELAY + 12);
    v4u zq;
    asm volatile("global_load_dwordx4 %0, %1, %2 sc1" : "=v"(zq) : "v"(zoff), "s"(p.ws) : "memory");
    RAWBAR();
    RAWBAR();
    RAWBAR();
    asm volatile("s_waitcnt vmcnt(0)" : "+v"(zq) :: "memory");
    if (act) {
      const v2u tg = *(const LAS v2u*)(L + L_TAG);
      const unsigned tlo = tg[0], thi = tg[1];
      if (!NONEBAD(TAGBAD(zq))) {
        for (unsigned spin = 0; spin < SPIN_MAX; ++spin) {
          asm volatile("" ::: "memory");
          zq = LD16(zoff);
          if (NONEBAD(TAGBAD(zq))) break;
        }
      }
      float z0 = __uint_as_float(zq[0]), z1 = __uint_as_float(zq[2]);
      z0 += dpp<ROR4>(z0); z1 += dpp<ROR4>(z1); z0 += dpp<XOR8>(z0); z1 += dpp<XOR8>(z1); z0 = x16_sum(z0); z1 = x16_sum(z1);
      const unsigned bw = *(const LAS unsigned*)(L + L_B1 + (lane & 3) * 4);
      float hv[2];
#pragma unroll
      for (int e = 0; e < 2; ++e) {
        const float z = rbf(rbf(e ? z1 : z0) + (e ? bfhi(bw) : bflo(bw)));
        const float uu = 0.7978845608028654f * (z + 0.044715f * z * z * z);
        const float th = 1.f - 2.f * __builtin_amdgcn_rcpf(1.f + __builtin_amdgcn_exp2f(2.885390081777927f * uu));
        hv[e] = 0.5f * z * (1.f + th);
      }
      const unsigned hp = pkbf(hv[0], hv[1]);
      const unsigned h0 = __builtin_amdgcn_readlane(hp, 0), h1 = __builtin_amdgcn_readlane(hp, 1), h2 = __builtin_amdgcn_readlane(hp, 2), h3 = __builtin_amdgcn_readlane(hp, 3);
      const int tsel = (c >> 1) & 3, prs = c & 1;
      v2u hm[4];
#pragma unroll
      for (int t = 0; t < 4; ++t) { const bool on = tsel == t && g < 2; hm[t][0] = on ? (g == 0 ? h0 : h2) : 0u; hm[t][1] = on ? (g == 0 ? h1 : h3) : 0u; }
      for (unsigned spin = 0; spin < SPIN_MAX && lds_peek(flg + 4) == 0u; ++spin) __builtin_amdgcn_s_sleep(1);
      LAS unsigned char* ab = L + L_W2 + ((4 * g + (c >> 2)) & 7) * 1024 + (c & 3) * 8;
      LAS unsigned char* mb = L + L_MP + (16 * tsel + 4 * g + 2 * prs) * 4;
#pragma unroll 2
      for (int T = 0; T < 8; ++T) {
        f32x4 d = {0.f, 0.f, 0.f, 0.f};
#pragma unroll
        for (int t = 0; t < 4; ++t) d = mfma16(trd(ab + (4 * T + t) * 32), hm[t], d);
        f32x2 o2; o2[0] = prs ? d[2] : d[0]; o2[1] = prs ? d[3] : d[1];
        if (c < 8) *(LAS f32x2*)(mb + T * 256) = o2;
      }
    }
    STAMP(6);
    BARRIER();
  } else {
    const int w = wave - 1;
    const int lc = j >> 2, rq = j & 3;
    const int lm = cg >> 3, kq = cg & 7;
    if (w == 0 || w == 4) {
      const v4u xr = *(const GAS v4u*)(p.x + lane * 8);
      v2u ev = {0u, 0u}; unsigned touch = 0u, small = 0u;
      if (w == 0) {
        ev = __builtin_amdgcn_raw_buffer_load_b64(rs, W_E, 0, 16);
        touch = __builtin_amdgcn_raw_buffer_load_b32(rs, W_TOUCH + (lane & 7) * 4, 0, 16);
        const unsigned long long a_b1 = (unsigned long long)(p.b1 + cg * 8), a_x = (unsigned long long)(p.x + col), a_b2 = (unsigned long long)(p.b2 + col);
        const unsigned long long a_sm = lane < 4 ? a_b1 + lane * 4 : (lane == 4 ? a_x : a_b2);
        small = *(const GAS unsigned*)a_sm;
      }
      __builtin_amdgcn_sched_barrier(0);
      RAWBAR();
      __builtin_amdgcn_sched_barrier(0);
      const v4u xn = ln_packed(xr);
      if (w == 0) {
        *(LAS v4u*)(L + L_XN + lane * 16) = xn;
        const unsigned long long E = (unsigned long long)(unsigned)__builtin_amdgcn_readfirstlane(ev[0]) | ((unsigned long long)(unsigned)__builtin_amdgcn_readfirstlane(ev[1]) << 32);
        const unsigned long long T = E * 6364136223846793005ull + 1442695040888963407ull;
        if (lane == 0) { v2u tg; tg[0] = (unsigned)T; tg[1] = (unsigned)(T >> 32); *(LAS v2u*)(L + L_TAG) = tg; }
        if (lane < 6) *(LAS unsigned*)(L + L_B1 + lane * 4) = small;
        asm volatile("s_waitcnt lgkmcnt(0)" ::: "memory");
        if (lane == 0) __hip_atomic_store(flg + 1, 1u, __ATOMIC_RELAXED, __HIP_MEMORY_SCOPE_WORKGROUP);
        asm volatile("" :: "v"(touch));
      } else {
        *(LAS v4u*)(L + L_Y + lane * 16) = ln_packed(xn);
        asm volatile("s_waitcnt lgkmcnt(0)" ::: "memory");
        if (lane == 0) __hip_atomic_store(flg + 3, 1u, __ATOMIC_RELAXED, __HIP_MEMORY_SCOPE_WORKGROUP);
      }
    } else {
      RAWBAR();
    }
    STAMP(1);
    for (unsigned spin = 0; spin < SPIN_MAX; ++spin) {
      const v4u f = *(volatile LAS v4u*)flg;
      asm volatile("" ::: "memory");
      const unsigned need = w < 4 ? (f[0] & f[1]) : (f[1] & f[2] & f[3]);
      if (__builtin_amdgcn_readfirstlane(need) != 0u) break;
      __builtin_amdgcn_s_sleep(1);
    }
    const v2u tg = *(const LAS v2u*)(L + L_TAG);
    const unsigned tlo = tg[0], thi = tg[1];
    {
      const int q4 = c >> 2, pp = c & 3, t = w & 3;
      const int ph = ((q4 >> 1) & 1) | ((g & 1) << 1);
      const int toff = (8 * g + q4) * 128 + (((2 * t + (pp >> 1)) ^ (ph << 1)) * 16) + (pp & 1) * 8;
      f32x4 a0 = {0.f, 0.f, 0.f, 0.f};
      int off;
      if (w < 4) {
#pragma unroll
        for (int s = 0; s < 4; ++s)
          a0 = mfma32(cat(trd(L + L_WQ + toff + s * 4096), trd(L + L_WQ + toff + s * 4096 + 512)), *(const LAS v4u*)(L + L_XN + (128 * rq + 32 * s + 8 * g) * 2), a0);
        off = W_Q + ((((h * 8 + lc) * 4 + rq) * 16) + 4 * t + g) * 16;
      } else {
#pragma unroll
        for (int s = 0; s < 2; ++s)
          a0 = mfma32(cat(trd(L + L_W1 + toff + s * 4096), trd(L + L_W1 + toff + s * 4096 + 512)), *(const LAS v4u*)(L + L_Y + (64 * kq + 32 * s + 8 * g) * 2), a0);
        off = W_Z + ((lm * 8 + 2 * t + (g >> 1)) * 8 + kq) * 128 + (2 * (g & 1) + c) * 16;
      }
      v4u gr;
      gr[0] = w < 4 ? pkbf(a0[0], a0[1]) : __float_as_uint((c & 1) ? a0[2] : a0[0]); gr[1] = tlo;
      gr[2] = w < 4 ? pkbf(a0[2], a0[3]) : __float_as_uint((c & 1) ? a0[3] : a0[1]); gr[3] = thi;
      if (w < 4 ? c == 0 : (c < 2 && act)) ST16(gr, off);
    }
    STAMP(11);
    const int head = c & 3, kk = c >> 2, q4 = c >> 2, pp = c & 3, tsel = (c >> 1) & 3, prs = c & 1;
    int qoff = W_Q + (h * 8 + w) * 1024 + lane * 16;
    int aQw; { const int dim = (w & 1) * 64 + 4 * (lane & 15); aQw = L_Q + (((dim >> 5) * 4 + ((dim >> 3) & 3)) * 4 + (w >> 1)) * 16 + (dim & 7) * 2; }
    int aK[4];
#pragma unroll
    for (int s = 0; s < 4; ++s) aK[s] = L_K + (w * 16 + c) * 256 + (((4 * s + g) ^ c) * 16);
    int aQr = L_Q + (g * 4 + head) * 16;
    int aMw = L_MX + ((lane & 3) * 8 + w) * 4;
    int aMr = L_MX + head * 32;
    int aPw = L_P + (((w >> 1) * 4 + 2 * (w & 1) + (g >> 1)) * 4 + head) * 16 + (g & 1) * 8;
    int aPr = L_P + (g * 4 + head) * 16;
    int aV[2];
#pragma unroll
    for (int t2 = 0; t2 < 2; ++t2) aV[t2] = L_V + (8 * g + 4 * t2 + q4) * 256 + (((2 * w + (pp >> 1)) ^ ((q4 << 2) | ((2 * g + t2) & 3))) * 16) + (pp & 1) * 8;
    int ooff = W_O + ((((h * 32 + head * 8 + w) * 32 + j) * 8) + 2 * g + kk) * 16;
    int offa = W_O + (((h * 32 + j) * 32 + 8 * (w & 3)) * 8 + lane) * 16;
    int aOw = L_OP + (w & 3) * 80 + lane * 8, aOr = L_OP + g * 16;
    int aWo[4];
#pragma unroll
    for (int t = 0; t < 4; ++t) { const int row = 4 * g + (c >> 2); aWo[t] = L_WO + row * 1024 + ((2 * (4 * w + t) + ((c & 3) >> 1)) ^ ((row & 7) << 1)) * 16 + (c & 1) * 8; }
    int aMp = L_MP + (16 * (4 * w + tsel) + 4 * g + 2 * prs) * 4;
    int soff = W_S + (((2 * (4 * w + tsel) + (g >> 1)) * 256 + b) * 4 + 2 * (g & 1) + prs) * 32 + (c >> 3) * 16;
    int foff; { const int cc = 8 * cc_cg + cc_i; foff = W_S + (((cc >> 2) * 256 + 32 * w + (lane >> 1)) * 4 + (cc & 3)) * 32 + (lane & 1) * 16; }
    unsigned tex = (lane & 1) ? thi : tlo, otag = (kk & 1) ? thi : tlo;
    unsigned tm[4];
#pragma unroll
    for (int t = 0; t < 4; ++t) tm[t] = tsel == t ? ~0u : 0u;
    PIN(qoff); PIN(aQw); PIN(aK[0]); PIN(aK[1]); PIN(aK[2]); PIN(aK[3]); PIN(aQr); PIN(aMw); PIN(aMr); PIN(aPw); PIN(aPr); PIN(aV[0]); PIN(aV[1]);
    PIN(ooff); PIN(offa); PIN(aOw); PIN(aOr); PIN(aWo[0]); PIN(aWo[1]); PIN(aWo[2]); PIN(aWo[3]); PIN(aMp); PIN(soff); PIN(foff); PIN(tex); PIN(otag);
    PIN(tm[0]); PIN(tm[1]); PIN(tm[2]); PIN(tm[3]);
    DELAY(KN_G1_DELAY);
    v4u q0;
    for (unsigned spin = 0; spin < SPIN_MAX; ++spin) {
      asm volatile("" ::: "memory");
      RETRY(0);
      q0 = LD16(qoff);
      if (NONEBAD(TAGBAD(q0))) break;
    }
    for (unsigned spin = 0; spin < SPIN_MAX && lds_peek(flg + 5) == 0u; ++spin) { }
    v4u kf[4];
#pragma unroll
    for (int s = 0; s < 4; ++s) kf[s] = *(const LAS v4u*)(L + aK[s]);
    {
      float u0 = bflo(q0[0]), u1 = bfhi(q0[0]), u2 = bflo(q0[2]), u3 = bfhi(q0[2]);
      u0 = x16_sum(u0); u1 = x16_sum(u1); u2 = x16_sum(u2); u3 = x16_sum(u3);
      u0 = x32_sum(u0); u1 = x32_sum(u1); u2 = x32_sum(u2); u3 = x32_sum(u3);
      v2u d; d[0] = pkbf(u0, u1); d[1] = pkbf(u2, u3);
      if (lane < 16) *(LAS v2u*)(L + aQw) = d;
    }
    STAMP(2);
    BARRIER();
    STAMP(3);
    f32x4 sc;
    {
      v4u qf[4];
#pragma unroll
      for (int s = 0; s < 4; ++s) qf[s] = *(const LAS v4u*)(L + aQr + s * 256);
      __builtin_amdgcn_sched_barrier(0);
      const f32x4 z4 = {0.f, 0.f, 0.f, 0.f};
      const f32x4 s0 = mfma32(kf[1], qf[1], mfma32(kf[0], qf[0], z4)), s1 = mfma32(kf[3], qf[3], mfma32(kf[2], qf[2], z4));
      sc = s0 + s1;
      int mx = imax(imax(fkey(sc[0]), fkey(sc[1])), imax(fkey(sc[2]), fkey(sc[3])));
      mx = x16_imax(mx); mx = x32_imax(mx);
      if (lane < 4) *(LAS int*)(L + aMw) = mx;
    }
    BARRIER();
    float mc;
    constexpr float C2 = 0.08838834764831845f * 1.4426950408889634f;
    {
      const v4u ma = *(const LAS v4u*)(L + aMr), mb = *(const LAS v4u*)(L + aMr + 16);
      const int M = imax(imax(imax((int)ma[0], (int)ma[1]), imax((int)ma[2], (int)ma[3])), imax(imax((int)mb[0], (int)mb[1]), imax((int)mb[2], (int)mb[3])));
      mc = funkey(M) * C2;
      float e[4];
#pragma unroll
      for (int r = 0; r < 4; ++r) e[r] = __builtin_amdgcn_exp2f(fmaf(sc[r], C2, -mc));
      v2u pk; pk[0] = pkbf(e[0], e[1]); pk[1] = pkbf(e[2], e[3]);
      if (c < 4) *(LAS v2u*)(L + aPw) = pk;
      float lsum = (bflo(pk[0]) + bfhi(pk[0])) + (bflo(pk[1]) + bfhi(pk[1]));
      lsum = x16_sum(lsum); lsum = x32_sum(lsum);
      if (lane < 4) *(LAS float*)(L + aMw + 128) = lsum;
    }
    STAMP(4);
    BARRIER();
    {
      v4u pf[4]; s16x4 va[4][2];
#pragma unroll
      for (int s = 0; s < 4; ++s) { va[s][0] = trd(L + aV[0] + s * 8192); va[s][1] = trd(L + aV[1] + s * 8192); pf[s] = *(const LAS v4u*)(L + aPr + s * 256); }
      const f32x4 la = *(const LAS f32x4*)(L + aMr + 128), lb = *(const LAS f32x4*)(L + aMr + 144);
      __builtin_amdgcn_sched_barrier(0);
      const f32x4 z4 = {0.f, 0.f, 0.f, 0.f};
      const f32x4 o0 = mfma32(cat(va[1][0], va[1][1]), pf[1], mfma32(cat(va[0][0], va[0][1]), pf[0], z4));
      const f32x4 o1 = mfma32(cat(va[3][0], va[3][1]), pf[3], mfma32(cat(va[2][0], va[2][1]), pf[2], z4));
      const float l8 = ((la[0] + la[1]) + (la[2] + la[3])) + ((lb[0] + lb[1]) + (lb[2] + lb[3]));
      const f32x4 oa = o0 + o1;
      v4u gr; gr[0] = __float_as_uint((kk & 1) ? oa[2] : oa[0]); gr[1] = __float_as_uint((kk & 1) ? oa[3] : oa[1]); gr[2] = __float_as_uint((kk & 1) ? l8 : mc); gr[3] = otag;
      if (kk < 2) ST16(gr, ooff);
    }
    STAMP(5);
    if (w < 4) {
      DELAY(KN_H2_DELAY);
      v4u ga;
      for (unsigned spin = 0; spin < SPIN_MAX; ++spin) {
        asm volatile("" ::: "memory");
        RETRY(1);
        ga = LD16(offa);
        if (NONEBAD(ga[3] ^ tex)) break;
      }
      STAMP(7);
      const float aux = __uint_as_float(ga[2]), axn = dpp<XOR1>(aux);
      const float mj = (lane & 1) ? axn : aux, lj = (lane & 1) ? aux : axn;
      int mk = fkey(mj);
      mk = imax(mk, dppi<XOR8>(mk)); mk = x16_imax(mk); mk = x32_imax(mk);
      const float Mw = funkey(mk);
      const float wt = __builtin_amdgcn_exp2f(mj - Mw);
      float o0 = __uint_as_float(ga[0]) * wt, o1 = __uint_as_float(ga[1]) * wt, ll = lj * wt;
      o0 += dpp<XOR8>(o0); o1 += dpp<XOR8>(o1); ll += dpp<XOR8>(ll);
      o0 = x16_sum(o0); o1 = x16_sum(o1); ll = x16_sum(ll);
      o0 = x32_sum(o0); o1 = x32_sum(o1); ll = x32_sum(ll);
      if (lane < 9) { f32x2 o2; o2[0] = lane < 8 ? o0 : Mw; o2[1] = lane < 8 ? o1 : ll; *(LAS f32x2*)(L + aOw) = o2; }
    }
    BARRIER();
    {
      f32x4 ou[4]; f32x2 ml[4]; s16x4 a[4];
#pragma unroll
      for (int u = 0; u < 4; ++u) { ml[u] = *(const LAS f32x2*)(L + L_OP + u * 80 + 64); ou[u] = *(const LAS f32x4*)(L + aOr + u * 80); }
#pragma unroll
      for (int t = 0; t < 4; ++t) a[t] = trd(L + aWo[t]);
      f32x2 mv = *(const LAS f32x2*)(L + aMp);
      __builtin_amdgcn_sched_barrier(0);
      if (!act) { mv[0] = 0.f; mv[1] = 0.f; }
      const float M = funkey(imax(imax(fkey(ml[0][0]), fkey(ml[1][0])), imax(fkey(ml[2][0]), fkey(ml[3][0]))));
      float f[4];
#pragma unroll
      for (int u = 0; u < 4; ++u) f[u] = __builtin_amdgcn_exp2f(ml[u][0] - M);
      const float li = __builtin_amdgcn_rcpf((f[0] * ml[0][1] + f[1] * ml[1][1]) + (f[2] * ml[2][1] + f[3] * ml[3][1]));
      const f32x4 o4 = ((ou[0] * f[0] + ou[1] * f[1]) + (ou[2] * f[2] + ou[3] * f[3])) * li;
      v2u of; of[0] = pkbf(o4[0], o4[1]); of[1] = pkbf(o4[2], o4[3]);
      const f32x4 z4 = {0.f, 0.f, 0.f, 0.f};
      v2u om[4];
#pragma unroll
      for (int t = 0; t < 4; ++t) { om[t][0] = of[0] & tm[t]; om[t][1] = of[1] & tm[t]; }
      const f32x4 d = mfma16(a[1], om[1], mfma16(a[0], om[0], z4)) + mfma16(a[3], om[3], mfma16(a[2], om[2], z4));
      v4u gr; gr[0] = __float_as_uint(c < 8 ? (prs ? d[2] : d[0]) : mv[0]); gr[1] = tlo; gr[2] = __float_as_uint(c < 8 ? (prs ? d[3] : d[1]) : mv[1]); gr[3] = thi;
      ST16(gr, soff);
    }
    STAMP(8);
    {
      DELAY(KN_H3_DELAY);
      v4u gq;
      for (unsigned spin = 0; spin < SPIN_MAX; ++spin) {
        asm volatile("" ::: "memory");
        RETRY(2);
        gq = LD16(foff);
        if (NONEBAD(TAGBAD(gq))) break;
      }
      STAMP(9);
      float a0 = __uint_as_float(gq[0]), a1 = __uint_as_float(gq[2]);
      a0 += dpp<XOR2>(a0); a1 += dpp<XOR2>(a1); a0 += dpp<ROR4>(a0); a1 += dpp<ROR4>(a1); a0 += dpp<XOR8>(a0); a1 += dpp<XOR8>(a1);
      a0 = x16_sum(a0); a1 = x16_sum(a1); a0 = x32_sum(a0); a1 = x32_sum(a1);
      if (lane < 2) { f32x2 o2; o2[0] = a0; o2[1] = a1; *(LAS f32x2*)(L + L_FS + lane * 64 + w * 8) = o2; }
    }
    BARRIER();
    if (w == 0) {
      const int e = (lane >> 3) & 1, u = lane & 7;
      const unsigned xw = *(const LAS unsigned*)(L + L_XB), b2w = *(const LAS unsigned*)(L + L_XB + 4);
      float s = *(const LAS float*)(L + L_FS + u * 8 + e * 4), m = *(const LAS float*)(L + L_FM + u * 8 + e * 4);
      s += dpp<XOR1>(s); m += dpp<XOR1>(m); s += dpp<XOR2>(s); m += dpp<XOR2>(m); s += dpp<XOR7>(s); m += dpp<XOR7>(m);
      const float z = rbf(rbf(m) + (e ? bfhi(b2w) : bflo(b2w)));
      const float gt = rbf(z * __builtin_amdgcn_rcpf(1.f + __builtin_amdgcn_exp2f(-1.4426950408889634f * z)));
      const float r = (e ? bfhi(xw) : bflo(xw)) + rbf(rbf(s) * gt);
      const float rn = dpp<XOR8>(r);
      if (lane == 0) {
        *(GAS unsigned*)(p.out + col) = pkbf(r, rn);
        if (b == 0) { const unsigned long long T = (unsigned long long)tlo | ((unsigned long long)thi << 32); __hip_atomic_store((GAS unsigned long long*)(p.ws + W_E), T, __ATOMIC_RELAXED, __HIP_MEMORY_SCOPE_AGENT); }
      }
    }
    STAMP(10);
  }
}

extern "C" void kernel_launch(void* const* d_in, const int* in_sizes, int n_in,
                              void* d_out, int out_size, void* d_ws, size_t ws_size,
                              hipStream_t stream) {
  Params p;
  p.x = (const u16*)d_in[0]; p.kc = (const u16*)d_in[1]; p.vc = (const u16*)d_in[2]; p.wq = (const u16*)d_in[3]; p.wo = (const u16*)d_in[4];
  p.w1 = (const u16*)d_in[6]; p.b1 = (const u16*)d_in[7]; p.w2 = (const u16*)d_in[8]; p.b2 = (const u16*)d_in[9];
  p.out = (u16*)d_out; p.ws = (unsigned char*)d_ws;
  fused_kernel<<<NB, NTHR, 0, stream>>>(p);
}
```

```cpp
#include <hip/hip_runtime.h>
#include <hip/hip_bf16.h>

#define LAS __attribute__((address_space(3)))
#define GAS __attribute__((address_space(1)))
typedef unsigned short u16;
typedef unsigned v4u __attribute__((ext_vector_type(4)));
typedef unsigned v2u __attribute__((ext_vector_type(2)));
typedef float f32x4 __attribute__((ext_vector_type(4)));
typedef float f32x2 __attribute__((ext_vector_type(2)));
typedef short s16x4 __attribute__((ext_vector_type(4)));
typedef short s16x8 __attribute__((ext_vector_type(8)));
typedef __bf16 bf16x8 __attribute__((ext_vector_type(8)));
typedef __bf16 bf16x2 __attribute__((ext_vector_type(2)));

constexpr int DLY_Q = 24, DLY_O = 24, DLY_S = 30;
constexpr int LWIN = 24;

constexpr int D = 512, SK = 65536, WIN = 4096, FF = 1792;
constexpr int NB = 256, NTHR = 640;
constexpr int L_WQ = 0, L_K = 16384, L_V = 49152, L_WO = 81920, L_W2 = 98304, L_W1 = 106496, L_XN = 114688, L_Y = 115712, L_Q = 116736, L_P = 117760;
constexpr int L_MX = 118784, L_SM = L_MX + 128, L_TAG = L_SM + 128, L_OP = L_TAG + 16, L_FS = L_OP + 512, L_FM = L_FS + 64, L_B1 = L_FM + 64, L_XB = L_B1 + 16, L_FLG = L_XB + 16, L_MP = L_FLG + 32, L_END = L_MP + 2048;
constexpr int W_E = 0;
constexpr int W_Q = 4096;
constexpr int W_Z = W_Q + 131072;
constexpr int W_O = W_Z + 262144;
constexpr int W_S = W_O + 1048576;
constexpr int W_TOUCH = W_S + 2097152;
constexpr int W_END = W_TOUCH + 4096;
constexpr unsigned SPIN_MAX = 1u << 17;

__device__ __forceinline__ float bflo(unsigned w) { return __uint_as_float(w << 16); }
__device__ __forceinline__ float bfhi(unsigned w) { return __uint_as_float(w & 0xffff0000u); }
__device__ __forceinline__ unsigned pkbf(float lo, float hi) { bf16x2 r; r[0] = (__bf16)lo; r[1] = (__bf16)hi; return __builtin_bit_cast(unsigned, r); }
__device__ __forceinline__ float rbf(float f) { return bflo(pkbf(f, f)); }
template <int CTRL> __device__ __forceinline__ float dpp(float x) {
  return __builtin_bit_cast(float, __builtin_amdgcn_mov_dpp(__builtin_bit_cast(int, x), CTRL, 0xf, 0xf, true));
}
constexpr int XOR1 = 0xB1, XOR2 = 0x4E, XOR7 = 0x141, XOR15 = 0x140, XOR8 = 0x128, ROR4 = 0x124;
__device__ __forceinline__ int fkey(float f) { const int i = __float_as_int(f); return i ^ ((i >> 31) & 0x7fffffff); }
__device__ __forceinline__ float funkey(int k) { return __int_as_float(k ^ ((k >> 31) & 0x7fffffff)); }
template <int CTRL> __device__ __forceinline__ int dppi(int x) { return __builtin_amdgcn_mov_dpp(x, CTRL, 0xf, 0xf, true); }
__device__ __forceinline__ int imax(int a, int b) { return a > b ? a : b; }
__device__ __forceinline__ int x16_imax(int x) { auto s = __builtin_amdgcn_permlane16_swap((unsigned)x, (unsigned)x, false, false); return imax((int)s[0], (int)s[1]); }
__device__ __forceinline__ int x32_imax(int x) { auto s = __builtin_amdgcn_permlane32_swap((unsigned)x, (unsigned)x, false, false); return imax((int)s[0], (int)s[1]); }
__device__ __forceinline__ float x16_sum(float x) { auto s = __builtin_amdgcn_permlane16_swap(__float_as_uint(x), __float_as_uint(x), false, false); return __uint_as_float(s[0]) + __uint_as_float(s[1]); }
__device__ __forceinline__ float x32_sum(float x) { auto s = __builtin_amdgcn_permlane32_swap(__float_as_uint(x), __float_as_uint(x), false, false); return __uint_as_float(s[0]) + __uint_as_float(s[1]); }
__device__ __forceinline__ void wave_sum2(float& a, float& b) {
  a += dpp<XOR1>(a); b += dpp<XOR1>(b); a += dpp<XOR2>(a); b += dpp<XOR2>(b); a += dpp<XOR7>(a); b += dpp<XOR7>(b); a += dpp<XOR15>(a); b += dpp<XOR15>(b);
  a = x16_sum(a); b = x16_sum(b); a = x32_sum(a); b = x32_sum(b);
}
__device__ __forceinline__ f32x4 mfma32(v4u a, v4u b, f32x4 c) {
  return __builtin_amdgcn_mfma_f32_16x16x32_bf16(__builtin_bit_cast(bf16x8, a), __builtin_bit_cast(bf16x8, b), c, 0, 0, 0);
}
__device__ __forceinline__ f32x4 mfma16(s16x4 a, v2u b, f32x4 c) {
  return __builtin_amdgcn_mfma_f32_16x16x16bf16_1k(a, __builtin_bit_cast(s16x4, b), c, 0, 0, 0);
}
__device__ __forceinline__ s16x4 trd(LAS unsigned char* p) { return __builtin_amdgcn_ds_read_tr16_b64_v4i16((LAS s16x4*)p); }
__device__ __forceinline__ v4u cat(s16x4 lo, s16x4 hi) { return __builtin_bit_cast(v4u, __builtin_shufflevector(lo, hi, 0, 1, 2, 3, 4, 5, 6, 7)); }
__device__ __forceinline__ v4u ln_packed(v4u x) {
  float v[8];
#pragma unroll
  for (int i = 0; i < 4; ++i) { v[2 * i] = bflo(x[i]); v[2 * i + 1] = bfhi(x[i]); }
  const float piv = __uint_as_float(__builtin_amdgcn_readfirstlane(x[0]) << 16);
  float s = 0.f, q = 0.f;
#pragma unroll
  for (int i = 0; i < 8; ++i) { const float d = v[i] - piv; s += d; q = fmaf(d, d, q); }
  wave_sum2(s, q);
  const float md = s * (1.0f / D);
  const float var = rbf(fmaxf(fmaf(-md, md, q * (1.0f / D)), 0.f));
  const float m = rbf(piv + md);
  const float sd = rbf(__builtin_amdgcn_sqrtf(rbf(var + 1e-5f)));
  const float inv = __builtin_amdgcn_rcpf(sd);
  v4u r;
#pragma unroll
  for (int i = 0; i < 4; ++i) r[i] = pkbf(rbf(v[2 * i] - m) * inv, rbf(v[2 * i + 1] - m) * inv);
  return r;
}
#define WAIT_VM(n) asm volatile("s_waitcnt vmcnt(" #n ")" ::: "memory")
#define WAIT_VMN(n) asm volatile("s_waitcnt vmcnt(%0)" :: "i"(n) : "memory")
static_assert(LWIN % 4 == 0 && LWIN >= 8 && LWIN <= 36, "loader window");
#define BARRIER() asm volatile("s_waitcnt lgkmcnt(0)\n\ts_barrier" ::: "memory")
#define RAWBAR() asm volatile("s_barrier" ::: "memory")
__device__ __forceinline__ void glds4(const void* sbase, unsigned v0, unsigned v1, unsigned v2, unsigned v3, unsigned ldsaddr) {
  unsigned keep;
  asm volatile("s_mov_b32 %0, m0\n\ts_mov_b32 m0, %6\n\ts_nop 0\n\tglobal_load_lds_dwordx4 %1, %5 nt\n\tglobal_load_lds_dwordx4 %2, %5 offset:1024 nt\n\t"
               "global_load_lds_dwordx4 %3, %5 offset:2048 nt\n\tglobal_load_lds_dwordx4 %4, %5 offset:3072 nt\n\ts_mov_b32 m0, %0"
               : "=&s"(keep) : "v"(v0), "v"(v1), "v"(v2), "v"(v3), "s"(sbase), "s"(ldsaddr) : "memory");
}
#define DELAY(n) do { if ((n) > 0) __builtin_amdgcn_s_sleep((n) > 127 ? 127 : (n)); } while (0)
#define LD16(off) __builtin_amdgcn_raw_buffer_load_b128(rs, (off), 0, 16)
#define ST16(v, off) __builtin_amdgcn_raw_buffer_store_b128((v), rs, (off), 0, 16)
#define TAGBAD(x) (((x)[1] ^ tlo) | ((x)[3] ^ thi))
#define NONEBAD(v) (__builtin_amdgcn_ballot_w64((v) != 0u) == 0ull)
#define PIN(x) asm volatile("" : "+v"(x))
__device__ __forceinline__ void lds_add(LAS unsigned* q, unsigned v) { __hip_atomic_fetch_add(q, v, __ATOMIC_RELAXED, __HIP_MEMORY_SCOPE_WORKGROUP); }
__device__ __forceinline__ unsigned lds_peek(LAS unsigned* q) { return __builtin_amdgcn_readfirstlane(__hip_atomic_load(q, __ATOMIC_RELAXED, __HIP_MEMORY_SCOPE_WORKGROUP)); }

constexpr int first_group(int need) { for (int g = 6; g < 22; ++g) if (4 * g - (LWIN - 4) >= need) return g; return 99; }

struct Params {
  const u16* x; const u16* kc; const u16* vc; const u16* wq; const u16* wo;
  const u16* w1; const u16* b1; const u16* w2; const u16* b2; u16* out; unsigned char* ws;
};

__global__ void __launch_bounds__(NTHR, 3) fused_kernel(Params p) {
  __shared__ __attribute__((aligned(16))) unsigned char lds[L_END];
  LAS unsigned char* L = (LAS unsigned char*)lds;
  const unsigned lbase = (unsigned)(size_t)L;
  const int tid = threadIdx.x, lane = tid & 63, wave = __builtin_amdgcn_readfirstlane(tid >> 6);
  const int b = blockIdx.x, h = b & 7, j = b >> 3;
  const int g = lane >> 4, c = lane & 15;
  asm volatile("" :: "s"(p.x), "s"(p.kc), "s"(p.vc), "s"(p.wq), "s"(p.wo), "s"(p.w1), "s"(p.b1), "s"(p.w2), "s"(p.b2), "s"(p.out), "s"(p.ws));
  const __amdgpu_buffer_rsrc_t rs = __builtin_amdgcn_make_buffer_rsrc((void*)p.ws, 0, W_END, 0x00020000);
  const bool act = j < 28;
  const int cg = h * 28 + (act ? j : j - 28);
  const int cc_cg = h * 4 + (j >> 3), cc_i = j & 7;
  const int col = 16 * cc_cg + 2 * cc_i;

  LAS unsigned* flg = (LAS unsigned*)(L + L_FLG);
  if (tid < 2) { v4u z = {0u, 0u, 0u, 0u}; *(LAS v4u*)(L + L_FLG + tid * 16) = z; }
  if (wave == 0) {
    const int lc = j >> 2, rq = j & 3, lm = cg >> 3, kq = cg & 7;
    __builtin_amdgcn_s_setprio(3);
    const unsigned kvoff = ((unsigned)h * SK + (SK - WIN) + (unsigned)j * 128u) * 256u;
    const unsigned char* kb = (const unsigned char*)p.kc + kvoff;
    const unsigned char* vb = (const unsigned char*)p.vc + kvoff;
    const unsigned k0 = (unsigned)g * 256u + (unsigned)((c ^ g) * 16), v0 = (unsigned)g * 256u + (unsigned)((c ^ (g << 2)) * 16), l16 = (unsigned)lane * 16u;
    BARRIER();
    {
      const unsigned ph = (unsigned)(lane >> 4) & 1u, c8 = (unsigned)lane & 7u;
      const unsigned ce = (c8 ^ (ph << 1)) * 16u, co = (c8 ^ ((ph | 2u) << 1)) * 16u;
      const unsigned rq8 = (unsigned)(lane >> 3) * 8192u, r18 = (unsigned)(lane >> 3) * (unsigned)(FF * 2);
      const unsigned char* sq = (const unsigned char*)p.wq + (unsigned)((h * 512 + lc * 64) * 2 + rq * 128 * 8192);
      const unsigned char* s1 = (const unsigned char*)p.w1 + (unsigned)(lm * 128 + kq * 64 * (FF * 2));
      constexpr unsigned SQ = 8u * 8192u - 1024u, S1 = 8u * (FF * 2) - 1024u;
#pragma unroll 1
      for (int n = 0; n < 4; ++n) glds4(sq + (unsigned)(n * 32 * 8192), rq8 + ce, rq8 + co + SQ, rq8 + ce + 2u * SQ, rq8 + co + 3u * SQ, lbase + L_WQ + n * 4096);
#pragma unroll 1
      for (int n = 0; n < 2; ++n) glds4(s1 + (unsigned)(n * 32 * (FF * 2)), r18 + ce, r18 + co + S1, r18 + ce + 2u * S1, r18 + co + 3u * S1, lbase + L_W1 + n * 4096);
    }
    constexpr int GFQ = first_group(16), GF1 = first_group(24), GFK = first_group(56);
    static_assert(GFQ <= 21 && GF1 <= 21 && GFK <= 21, "flags are set inside the K and V loops");
#define GROUP_FLAGS(gi) do { \
      if ((gi) == GFQ && lane == 0) __hip_atomic_store(flg + 0, 1u, __ATOMIC_RELAXED, __HIP_MEMORY_SCOPE_WORKGROUP); \
      if ((gi) == GF1 && lane == 0) __hip_atomic_store(flg + 2, 1u, __ATOMIC_RELAXED, __HIP_MEMORY_SCOPE_WORKGROUP); \
      if ((gi) == GFK && lane == 0) __hip_atomic_store(flg + 5, 1u, __ATOMIC_RELAXED, __HIP_MEMORY_SCOPE_WORKGROUP); } while (0)
#pragma unroll 1
    for (int n = 0; n < 8; ++n) { WAIT_VMN(LWIN - 4); GROUP_FLAGS(6 + n); glds4(kb + n * 4096, k0, k0 ^ 64u, k0 ^ 128u, k0 ^ 192u, lbase + L_K + n * 4096); }
#pragma unroll 1
    for (int n = 0; n < 8; ++n) { WAIT_VMN(LWIN - 4); GROUP_FLAGS(14 + n); glds4(vb + n * 4096, v0, v0 ^ 16u, v0 ^ 32u, v0 ^ 48u, lbase + L_V + n * 4096); }
    WAIT_VMN(32);
    RAWBAR();
    RAWBAR();
#pragma unroll 1
    for (int n = 0; n < 2; ++n) glds4((const unsigned char*)p.w2 + (unsigned)(cg * 8192 + n * 4096), l16, l16, l16, l16, lbase + L_W2 + n * 4096);
#pragma unroll 1
    for (int n = 0; n < 4; ++n) {
      const unsigned r0 = (unsigned)((4 * n) & 7) << 5;
      glds4((const unsigned char*)p.wo + (unsigned)((h * 512 + j * 16 + 4 * n) * 1024), l16 ^ r0, l16 ^ (r0 + 32u), l16 ^ (r0 + 64u), l16 ^ (r0 + 96u), lbase + L_WO + n * 4096);
    }
    WAIT_VMN(24);
    RAWBAR();
    WAIT_VMN(16);
    if (lane == 0) __hip_atomic_store(flg + 4, 1u, __ATOMIC_RELAXED, __HIP_MEMORY_SCOPE_WORKGROUP);
    WAIT_VM(0);
    RAWBAR();
  } else if (wave == 9) {
    const int lm = cg >> 3, kq = cg & 7;
    const int tsel = (c >> 1) & 3, prs = c & 1;
    {
      const v4u xr = *(const GAS v4u*)(p.x + lane * 8);
      const unsigned touch = __builtin_amdgcn_raw_buffer_load_b32(rs, W_TOUCH + (lane & 7) * 4, 0, 16);
      const unsigned long long a_b1 = (unsigned long long)(p.b1 + cg * 8), a_x = (unsigned long long)(p.x + col), a_b2 = (unsigned long long)(p.b2 + col);
      const unsigned long long a_sm = lane < 4 ? a_b1 + lane * 4 : (lane == 4 ? a_x : a_b2);
      const unsigned small = *(const GAS unsigned*)a_sm;
      __builtin_amdgcn_sched_barrier(0);
      RAWBAR();
      __builtin_amdgcn_sched_barrier(0);
      *(LAS v4u*)(L + L_Y + lane * 16) = ln_packed(ln_packed(xr));
      if (lane < 6) *(LAS unsigned*)(L + L_B1 + lane * 4) = small;
      asm volatile("" :: "v"(touch));
    }
    const int zoff = W_Z + ((lm * 8 + kq) * 8 + ((lane & 31) >> 2)) * 128 + (lane & 3) * 16;
    {
      const int q4 = c >> 2, pp = c & 3;
      const int ph = ((q4 >> 1) & 1) | ((g & 1) << 1);
      v4u yf[2];
#pragma unroll
      for (int s = 0; s < 2; ++s) yf[s] = *(const LAS v4u*)(L + L_Y + (64 * kq + 32 * s + 8 * g) * 2);
      for (unsigned spin = 0; spin < SPIN_MAX; ++spin) {
        const v4u f = *(volatile LAS v4u*)flg;
        asm volatile("" ::: "memory");
        if (__builtin_amdgcn_readfirstlane(f[1] & f[2]) != 0u) break;
        __builtin_amdgcn_s_sleep(1);
      }
      const v2u tg = *(const LAS v2u*)(L + L_TAG);
      f32x4 a0 = {0.f, 0.f, 0.f, 0.f};
#pragma unroll
      for (int t = 0; t < 4; ++t) {
        LAS unsigned char* ab = L + L_W1 + (8 * g + q4) * 128 + (((2 * t + (pp >> 1)) ^ (ph << 1)) * 16) + (pp & 1) * 8;
        const unsigned m = tsel == t ? ~0u : 0u;
#pragma unroll
        for (int s = 0; s < 2; ++s) { v4u ym; ym[0] = yf[s][0] & m; ym[1] = yf[s][1] & m; ym[2] = yf[s][2] & m; ym[3] = yf[s][3] & m; a0 = mfma32(cat(trd(ab + s * 4096), trd(ab + s * 4096 + 512)), ym, a0); }
      }
      v4u gr; gr[0] = __float_as_uint(prs ? a0[2] : a0[0]); gr[1] = tg[0]; gr[2] = __float_as_uint(prs ? a0[3] : a0[1]); gr[3] = tg[1];
      if (c < 8 && act) ST16(gr, W_Z + ((lm * 8 + 2 * tsel + (g >> 1)) * 8 + kq) * 128 + (2 * (g & 1) + prs) * 16);
    }
    DELAY(DLY_Q + 8);
    v4u zq;
    asm volatile("global_load_dwordx4 %0, %1, %2 sc1" : "=v"(zq) : "v"(zoff), "s"(p.ws) : "memory");
    RAWBAR();
    RAWBAR();
    asm volatile("s_waitcnt vmcnt(0)" : "+v"(zq) :: "memory");
    v2u hm[4];
    if (act) {
      const v2u tg = *(const LAS v2u*)(L + L_TAG);
      const unsigned tlo = tg[0], thi = tg[1];
      if (!NONEBAD(TAGBAD(zq))) {
        for (unsigned spin = 0; spin < SPIN_MAX; ++spin) {
          asm volatile("" ::: "memory");
          zq = LD16(zoff);
          if (NONEBAD(TAGBAD(zq))) break;
        }
      }
      float z0 = __uint_as_float(zq[0]), z1 = __uint_as_float(zq[2]);
      z0 += dpp<ROR4>(z0); z1 += dpp<ROR4>(z1); z0 += dpp<XOR8>(z0); z1 += dpp<XOR8>(z1); z0 = x16_sum(z0); z1 = x16_sum(z1);
      const unsigned bw = *(const LAS unsigned*)(L + L_B1 + (lane & 3) * 4);
      float hv[2];
#pragma unroll
      for (int e = 0; e < 2; ++e) {
        const float z = rbf(rbf(e ? z1 : z0) + (e ? bfhi(bw) : bflo(bw)));
        const float uu = 0.7978845608028654f * (z + 0.044715f * z * z * z);
        const float th = 1.f - 2.f * __builtin_amdgcn_rcpf(1.f + __builtin_amdgcn_exp2f(2.885390081777927f * uu));
        hv[e] = 0.5f * z * (1.f + th);
      }
      const unsigned hp = pkbf(hv[0], hv[1]);
      const unsigned h0 = __builtin_amdgcn_readlane(hp, 0), h1 = __builtin_amdgcn_readlane(hp, 1), h2 = __builtin_amdgcn_readlane(hp, 2), h3 = __builtin_amdgcn_readlane(hp, 3);
#pragma unroll
      for (int t = 0; t < 4; ++t) { const bool on = tsel == t && g < 2; hm[t][0] = on ? (g == 0 ? h0 : h2) : 0u; hm[t][1] = on ? (g == 0 ? h1 : h3) : 0u; }
    }
    RAWBAR();
    if (act) {
      for (unsigned spin = 0; spin < SPIN_MAX && lds_peek(flg + 4) == 0u; ++spin) __builtin_amdgcn_s_sleep(1);
      LAS unsigned char* ab = L + L_W2 + ((4 * g + (c >> 2)) & 7) * 1024 + (c & 3) * 8;
      LAS unsigned char* mb = L + L_MP + (16 * tsel + 4 * g + 2 * prs) * 4;
#pragma unroll 2
      for (int T = 0; T < 8; ++T) {
        f32x4 d = {0.f, 0.f, 0.f, 0.f};
#pragma unroll
        for (int t = 0; t < 4; ++t) d = mfma16(trd(ab + (4 * T + t) * 32), hm[t], d);
        f32x2 o2; o2[0] = prs ? d[2] : d[0]; o2[1] = prs ? d[3] : d[1];
        if (c < 8) *(LAS f32x2*)(mb + T * 256) = o2;
      }
    }
    BARRIER();
  } else {
    const int w = wave - 1;
    __builtin_amdgcn_s_setprio(2);
    const int lc = j >> 2, rq = j & 3;
    if (w == 0) {
      const v4u xr = *(const GAS v4u*)(p.x + lane * 8);
      const v2u ev = __builtin_amdgcn_raw_buffer_load_b64(rs, W_E, 0, 16);
      __builtin_amdgcn_sched_barrier(0);
      RAWBAR();
      __builtin_amdgcn_sched_barrier(0);
      *(LAS v4u*)(L + L_XN + lane * 16) = ln_packed(xr);
      const unsigned long long E = (unsigned long long)(unsigned)__builtin_amdgcn_readfirstlane(ev[0]) | ((unsigned long long)(unsigned)__builtin_amdgcn_readfirstlane(ev[1]) << 32);
      const unsigned long long T = E * 6364136223846793005ull + 1442695040888963407ull;
      if (lane == 0) { v2u tg; tg[0] = (unsigned)T; tg[1] = (unsigned)(T >> 32); *(LAS v2u*)(L + L_TAG) = tg; }
      asm volatile("s_waitcnt lgkmcnt(0)" ::: "memory");
      if (lane == 0) __hip_atomic_store(flg + 1, 1u, __ATOMIC_RELAXED, __HIP_MEMORY_SCOPE_WORKGROUP);
    } else {
      RAWBAR();
    }
    unsigned tlo, thi;
    {
      const int q4 = c >> 2, pp = c & 3, t = w & 3;
      int toff = L_WQ + (8 * g + q4) * 128 + (((2 * t + (pp >> 1)) ^ ((((q4 >> 1) & 1) | ((g & 1) << 1)) << 1)) * 16) + (pp & 1) * 8;
      int xoff = L_XN + (128 * rq + 8 * g) * 2;
      int off = W_Q + ((((h * 8 + lc) * 4 + rq) * 16) + 4 * t + g) * 16;
      PIN(toff); PIN(xoff); PIN(off);
      for (unsigned spin = 0; spin < SPIN_MAX && lds_peek(flg + 1) == 0u; ++spin) __builtin_amdgcn_s_sleep(1);
      const v2u tg = *(const LAS v2u*)(L + L_TAG);
      tlo = tg[0]; thi = tg[1];
      if (w < 4) {
        v4u xf[4];
#pragma unroll
        for (int s = 0; s < 4; ++s) xf[s] = *(const LAS v4u*)(L + xoff + s * 64);
        for (unsigned spin = 0; spin < SPIN_MAX && lds_peek(flg + 0) == 0u; ++spin) { }
        s16x4 a[4][2];
#pragma unroll
        for (int s = 0; s < 4; ++s) { a[s][0] = trd(L + toff + s * 4096); a[s][1] = trd(L + toff + s * 4096 + 512); }
        __builtin_amdgcn_sched_barrier(0);
        const f32x4 z4 = {0.f, 0.f, 0.f, 0.f};
        const f32x4 a0 = mfma32(cat(a[1][0], a[1][1]), xf[1], mfma32(cat(a[0][0], a[0][1]), xf[0], z4)) + mfma32(cat(a[3][0], a[3][1]), xf[3], mfma32(cat(a[2][0], a[2][1]), xf[2], z4));
        v4u gr; gr[0] = pkbf(a0[0], a0[1]); gr[1] = tlo; gr[2] = pkbf(a0[2], a0[3]); gr[3] = thi;
        if (c == 0) ST16(gr, off);
        if (lane == 0) lds_add(flg + 3, 1u);
      } else {
        for (unsigned spin = 0; spin < SPIN_MAX && lds_peek(flg + 3) < 4u; ++spin) __builtin_amdgcn_s_sleep(1);
      }
    }
    const int head = c & 3, kk = c >> 2, q4 = c >> 2, pp = c & 3, tsel = (c >> 1) & 3, prs = c & 1;
    int qoff = W_Q + (h * 8 + w) * 1024 + lane * 16;
    int aQw; { const int dim = (w & 1) * 64 + 4 * (lane & 15); aQw = L_Q + (((dim >> 5) * 4 + ((dim >> 3) & 3)) * 4 + (w >> 1)) * 16 + (dim & 7) * 2; }
    int aK[4];
#pragma unroll
    for (int s = 0; s < 4; ++s) aK[s] = L_K + (w * 16 + c) * 256 + (((4 * s + g) ^ c) * 16);
    int aQr = L_Q + (g * 4 + head) * 16;
    int aMw = L_MX + ((lane & 3) * 8 + w) * 4;
    int aMr = L_MX + head * 32;
    int aPw = L_P + (((w >> 1) * 4 + 2 * (w & 1) + (g >> 1)) * 4 + head) * 16 + (g & 1) * 8;
    int aPr = L_P + (g * 4 + head) * 16;
    int aV[2];
#pragma unroll
    for (int t2 = 0; t2 < 2; ++t2) aV[t2] = L_V + (8 * g + 4 * t2 + q4) * 256 + (((2 * w + (pp >> 1)) ^ ((q4 << 2) | ((2 * g + t2) & 3))) * 16) + (pp & 1) * 8;
    int ooff = W_O + ((((h * 32 + head * 8 + w) * 32 + j) * 8) + 2 * g + kk) * 16;
    int offa = W_O + (((h * 32 + j) * 32 + 8 * (w & 3)) * 8 + lane) * 16;
    int aOw = L_OP + (w & 3) * 80 + lane * 8, aOr = L_OP + g * 16;
    int aWo[4];
#pragma unroll
    for (int t = 0; t < 4; ++t) { const int row = 4 * g + (c >> 2); aWo[t] = L_WO + row * 1024 + ((2 * (4 * w + t) + ((c & 3) >> 1)) ^ ((row & 7) << 1)) * 16 + (c & 1) * 8; }
    int aMp = L_MP + (16 * (4 * w + tsel) + 4 * g + 2 * prs) * 4;
    int soff = W_S + (((2 * (4 * w + tsel) + (g >> 1)) * 256 + b) * 4 + 2 * (g & 1) + prs) * 32 + (c >> 3) * 16;
    int foff; { const int cc = 8 * cc_cg + cc_i; foff = W_S + (((cc >> 2) * 256 + 32 * w + (lane >> 1)) * 4 + (cc & 3)) * 32 + (lane & 1) * 16; }
    unsigned tex = (lane & 1) ? thi : tlo, otag = (kk & 1) ? thi : tlo;
    unsigned tm[4];
#pragma unroll
    for (int t = 0; t < 4; ++t) tm[t] = tsel == t ? ~0u : 0u;
    PIN(qoff); PIN(aQw); PIN(aK[0]); PIN(aK[1]); PIN(aK[2]); PIN(aK[3]); PIN(aQr); PIN(aMw); PIN(aMr); PIN(aPw); PIN(aPr); PIN(aV[0]); PIN(aV[1]);
    PIN(ooff); PIN(offa); PIN(aOw); PIN(aOr); PIN(aWo[0]); PIN(aWo[1]); PIN(aWo[2]); PIN(aWo[3]); PIN(aMp); PIN(soff); PIN(foff); PIN(tex); PIN(otag);
    PIN(tm[0]); PIN(tm[1]); PIN(tm[2]); PIN(tm[3]);
    DELAY(DLY_Q);
    v4u q0;
    for (unsigned spin = 0; spin < SPIN_MAX; ++spin) {
      asm volatile("" ::: "memory");
      q0 = LD16(qoff);
      if (NONEBAD(TAGBAD(q0))) break;
    }
    for (unsigned spin = 0; spin < SPIN_MAX && lds_peek(flg + 5) == 0u; ++spin) { }
    v4u kf[4];
#pragma unroll
    for (int s = 0; s < 4; ++s) kf[s] = *(const LAS v4u*)(L + aK[s]);
    {
      float u0 = bflo(q0[0]), u1 = bfhi(q0[0]), u2 = bflo(q0[2]), u3 = bfhi(q0[2]);
      u0 = x16_sum(u0); u1 = x16_sum(u1); u2 = x16_sum(u2); u3 = x16_sum(u3);
      u0 = x32_sum(u0); u1 = x32_sum(u1); u2 = x32_sum(u2); u3 = x32_sum(u3);
      v2u d; d[0] = pkbf(u0, u1); d[1] = pkbf(u2, u3);
      if (lane < 16) *(LAS v2u*)(L + aQw) = d;
    }
    BARRIER();
    f32x4 sc;
    {
      v4u qf[4];
#pragma unroll
      for (int s = 0; s < 4; ++s) qf[s] = *(const LAS v4u*)(L + aQr + s * 256);
      __builtin_amdgcn_sched_barrier(0);
      const f32x4 z4 = {0.f, 0.f, 0.f, 0.f};
      const f32x4 s0 = mfma32(kf[1], qf[1], mfma32(kf[0], qf[0], z4)), s1 = mfma32(kf[3], qf[3], mfma32(kf[2], qf[2], z4));
      sc = s0 + s1;
      int mx = imax(imax(fkey(sc[0]), fkey(sc[1])), imax(fkey(sc[2]), fkey(sc[3])));
      mx = x16_imax(mx); mx = x32_imax(mx);
      if (lane < 4) *(LAS int*)(L + aMw) = mx;
    }
    BARRIER();
    float mc;
    constexpr float C2 = 0.08838834764831845f * 1.4426950408889634f;
    {
      const v4u ma = *(const LAS v4u*)(L + aMr), mb = *(const LAS v4u*)(L + aMr + 16);
      const int M = imax(imax(imax((int)ma[0], (int)ma[1]), imax((int)ma[2], (int)ma[3])), imax(imax((int)mb[0], (int)mb[1]), imax((int)mb[2], (int)mb[3])));
      mc = funkey(M) * C2;
      float e[4];
#pragma unroll
      for (int r = 0; r < 4; ++r) e[r] = __builtin_amdgcn_exp2f(fmaf(sc[r], C2, -mc));
      v2u pk; pk[0] = pkbf(e[0], e[1]); pk[1] = pkbf(e[2], e[3]);
      if (c < 4) *(LAS v2u*)(L + aPw) = pk;
      float lsum = (bflo(pk[0]) + bfhi(pk[0])) + (bflo(pk[1]) + bfhi(pk[1]));
      lsum = x16_sum(lsum); lsum = x32_sum(lsum);
      if (lane < 4) *(LAS float*)(L + aMw + 128) = lsum;
    }
    BARRIER();
    {
      v4u pf[4]; s16x4 va[4][2];
#pragma unroll
      for (int s = 0; s < 4; ++s) { va[s][0] = trd(L + aV[0] + s * 8192); va[s][1] = trd(L + aV[1] + s * 8192); pf[s] = *(const LAS v4u*)(L + aPr + s * 256); }
      const f32x4 la = *(const LAS f32x4*)(L + aMr + 128), lb = *(const LAS f32x4*)(L + aMr + 144);
      __builtin_amdgcn_sched_barrier(0);
      const f32x4 z4 = {0.f, 0.f, 0.f, 0.f};
      const f32x4 o0 = mfma32(cat(va[1][0], va[1][1]), pf[1], mfma32(cat(va[0][0], va[0][1]), pf[0], z4));
      const f32x4 o1 = mfma32(cat(va[3][0], va[3][1]), pf[3], mfma32(cat(va[2][0], va[2][1]), pf[2], z4));
      const float l8 = ((la[0] + la[1]) + (la[2] + la[3])) + ((lb[0] + lb[1]) + (lb[2] + lb[3]));
      const f32x4 oa = o0 + o1;
      v4u gr; gr[0] = __float_as_uint((kk & 1) ? oa[2] : oa[0]); gr[1] = __float_as_uint((kk & 1) ? oa[3] : oa[1]); gr[2] = __float_as_uint((kk & 1) ? l8 : mc); gr[3] = otag;
      if (kk < 2) ST16(gr, ooff);
    }
    if (w < 4) {
      DELAY(DLY_O);
      v4u ga;
      for (unsigned spin = 0; spin < SPIN_MAX; ++spin) {
        asm volatile("" ::: "memory");
        ga = LD16(offa);
        if (NONEBAD(ga[3] ^ tex)) break;
      }
      const float aux = __uint_as_float(ga[2]), axn = dpp<XOR1>(aux);
      const float mj = (lane & 1) ? axn : aux, lj = (lane & 1) ? aux : axn;
      int mk = fkey(mj);
      mk = imax(mk, dppi<XOR8>(mk)); mk = x16_imax(mk); mk = x32_imax(mk);
      const float Mw = funkey(mk);
      const float wt = __builtin_amdgcn_exp2f(mj - Mw);
      float o0 = __uint_as_float(ga[0]) * wt, o1 = __uint_as_float(ga[1]) * wt, ll = lj * wt;
      o0 += dpp<XOR8>(o0); o1 += dpp<XOR8>(o1); ll += dpp<XOR8>(ll);
      o0 = x16_sum(o0); o1 = x16_sum(o1); ll = x16_sum(ll);
      o0 = x32_sum(o0); o1 = x32_sum(o1); ll = x32_sum(ll);
      if (lane < 9) { f32x2 o2; o2[0] = lane < 8 ? o0 : Mw; o2[1] = lane < 8 ? o1 : ll; *(LAS f32x2*)(L + aOw) = o2; }
    }
    BARRIER();
    {
      f32x4 ou[4]; f32x2 ml[4]; s16x4 a[4];
#pragma unroll
      for (int u = 0; u < 4; ++u) { ml[u] = *(const LAS f32x2*)(L + L_OP + u * 80 + 64); ou[u] = *(const LAS f32x4*)(L + aOr + u * 80); }
#pragma unroll
      for (int t = 0; t < 4; ++t) a[t] = trd(L + aWo[t]);
      f32x2 mv = *(const LAS f32x2*)(L + aMp);
      __builtin_amdgcn_sched_barrier(0);
      if (!act) { mv[0] = 0.f; mv[1] = 0.f; }
      const float M = funkey(imax(imax(fkey(ml[0][0]), fkey(ml[1][0])), imax(fkey(ml[2][0]), fkey(ml[3][0]))));
      float f[4];
#pragma unroll
      for (int u = 0; u < 4; ++u) f[u] = __builtin_amdgcn_exp2f(ml[u][0] - M);
      const float li = __builtin_amdgcn_rcpf((f[0] * ml[0][1] + f[1] * ml[1][1]) + (f[2] * ml[2][1] + f[3] * ml[3][1]));
      const f32x4 o4 = ((ou[0] * f[0] + ou[1] * f[1]) + (ou[2] * f[2] + ou[3] * f[3])) * li;
      v2u of; of[0] = pkbf(o4[0], o4[1]); of[1] = pkbf(o4[2], o4[3]);
      const f32x4 z4 = {0.f, 0.f, 0.f, 0.f};
      v2u om[4];
#pragma unroll
      for (int t = 0; t < 4; ++t) { om[t][0] = of[0] & tm[t]; om[t][1] = of[1] & tm[t]; }
      const f32x4 d = mfma16(a[1], om[1], mfma16(a[0], om[0], z4)) + mfma16(a[3], om[3], mfma16(a[2], om[2], z4));
      v4u gr; gr[0] = __float_as_uint(c < 8 ? (prs ? d[2] : d[0]) : mv[0]); gr[1] = tlo; gr[2] = __float_as_uint(c < 8 ? (prs ? d[3] : d[1]) : mv[1]); gr[3] = thi;
      ST16(gr, soff);
    }
    {
      DELAY(DLY_S);
      v4u gq;
      for (unsigned spin = 0; spin < SPIN_MAX; ++spin) {
        asm volatile("" ::: "memory");
        gq = LD16(foff);
        if (NONEBAD(TAGBAD(gq))) break;
      }
      float a0 = __uint_as_float(gq[0]), a1 = __uint_as_float(gq[2]);
      a0 += dpp<XOR2>(a0); a1 += dpp<XOR2>(a1); a0 += dpp<ROR4>(a0); a1 += dpp<ROR4>(a1); a0 += dpp<XOR8>(a0); a1 += dpp<XOR8>(a1);
      a0 = x16_sum(a0); a1 = x16_sum(a1); a0 = x32_sum(a0); a1 = x32_sum(a1);
      if (lane < 2) { f32x2 o2; o2[0] = a0; o2[1] = a1; *(LAS f32x2*)(L + L_FS + lane * 64 + w * 8) = o2; }
    }
    BARRIER();
    if (w == 0) {
      const int e = (lane >> 3) & 1, u = lane & 7;
      const unsigned xw = *(const LAS unsigned*)(L + L_XB), b2w = *(const LAS unsigned*)(L + L_XB + 4);
      float s = *(const LAS float*)(L + L_FS + u * 8 + e * 4), m = *(const LAS float*)(L + L_FM + u * 8 + e * 4);
      s += dpp<XOR1>(s); m += dpp<XOR1>(m); s += dpp<XOR2>(s); m += dpp<XOR2>(m); s += dpp<XOR7>(s); m += dpp<XOR7>(m);
      const float z = rbf(rbf(m) + (e ? bfhi(b2w) : bflo(b2w)));
      const float gt = rbf(z * __builtin_amdgcn_rcpf(1.f + __builtin_amdgcn_exp2f(-1.4426950408889634f * z)));
      const float r = (e ? bfhi(xw) : bflo(xw)) + rbf(rbf(s) * gt);
      const float rn = dpp<XOR8>(r);
      if (lane == 0) {
        *(GAS unsigned*)(p.out + col) = pkbf(r, rn);
        if (b == 0) { const unsigned long long T = (unsigned long long)tlo | ((unsigned long long)thi << 32); __hip_atomic_store((GAS unsigned long long*)(p.ws + W_E), T, __ATOMIC_RELAXED, __HIP_MEMORY_SCOPE_AGENT); }
      }
    }
  }
}

extern "C" void kernel_launch(void* const* d_in, const int* in_sizes, int n_in,
                              void* d_out, int out_size, void* d_ws, size_t ws_size,
                              hipStream_t stream) {
  Params p;
  p.x = (const u16*)d_in[0]; p.kc = (const u16*)d_in[1]; p.vc = (const u16*)d_in[2]; p.wq = (const u16*)d_in[3]; p.wo = (const u16*)d_in[4];
  p.w1 = (const u16*)d_in[6]; p.b1 = (const u16*)d_in[7]; p.w2 = (const u16*)d_in[8]; p.b2 = (const u16*)d_in[9];
  p.out = (u16*)d_out; p.ws = (unsigned char*)d_ws;
  fused_kernel<<<NB, NTHR, 0, stream>>>(p);
}
```

```cpp
#include <hip/hip_runtime.h>
#include <hip/hip_bf16.h>

#define LAS __attribute__((address_space(3)))
#define GAS __attribute__((address_space(1)))
typedef unsigned short u16;
typedef unsigned v4u __attribute__((ext_vector_type(4)));
typedef unsigned v2u __attribute__((ext_vector_type(2)));
typedef float f32x4 __attribute__((ext_vector_type(4)));
typedef float f32x2 __attribute__((ext_vector_type(2)));
typedef short s16x4 __attribute__((ext_vector_type(4)));
typedef short s16x8 __attribute__((ext_vector_type(8)));
typedef __bf16 bf16x8 __attribute__((ext_vector_type(8)));
typedef __bf16 bf16x2 __attribute__((ext_vector_type(2)));

constexpr int DLY_Q = 24, DLY_O = 24, DLY_S = 30;
constexpr int LWIN = 24;

constexpr int D = 512, SK = 65536, WIN = 4096, FF = 1792;
constexpr int NB = 256, NTHR = 640;
constexpr int L_WQ = 0, L_K = 16384, L_V = 49152, L_WO = 81920, L_W2 = 98304, L_W1 = 106496, L_XN = 114688, L_Y = 115712, L_Q = 116736, L_P = 117760;
constexpr int L_MX = 118784, L_SM = L_MX + 128, L_TAG = L_SM + 128, L_OP = L_TAG + 16, L_FS = L_OP + 512, L_FM = L_FS + 64, L_B1 = L_FM + 64, L_XB = L_B1 + 16, L_FLG = L_XB + 16, L_MP = L_FLG + 32, L_END = L_MP + 2048;
constexpr int W_E = 0;
constexpr int W_Q = 4096;
constexpr int W_Z = W_Q + 131072;
constexpr int W_O = W_Z + 262144;
constexpr int W_S = W_O + 1048576;
constexpr int W_TOUCH = W_S + 2097152;
constexpr int W_END = W_TOUCH + 4096;
constexpr unsigned SPIN_MAX = 1u << 17;

__device__ __forceinline__ float bflo(unsigned w) { return __uint_as_float(w << 16); }
__device__ __forceinline__ float bfhi(unsigned w) { return __uint_as_float(w & 0xffff0000u); }
__device__ __forceinline__ unsigned pkbf(float lo, float hi) { bf16x2 r; r[0] = (__bf16)lo; r[1] = (__bf16)hi; return __builtin_bit_cast(unsigned, r); }
__device__ __forceinline__ float rbf(float f) { return bflo(pkbf(f, f)); }
template <int CTRL> __device__ __forceinline__ float dpp(float x) {
  return __builtin_bit_cast(float, __builtin_amdgcn_mov_dpp(__builtin_bit_cast(int, x), CTRL, 0xf, 0xf, true));
}
constexpr int XOR1 = 0xB1, XOR2 = 0x4E, XOR7 = 0x141, XOR15 = 0x140, XOR8 = 0x128, ROR4 = 0x124;
__device__ __forceinline__ int fkey(float f) { const int i = __float_as_int(f); return i ^ ((i >> 31) & 0x7fffffff); }
__device__ __forceinline__ float funkey(int k) { return __int_as_float(k ^ ((k >> 31) & 0x7fffffff)); }
template <int CTRL> __device__ __forceinline__ int dppi(int x) { return __builtin_amdgcn_mov_dpp(x, CTRL, 0xf, 0xf, true); }
__device__ __forceinline__ int imax(int a, int b) { return a > b ? a : b; }
__device__ __forceinline__ int x16_imax(int x) { auto s = __builtin_amdgcn_permlane16_swap((unsigned)x, (unsigned)x, false, false); return imax((int)s[0], (int)s[1]); }
__device__ __forceinline__ int x32_imax(int x) { auto s = __builtin_amdgcn_permlane32_swap((unsigned)x, (unsigned)x, false, false); return imax((int)s[0], (int)s[1]); }
__device__ __forceinline__ float x16_sum(float x) { auto s = __builtin_amdgcn_permlane16_swap(__float_as_uint(x), __float_as_uint(x), false, false); return __uint_as_float(s[0]) + __uint_as_float(s[1]); }
__device__ __forceinline__ float x32_sum(float x) { auto s = __builtin_amdgcn_permlane32_swap(__float_as_uint(x), __float_as_uint(x), false, false); return __uint_as_float(s[0]) + __uint_as_float(s[1]); }
__device__ __forceinline__ void wave_sum2(float& a, float& b) {
  a += dpp<XOR1>(a); b += dpp<XOR1>(b); a += dpp<XOR2>(a); b += dpp<XOR2>(b); a += dpp<XOR7>(a); b += dpp<XOR7>(b); a += dpp<XOR15>(a); b += dpp<XOR15>(b);
  a = x16_sum(a); b = x16_sum(b); a = x32_sum(a); b = x32_sum(b);
}
__device__ __forceinline__ f32x4 mfma32(v4u a, v4u b, f32x4 c) {
  return __builtin_amdgcn_mfma_f32_16x16x32_bf16(__builtin_bit_cast(bf16x8, a), __builtin_bit_cast(bf16x8, b), c, 0, 0, 0);
}
__device__ __forceinline__ f32x4 mfma16(s16x4 a, v2u b, f32x4 c) {
  return __builtin_amdgcn_mfma_f32_16x16x16bf16_1k(a, __builtin_bit_cast(s16x4, b), c, 0, 0, 0);
}
__device__ __forceinline__ s16x4 trd(LAS unsigned char* p) { return __builtin_amdgcn_ds_read_tr16_b64_v4i16((LAS s16x4*)p); }
__device__ __forceinline__ v4u cat(s16x4 lo, s16x4 hi) { return __builtin_bit_cast(v4u, __builtin_shufflevector(lo, hi, 0, 1, 2, 3, 4, 5, 6, 7)); }
__device__ __forceinline__ v4u ln_packed(v4u x) {
  float v[8];
#pragma unroll
  for (int i = 0; i < 4; ++i) { v[2 * i] = bflo(x[i]); v[2 * i + 1] = bfhi(x[i]); }
  const float piv = __uint_as_float(__builtin_amdgcn_readfirstlane(x[0]) << 16);
  float s = 0.f, q = 0.f;
#pragma unroll
  for (int i = 0; i < 8; ++i) { const float d = v[i] - piv; s += d; q = fmaf(d, d, q); }
  wave_sum2(s, q);
  const float md = s * (1.0f / D);
  const float var = rbf(fmaxf(fmaf(-md, md, q * (1.0f / D)), 0.f));
  const float m = rbf(piv + md);
  const float sd = rbf(__builtin_amdgcn_sqrtf(rbf(var + 1e-5f)));
  const float inv = __builtin_amdgcn_rcpf(sd);
  v4u r;
#pragma unroll
  for (int i = 0; i < 4; ++i) r[i] = pkbf(rbf(v[2 * i] - m) * inv, rbf(v[2 * i + 1] - m) * inv);
  return r;
}
#define WAIT_VM(n) asm volatile("s_waitcnt vmcnt(" #n ")" ::: "memory")
#define WAIT_VMN(n) asm volatile("s_waitcnt vmcnt(%0)" :: "i"(n) : "memory")
static_assert(LWIN % 4 == 0 && LWIN >= 8 && LWIN <= 36, "loader window");
#define BARRIER() asm volatile("s_waitcnt lgkmcnt(0)\n\ts_barrier" ::: "memory")
#define RAWBAR() asm volatile("s_barrier" ::: "memory")
__device__ __forceinline__ void glds4(const void* sbase, unsigned v0, unsigned v1, unsigned v2, unsigned v3, unsigned ldsaddr) {
  unsigned keep;
  asm volatile("s_mov_b32 %0, m0\n\ts_mov_b32 m0, %6\n\ts_nop 0\n\tglobal_load_lds_dwordx4 %1, %5 nt\n\tglobal_load_lds_dwordx4 %2, %5 offset:1024 nt\n\t"
               "global_load_lds_dwordx4 %3, %5 offset:2048 nt\n\tglobal_load_lds_dwordx4 %4, %5 offset:3072 nt\n\ts_mov_b32 m0, %0"
               : "=&s"(keep) : "v"(v0), "v"(v1), "v"(v2), "v"(v3), "s"(sbase), "s"(ldsaddr) : "memory");
}
#define DELAY(n) do { if ((n) > 0) __builtin_amdgcn_s_sleep((n) > 127 ? 127 : (n)); } while (0)
#define LD16(off) __builtin_amdgcn_raw_buffer_load_b128(rs, (off), 0, 16)
#define ST16(v, off) __builtin_amdgcn_raw_buffer_store_b128((v), rs, (off), 0, 16)
#define TAGBAD(x) (((x)[1] ^ tlo) | ((x)[3] ^ thi))
#define NONEBAD(v) (__builtin_amdgcn_ballot_w64((v) != 0u) == 0ull)
#define PIN(x) asm volatile("" : "+v"(x))
__device__ __forceinline__ void lds_add(LAS unsigned* q, unsigned v) { __hip_atomic_fetch_add(q, v, __ATOMIC_RELAXED, __HIP_MEMORY_SCOPE_WORKGROUP); }
__device__ __forceinline__ unsigned lds_peek(LAS unsigned* q) { return __builtin_amdgcn_readfirstlane(__hip_atomic_load(q, __ATOMIC_RELAXED, __HIP_MEMORY_SCOPE_WORKGROUP)); }

constexpr int first_group(int need) { for (int g = 6; g < 22; ++g) if (4 * g - (LWIN - 4) >= need) return g; return 99; }

struct Params {
  const u16* x; const u16* kc; const u16* vc; const u16* wq; const u16* wo;
  const u16* w1; const u16* b1; const u16* w2; const u16* b2; u16* out; unsigned char* ws;
};

__global__ void __launch_bounds__(NTHR, 3) fused_kernel(Params p) {
  __shared__ __attribute__((aligned(16))) unsigned char lds[L_END];
  LAS unsigned char* L = (LAS unsigned char*)lds;
  const unsigned lbase = (unsigned)(size_t)L;
  const int tid = threadIdx.x, lane = tid & 63, wave = __builtin_amdgcn_readfirstlane(tid >> 6);
  const int b = blockIdx.x, h = b & 7, j = b >> 3;
  const int g = lane >> 4, c = lane & 15;
  asm volatile("" :: "s"(p.x), "s"(p.kc), "s"(p.vc), "s"(p.wq), "s"(p.wo), "s"(p.w1), "s"(p.b1), "s"(p.w2), "s"(p.b2), "s"(p.out), "s"(p.ws));
  const __amdgpu_buffer_rsrc_t rs = __builtin_amdgcn_make_buffer_rsrc((void*)p.ws, 0, W_END, 0x00020000);
  const bool act = j < 28;
  const int cg = h * 28 + (act ? j : j - 28);
  const int cc_cg = h * 4 + (j >> 3), cc_i = j & 7;
  const int col = 16 * cc_cg + 2 * cc_i;

  LAS unsigned* flg = (LAS unsigned*)(L + L_FLG);
  if (tid < 2) { v4u z = {0u, 0u, 0u, 0u}; *(LAS v4u*)(L + L_FLG + tid * 16) = z; }
  if (wave == 0) {
    const int lc = j >> 2, rq = j & 3, lm = cg >> 3, kq = cg & 7;
    __builtin_amdgcn_s_setprio(3);
    const unsigned kvoff = ((unsigned)h * SK + (SK - WIN) + (unsigned)j * 128u) * 256u;
    const unsigned char* kb = (const unsigned char*)p.kc + kvoff;
    const unsigned char* vb = (const unsigned char*)p.vc + kvoff;
    const unsigned k0 = (unsigned)g * 256u + (unsigned)((c ^ g) * 16), v0 = (unsigned)g * 256u + (unsigned)((c ^ (g << 2)) * 16), l16 = (unsigned)lane * 16u;
    BARRIER();
    {
      const unsigned ph = (unsigned)(lane >> 4) & 1u, c8 = (unsigned)lane & 7u;
      const unsigned ce = (c8 ^ (ph << 1)) * 16u, co = (c8 ^ ((ph | 2u) << 1)) * 16u;
      const unsigned rq8 = (unsigned)(lane >> 3) * 8192u, r18 = (unsigned)(lane >> 3) * (unsigned)(FF * 2);
      const unsigned char* sq = (const unsigned char*)p.wq + (unsigned)((h * 512 + lc * 64) * 2 + rq * 128 * 8192);
      const unsigned char* s1 = (const unsigned char*)p.w1 + (unsigned)(lm * 128 + kq * 64 * (FF * 2));
      constexpr unsigned SQ = 8u * 8192u - 1024u, S1 = 8u * (FF * 2) - 1024u;
#pragma unroll 1
      for (int n = 0; n < 4; ++n) glds4(sq + (unsigned)(n * 32 * 8192), rq8 + ce, rq8 + co + SQ, rq8 + ce + 2u * SQ, rq8 + co + 3u * SQ, lbase + L_WQ + n * 4096);
#pragma unroll 1
      for (int n = 0; n < 2; ++n) glds4(s1 + (unsigned)(n * 32 * (FF * 2)), r18 + ce, r18 + co + S1, r18 + ce + 2u * S1, r18 + co + 3u * S1, lbase + L_W1 + n * 4096);
    }
    constexpr int GFQ = first_group(16), GF1 = first_group(24), GFK = first_group(56);
    static_assert(GFQ <= 21 && GF1 <= 21 && GFK <= 21, "flags are set inside the K and V loops");
#define GROUP_FLAGS(gi) do { \
      if ((gi) == GFQ && lane == 0) __hip_atomic_store(flg + 0, 1u, __ATOMIC_RELAXED, __HIP_MEMORY_SCOPE_WORKGROUP); \
      if ((gi) == GF1 && lane == 0) __hip_atomic_store(flg + 2, 1u, __ATOMIC_RELAXED, __HIP_MEMORY_SCOPE_WORKGROUP); \
      if ((gi) == GFK && lane == 0) __hip_atomic_store(flg + 5, 1u, __ATOMIC_RELAXED, __HIP_MEMORY_SCOPE_WORKGROUP); } while (0)
#pragma unroll 1
    for (int n = 0; n < 8; ++n) { const int m = (n + j) & 7; WAIT_VMN(LWIN - 4); GROUP_FLAGS(6 + n); glds4(kb + m * 4096, k0, k0 ^ 64u, k0 ^ 128u, k0 ^ 192u, lbase + L_K + m * 4096); }
#pragma unroll 1
    for (int n = 0; n < 8; ++n) { const int m = (n + j) & 7; WAIT_VMN(LWIN - 4); GROUP_FLAGS(14 + n); glds4(vb + m * 4096, v0, v0 ^ 16u, v0 ^ 32u, v0 ^ 48u, lbase + L_V + m * 4096); }
    WAIT_VMN(32);
    RAWBAR();
    RAWBAR();
#pragma unroll 1
    for (int n = 0; n < 2; ++n) glds4((const unsigned char*)p.w2 + (unsigned)(cg * 8192 + n * 4096), l16, l16, l16, l16, lbase + L_W2 + n * 4096);
#pragma unroll 1
    for (int n0 = 0; n0 < 4; ++n0) {
      const int n = (n0 + j) & 3;
      const unsigned r0 = (unsigned)((4 * n) & 7) << 5;
      glds4((const unsigned char*)p.wo + (unsigned)((h * 512 + j * 16 + 4 * n) * 1024), l16 ^ r0, l16 ^ (r0 + 32u), l16 ^ (r0 + 64u), l16 ^ (r0 + 96u), lbase + L_WO + n * 4096);
    }
    WAIT_VMN(24);
    RAWBAR();
    WAIT_VMN(16);
    if (lane == 0) __hip_atomic_store(flg + 4, 1u, __ATOMIC_RELAXED, __HIP_MEMORY_SCOPE_WORKGROUP);
    WAIT_VM(0);
    RAWBAR();
  } else if (wave == 9) {
    const int lm = cg >> 3, kq = cg & 7;
    const int tsel = (c >> 1) & 3, prs = c & 1;
    {
      const v4u xr = *(const GAS v4u*)(p.x + lane * 8);
      const unsigned touch = __builtin_amdgcn_raw_buffer_load_b32(rs, W_TOUCH + (lane & 7) * 4, 0, 16);
      const unsigned long long a_b1 = (unsigned long long)(p.b1 + cg * 8), a_x = (unsigned long long)(p.x + col), a_b2 = (unsigned long long)(p.b2 + col);
      const unsigned long long a_sm = lane < 4 ? a_b1 + lane * 4 : (lane == 4 ? a_x : a_b2);
      const unsigned small = *(const GAS unsigned*)a_sm;
      __builtin_amdgcn_sched_barrier(0);
      RAWBAR();
      __builtin_amdgcn_sched_barrier(0);
      *(LAS v4u*)(L + L_Y + lane * 16) = ln_packed(ln_packed(xr));
      if (lane < 6) *(LAS unsigned*)(L + L_B1 + lane * 4) = small;
      asm volatile("" :: "v"(touch));
    }
    const int zoff = W_Z + ((lm * 8 + kq) * 8 + ((lane & 31) >> 2)) * 128 + (lane & 3) * 16;
    {
      const int q4 = c >> 2, pp = c & 3;
      const int ph = ((q4 >> 1) & 1) | ((g & 1) << 1);
      v4u yf[2];
#pragma unroll
      for (int s = 0; s < 2; ++s) yf[s] = *(const LAS v4u*)(L + L_Y + (64 * kq + 32 * s + 8 * g) * 2);
      for (unsigned spin = 0; spin < SPIN_MAX; ++spin) {
        const v4u f = *(volatile LAS v4u*)flg;
        asm volatile("" ::: "memory");
        if (__builtin_amdgcn_readfirstlane(f[1] & f[2]) != 0u) break;
        __builtin_amdgcn_s_sleep(1);
      }
      const v2u tg = *(const LAS v2u*)(L + L_TAG);
      f32x4 a0 = {0.f, 0.f, 0.f, 0.f};
#pragma unroll
      for (int t = 0; t < 4; ++t) {
        LAS unsigned char* ab = L + L_W1 + (8 * g + q4) * 128 + (((2 * t + (pp >> 1)) ^ (ph << 1)) * 16) + (pp & 1) * 8;
        const unsigned m = tsel == t ? ~0u : 0u;
#pragma unroll
        for (int s = 0; s < 2; ++s) { v4u ym; ym[0] = yf[s][0] & m; ym[1] = yf[s][1] & m; ym[2] = yf[s][2] & m; ym[3] = yf[s][3] & m; a0 = mfma32(cat(trd(ab + s * 4096), trd(ab + s * 4096 + 512)), ym, a0); }
      }
      v4u gr; gr[0] = __float_as_uint(prs ? a0[2] : a0[0]); gr[1] = tg[0]; gr[2] = __float_as_uint(prs ? a0[3] : a0[1]); gr[3] = tg[1];
      if (c < 8 && act) ST16(gr, W_Z + ((lm * 8 + 2 * tsel + (g >> 1)) * 8 + kq) * 128 + (2 * (g & 1) + prs) * 16);
    }
    DELAY(DLY_Q + 8);
    v4u zq;
    asm volatile("global_load_dwordx4 %0, %1, %2 sc1" : "=v"(zq) : "v"(zoff), "s"(p.ws) : "memory");
    RAWBAR();
    RAWBAR();
    asm volatile("s_waitcnt vmcnt(0)" : "+v"(zq) :: "memory");
    v2u hm[4];
    if (act) {
      const v2u tg = *(const LAS v2u*)(L + L_TAG);
      const unsigned tlo = tg[0], thi = tg[1];
      if (!NONEBAD(TAGBAD(zq))) {
        for (unsigned spin = 0; spin < SPIN_MAX; ++spin) {
          asm volatile("" ::: "memory");
          zq = LD16(zoff);
          if (NONEBAD(TAGBAD(zq))) break;
        }
      }
      float z0 = __uint_as_float(zq[0]), z1 = __uint_as_float(zq[2]);
      z0 += dpp<ROR4>(z0); z1 += dpp<ROR4>(z1); z0 += dpp<XOR8>(z0); z1 += dpp<XOR8>(z1); z0 = x16_sum(z0); z1 = x16_sum(z1);
      const unsigned bw = *(const LAS unsigned*)(L + L_B1 + (lane & 3) * 4);
      float hv[2];
#pragma unroll
      for (int e = 0; e < 2; ++e) {
        const float z = rbf(rbf(e ? z1 : z0) + (e ? bfhi(bw) : bflo(bw)));
        const float uu = 0.7978845608028654f * (z + 0.044715f * z * z * z);
        const float th = 1.f - 2.f * __builtin_amdgcn_rcpf(1.f + __builtin_amdgcn_exp2f(2.885390081777927f * uu));
        hv[e] = 0.5f * z * (1.f + th);
      }
      const unsigned hp = pkbf(hv[0], hv[1]);
      const unsigned h0 = __builtin_amdgcn_readlane(hp, 0), h1 = __builtin_amdgcn_readlane(hp, 1), h2 = __builtin_amdgcn_readlane(hp, 2), h3 = __builtin_amdgcn_readlane(hp, 3);
#pragma unroll
      for (int t = 0; t < 4; ++t) { const bool on = tsel == t && g < 2; hm[t][0] = on ? (g == 0 ? h0 : h2) : 0u; hm[t][1] = on ? (g == 0 ? h1 : h3) : 0u; }
    }
    RAWBAR();
    if (act) {
      for (unsigned spin = 0; spin < SPIN_MAX && lds_peek(flg + 4) == 0u; ++spin) __builtin_amdgcn_s_sleep(1);
      LAS unsigned char* ab = L + L_W2 + ((4 * g + (c >> 2)) & 7) * 1024 + (c & 3) * 8;
      LAS unsigned char* mb = L + L_MP + (16 * tsel + 4 * g + 2 * prs) * 4;
#pragma unroll 2
      for (int T = 0; T < 8; ++T) {
        f32x4 d = {0.f, 0.f, 0.f, 0.f};
#pragma unroll
        for (int t = 0; t < 4; ++t) d = mfma16(trd(ab + (4 * T + t) * 32), hm[t], d);
        f32x2 o2; o2[0] = prs ? d[2] : d[0]; o2[1] = prs ? d[3] : d[1];
        if (c < 8) *(LAS f32x2*)(mb + T * 256) = o2;
      }
    }
    BARRIER();
  } else {
    const int w = wave - 1;
    __builtin_amdgcn_s_setprio(2);
    const int lc = j >> 2, rq = j & 3;
    if (w == 0) {
      const v4u xr = *(const GAS v4u*)(p.x + lane * 8);
      const v2u ev = __builtin_amdgcn_raw_buffer_load_b64(rs, W_E, 0, 16);
      __builtin_amdgcn_sched_barrier(0);
      RAWBAR();
      __builtin_amdgcn_sched_barrier(0);
      *(LAS v4u*)(L + L_XN + lane * 16) = ln_packed(xr);
      const unsigned long long E = (unsigned long long)(unsigned)__builtin_amdgcn_readfirstlane(ev[0]) | ((unsigned long long)(unsigned)__builtin_amdgcn_readfirstlane(ev[1]) << 32);
      const unsigned long long T = E * 6364136223846793005ull + 1442695040888963407ull;
      if (lane == 0) { v2u tg; tg[0] = (unsigned)T; tg[1] = (unsigned)(T >> 32); *(LAS v2u*)(L + L_TAG) = tg; }
      asm volatile("s_waitcnt lgkmcnt(0)" ::: "memory");
      if (lane == 0) __hip_atomic_store(flg + 1, 1u, __ATOMIC_RELAXED, __HIP_MEMORY_SCOPE_WORKGROUP);
    } else {
      RAWBAR();
    }
    unsigned tlo, thi;
    {
      const int q4 = c >> 2, pp = c & 3, t = w & 3;
      int toff = L_WQ + (8 * g + q4) * 128 + (((2 * t + (pp >> 1)) ^ ((((q4 >> 1) & 1) | ((g & 1) << 1)) << 1)) * 16) + (pp & 1) * 8;
      int xoff = L_XN + (128 * rq + 8 * g) * 2;
      int off = W_Q + ((((h * 8 + lc) * 4 + rq) * 16) + 4 * t + g) * 16;
      PIN(toff); PIN(xoff); PIN(off);
      for (unsigned spin = 0; spin < SPIN_MAX && lds_peek(flg + 1) == 0u; ++spin) __builtin_amdgcn_s_sleep(1);
      const v2u tg = *(const LAS v2u*)(L + L_TAG);
      tlo = tg[0]; thi = tg[1];
      if (w < 4) {
        v4u xf[4];
#pragma unroll
        for (int s = 0; s < 4; ++s) xf[s] = *(const LAS v4u*)(L + xoff + s * 64);
        for (unsigned spin = 0; spin < SPIN_MAX && lds_peek(flg + 0) == 0u; ++spin) { }
        s16x4 a[4][2];
#pragma unroll
        for (int s = 0; s < 4; ++s) { a[s][0] = trd(L + toff + s * 4096); a[s][1] = trd(L + toff + s * 4096 + 512); }
        __builtin_amdgcn_sched_barrier(0);
        const f32x4 z4 = {0.f, 0.f, 0.f, 0.f};
        const f32x4 a0 = mfma32(cat(a[1][0], a[1][1]), xf[1], mfma32(cat(a[0][0], a[0][1]), xf[0], z4)) + mfma32(cat(a[3][0], a[3][1]), xf[3], mfma32(cat(a[2][0], a[2][1]), xf[2], z4));
        v4u gr; gr[0] = pkbf(a0[0], a0[1]); gr[1] = tlo; gr[2] = pkbf(a0[2], a0[3]); gr[3] = thi;
        if (c == 0) ST16(gr, off);
        if (lane == 0) lds_add(flg + 3, 1u);
      } else {
        for (unsigned spin = 0; spin < SPIN_MAX && lds_peek(flg + 3) < 4u; ++spin) __builtin_amdgcn_s_sleep(1);
      }
    }
    const int head = c & 3, kk = c >> 2, q4 = c >> 2, pp = c & 3, tsel = (c >> 1) & 3, prs = c & 1;
    int qoff = W_Q + (h * 8 + w) * 1024 + lane * 16;
    int aQw; { const int dim = (w & 1) * 64 + 4 * (lane & 15); aQw = L_Q + (((dim >> 5) * 4 + ((dim >> 3) & 3)) * 4 + (w >> 1)) * 16 + (dim & 7) * 2; }
    int aK[4];
#pragma unroll
    for (int s = 0; s < 4; ++s) aK[s] = L_K + (w * 16 + c) * 256 + (((4 * s + g) ^ c) * 16);
    int aQr = L_Q + (g * 4 + head) * 16;
    int aMw = L_MX + ((lane & 3) * 8 + w) * 4;
    int aMr = L_MX + head * 32;
    int aPw = L_P + (((w >> 1) * 4 + 2 * (w & 1) + (g >> 1)) * 4 + head) * 16 + (g & 1) * 8;
    int aPr = L_P + (g * 4 + head) * 16;
    int aV[2];
#pragma unroll
    for (int t2 = 0; t2 < 2; ++t2) aV[t2] = L_V + (8 * g + 4 * t2 + q4) * 256 + (((2 * w + (pp >> 1)) ^ ((q4 << 2) | ((2 * g + t2) & 3))) * 16) + (pp & 1) * 8;
    int ooff = W_O + ((((h * 32 + head * 8 + w) * 32 + j) * 8) + 2 * g + kk) * 16;
    int offa = W_O + (((h * 32 + j) * 32 + 8 * (w & 3)) * 8 + lane) * 16;
    int aOw = L_OP + (w & 3) * 80 + lane * 8, aOr = L_OP + g * 16;
    int aWo[4];
#pragma unroll
    for (int t = 0; t < 4; ++t) { const int row = 4 * g + (c >> 2); aWo[t] = L_WO + row * 1024 + ((2 * (4 * w + t) + ((c & 3) >> 1)) ^ ((row & 7) << 1)) * 16 + (c & 1) * 8; }
    int aMp = L_MP + (16 * (4 * w + tsel) + 4 * g + 2 * prs) * 4;
    int soff = W_S + (((2 * (4 * w + tsel) + (g >> 1)) * 256 + b) * 4 + 2 * (g & 1) + prs) * 32 + (c >> 3) * 16;
    int foff; { const int cc = 8 * cc_cg + cc_i; foff = W_S + (((cc >> 2) * 256 + 32 * w + (lane >> 1)) * 4 + (cc & 3)) * 32 + (lane & 1) * 16; }
    unsigned tex = (lane & 1) ? thi : tlo, otag = (kk & 1) ? thi : tlo;
    unsigned tm[4];
#pragma unroll
    for (int t = 0; t < 4; ++t) tm[t] = tsel == t ? ~0u : 0u;
    PIN(qoff); PIN(aQw); PIN(aK[0]); PIN(aK[1]); PIN(aK[2]); PIN(aK[3]); PIN(aQr); PIN(aMw); PIN(aMr); PIN(aPw); PIN(aPr); PIN(aV[0]); PIN(aV[1]);
    PIN(ooff); PIN(offa); PIN(aOw); PIN(aOr); PIN(aWo[0]); PIN(aWo[1]); PIN(aWo[2]); PIN(aWo[3]); PIN(aMp); PIN(soff); PIN(foff); PIN(tex); PIN(otag);
    PIN(tm[0]); PIN(tm[1]); PIN(tm[2]); PIN(tm[3]);
    DELAY(DLY_Q);
    v4u q0;
    for (unsigned spin = 0; spin < SPIN_MAX; ++spin) {
      asm volatile("" ::: "memory");
      q0 = LD16(qoff);
      if (NONEBAD(TAGBAD(q0))) break;
    }
    for (unsigned spin = 0; spin < SPIN_MAX && lds_peek(flg + 5) == 0u; ++spin) { }
    v4u kf[4];
#pragma unroll
    for (int s = 0; s < 4; ++s) kf[s] = *(const LAS v4u*)(L + aK[s]);
    {
      float u0 = bflo(q0[0]), u1 = bfhi(q0[0]), u2 = bflo(q0[2]), u3 = bfhi(q0[2]);
      u0 = x16_sum(u0); u1 = x16_sum(u1); u2 = x16_sum(u2); u3 = x16_sum(u3);
      u0 = x32_sum(u0); u1 = x32_sum(u1); u2 = x32_sum(u2); u3 = x32_sum(u3);
      v2u d; d[0] = pkbf(u0, u1); d[1] = pkbf(u2, u3);
      if (lane < 16) *(LAS v2u*)(L + aQw) = d;
    }
    BARRIER();
    f32x4 sc;
    {
      v4u qf[4];
#pragma unroll
      for (int s = 0; s < 4; ++s) qf[s] = *(const LAS v4u*)(L + aQr + s * 256);
      __builtin_amdgcn_sched_barrier(0);
      const f32x4 z4 = {0.f, 0.f, 0.f, 0.f};
      const f32x4 s0 = mfma32(kf[1], qf[1], mfma32(kf[0], qf[0], z4)), s1 = mfma32(kf[3], qf[3], mfma32(kf[2], qf[2], z4));
      sc = s0 + s1;
      int mx = imax(imax(fkey(sc[0]), fkey(sc[1])), imax(fkey(sc[2]), fkey(sc[3])));
      mx = x16_imax(mx); mx = x32_imax(mx);
      if (lane < 4) *(LAS int*)(L + aMw) = mx;
    }
    BARRIER();
    float mc;
    constexpr float C2 = 0.08838834764831845f * 1.4426950408889634f;
    {
      const v4u ma = *(const LAS v4u*)(L + aMr), mb = *(const LAS v4u*)(L + aMr + 16);
      const int M = imax(imax(imax((int)ma[0], (int)ma[1]), imax((int)ma[2], (int)ma[3])), imax(imax((int)mb[0], (int)mb[1]), imax((int)mb[2], (int)mb[3])));
      mc = funkey(M) * C2;
      float e[4];
#pragma unroll
      for (int r = 0; r < 4; ++r) e[r] = __builtin_amdgcn_exp2f(fmaf(sc[r], C2, -mc));
      v2u pk; pk[0] = pkbf(e[0], e[1]); pk[1] = pkbf(e[2], e[3]);
      if (c < 4) *(LAS v2u*)(L + aPw) = pk;
      float lsum = (bflo(pk[0]) + bfhi(pk[0])) + (bflo(pk[1]) + bfhi(pk[1]));
      lsum = x16_sum(lsum); lsum = x32_sum(lsum);
      if (lane < 4) *(LAS float*)(L + aMw + 128) = lsum;
    }
    BARRIER();
    {
      v4u pf[4]; s16x4 va[4][2];
#pragma unroll
      for (int s = 0; s < 4; ++s) { va[s][0] = trd(L + aV[0] + s * 8192); va[s][1] = trd(L + aV[1] + s * 8192); pf[s] = *(const LAS v4u*)(L + aPr + s * 256); }
      const f32x4 la = *(const LAS f32x4*)(L + aMr + 128), lb = *(const LAS f32x4*)(L + aMr + 144);
      __builtin_amdgcn_sched_barrier(0);
      const f32x4 z4 = {0.f, 0.f, 0.f, 0.f};
      const f32x4 o0 = mfma32(cat(va[1][0], va[1][1]), pf[1], mfma32(cat(va[0][0], va[0][1]), pf[0], z4));
      const f32x4 o1 = mfma32(cat(va[3][0], va[3][1]), pf[3], mfma32(cat(va[2][0], va[2][1]), pf[2], z4));
      const float l8 = ((la[0] + la[1]) + (la[2] + la[3])) + ((lb[0] + lb[1]) + (lb[2] + lb[3]));
      const f32x4 oa = o0 + o1;
      v4u gr; gr[0] = __float_as_uint((kk & 1) ? oa[2] : oa[0]); gr[1] = __float_as_uint((kk & 1) ? oa[3] : oa[1]); gr[2] = __float_as_uint((kk & 1) ? l8 : mc); gr[3] = otag;
      if (kk < 2) ST16(gr, ooff);
    }
    if (w < 4) {
      DELAY(DLY_O);
      v4u ga;
      for (unsigned spin = 0; spin < SPIN_MAX; ++spin) {
        asm volatile("" ::: "memory");
        ga = LD16(offa);
        if (NONEBAD(ga[3] ^ tex)) break;
      }
      const float aux = __uint_as_float(ga[2]), axn = dpp<XOR1>(aux);
      const float mj = (lane & 1) ? axn : aux, lj = (lane & 1) ? aux : axn;
      int mk = fkey(mj);
      mk = imax(mk, dppi<XOR8>(mk)); mk = x16_imax(mk); mk = x32_imax(mk);
      const float Mw = funkey(mk);
      const float wt = __builtin_amdgcn_exp2f(mj - Mw);
      float o0 = __uint_as_float(ga[0]) * wt, o1 = __uint_as_float(ga[1]) * wt, ll = lj * wt;
      o0 += dpp<XOR8>(o0); o1 += dpp<XOR8>(o1); ll += dpp<XOR8>(ll);
      o0 = x16_sum(o0); o1 = x16_sum(o1); ll = x16_sum(ll);
      o0 = x32_sum(o0); o1 = x32_sum(o1); ll = x32_sum(ll);
      if (lane < 9) { f32x2 o2; o2[0] = lane < 8 ? o0 : Mw; o2[1] = lane < 8 ? o1 : ll; *(LAS f32x2*)(L + aOw) = o2; }
    }
    BARRIER();
    {
      f32x4 ou[4]; f32x2 ml[4]; s16x4 a[4];
#pragma unroll
      for (int u = 0; u < 4; ++u) { ml[u] = *(const LAS f32x2*)(L + L_OP + u * 80 + 64); ou[u] = *(const LAS f32x4*)(L + aOr + u * 80); }
#pragma unroll
      for (int t = 0; t < 4; ++t) a[t] = trd(L + aWo[t]);
      f32x2 mv = *(const LAS f32x2*)(L + aMp);
      __builtin_amdgcn_sched_barrier(0);
      if (!act) { mv[0] = 0.f; mv[1] = 0.f; }
      const float M = funkey(imax(imax(fkey(ml[0][0]), fkey(ml[1][0])), imax(fkey(ml[2][0]), fkey(ml[3][0]))));
      float f[4];
#pragma unroll
      for (int u = 0; u < 4; ++u) f[u] = __builtin_amdgcn_exp2f(ml[u][0] - M);
      const float li = __builtin_amdgcn_rcpf((f[0] * ml[0][1] + f[1] * ml[1][1]) + (f[2] * ml[2][1] + f[3] * ml[3][1]));
      const f32x4 o4 = ((ou[0] * f[0] + ou[1] * f[1]) + (ou[2] * f[2] + ou[3] * f[3])) * li;
      v2u of; of[0] = pkbf(o4[0], o4[1]); of[1] = pkbf(o4[2], o4[3]);
      const f32x4 z4 = {0.f, 0.f, 0.f, 0.f};
      v2u om[4];
#pragma unroll
      for (int t = 0; t < 4; ++t) { om[t][0] = of[0] & tm[t]; om[t][1] = of[1] & tm[t]; }
      const f32x4 d = mfma16(a[1], om[1], mfma16(a[0], om[0], z4)) + mfma16(a[3], om[3], mfma16(a[2], om[2], z4));
      v4u gr; gr[0] = __float_as_uint(c < 8 ? (prs ? d[2] : d[0]) : mv[0]); gr[1] = tlo; gr[2] = __float_as_uint(c < 8 ? (prs ? d[3] : d[1]) : mv[1]); gr[3] = thi;
      ST16(gr, soff);
    }
    {
      DELAY(DLY_S);
      v4u gq;
      for (unsigned spin = 0; spin < SPIN_MAX; ++spin) {
        asm volatile("" ::: "memory");
        gq = LD16(foff);
        if (NONEBAD(TAGBAD(gq))) break;
      }
      float a0 = __uint_as_float(gq[0]), a1 = __uint_as_float(gq[2]);
      a0 += dpp<XOR2>(a0); a1 += dpp<XOR2>(a1); a0 += dpp<ROR4>(a0); a1 += dpp<ROR4>(a1); a0 += dpp<XOR8>(a0); a1 += dpp<XOR8>(a1);
      a0 = x16_sum(a0); a1 = x16_sum(a1); a0 = x32_sum(a0); a1 = x32_sum(a1);
      if (lane < 2) { f32x2 o2; o2[0] = a0; o2[1] = a1; *(LAS f32x2*)(L + L_FS + lane * 64 + w * 8) = o2; }
    }
    BARRIER();
    if (w == 0) {
      const int e = (lane >> 3) & 1, u = lane & 7;
      const unsigned xw = *(const LAS unsigned*)(L + L_XB), b2w = *(const LAS unsigned*)(L + L_XB + 4);
      float s = *(const LAS float*)(L + L_FS + u * 8 + e * 4), m = *(const LAS float*)(L + L_FM + u * 8 + e * 4);
      s += dpp<XOR1>(s); m += dpp<XOR1>(m); s += dpp<XOR2>(s); m += dpp<XOR2>(m); s += dpp<XOR7>(s); m += dpp<XOR7>(m);
      const float z = rbf(rbf(m) + (e ? bfhi(b2w) : bflo(b2w)));
      const float gt = rbf(z * __builtin_amdgcn_rcpf(1.f + __builtin_amdgcn_exp2f(-1.4426950408889634f * z)));
      const float r = (e ? bfhi(xw) : bflo(xw)) + rbf(rbf(s) * gt);
      const float rn = dpp<XOR8>(r);
      if (lane == 0) {
        *(GAS unsigned*)(p.out + col) = pkbf(r, rn);
        if (b == 0) { const unsigned long long T = (unsigned long long)tlo | ((unsigned long long)thi << 32); __hip_atomic_store((GAS unsigned long long*)(p.ws + W_E), T, __ATOMIC_RELAXED, __HIP_MEMORY_SCOPE_AGENT); }
      }
    }
  }
}

extern "C" void kernel_launch(void* const* d_in, const int* in_sizes, int n_in,
                              void* d_out, int out_size, void* d_ws, size_t ws_size,
                              hipStream_t stream) {
  Params p;
  p.x = (const u16*)d_in[0]; p.kc = (const u16*)d_in[1]; p.vc = (const u16*)d_in[2]; p.wq = (const u16*)d_in[3]; p.wo = (const u16*)d_in[4];
  p.w1 = (const u16*)d_in[6]; p.b1 = (const u16*)d_in[7]; p.w2 = (const u16*)d_in[8]; p.b2 = (const u16*)d_in[9];
  p.out = (u16*)d_out; p.ws = (unsigned char*)d_ws;
  fused_kernel<<<NB, NTHR, 0, stream>>>(p);
}
```

```cpp
#define KN_TS 14
#include <hip/hip_runtime.h>
#include <hip/hip_bf16.h>

#define LAS __attribute__((address_space(3)))
#define GAS __attribute__((address_space(1)))
typedef unsigned short u16;
typedef unsigned v4u __attribute__((ext_vector_type(4)));
typedef unsigned v2u __attribute__((ext_vector_type(2)));
typedef float f32x4 __attribute__((ext_vector_type(4)));
typedef float f32x2 __attribute__((ext_vector_type(2)));
typedef short s16x4 __attribute__((ext_vector_type(4)));
typedef short s16x8 __attribute__((ext_vector_type(8)));
typedef __bf16 bf16x8 __attribute__((ext_vector_type(8)));
typedef __bf16 bf16x2 __attribute__((ext_vector_type(2)));

constexpr int DLY_Q = 24, DLY_O = 24, DLY_S = 30;
constexpr int LWIN = 24;

constexpr int D = 512, SK = 65536, WIN = 4096, FF = 1792;
constexpr int NB = 256, NTHR = 640;
constexpr int L_WQ = 0, L_K = 16384, L_V = 49152, L_WO = 81920, L_W2 = 98304, L_W1 = 106496, L_XN = 114688, L_Y = 115712, L_Q = 116736, L_P = 117760;
constexpr int L_MX = 118784, L_SM = L_MX + 128, L_TAG = L_SM + 128, L_OP = L_TAG + 16, L_FS = L_OP + 512, L_FM = L_FS + 64, L_B1 = L_FM + 64, L_XB = L_B1 + 16, L_FLG = L_XB + 16, L_MP = L_FLG + 32, L_END = L_MP + 2048;
constexpr int W_E = 0;
constexpr int W_Q = 4096;
constexpr int W_Z = W_Q + 131072;
constexpr int W_O = W_Z + 262144;
constexpr int W_S = W_O + 1048576;
constexpr int W_TOUCH = W_S + 2097152;
constexpr int W_END = W_TOUCH + 4096;
constexpr unsigned SPIN_MAX = 1u << 17;

__device__ __forceinline__ float bflo(unsigned w) { return __uint_as_float(w << 16); }
__device__ __forceinline__ float bfhi(unsigned w) { return __uint_as_float(w & 0xffff0000u); }
__device__ __forceinline__ unsigned pkbf(float lo, float hi) { bf16x2 r; r[0] = (__bf16)lo; r[1] = (__bf16)hi; return __builtin_bit_cast(unsigned, r); }
__device__ __forceinline__ float rbf(float f) { return bflo(pkbf(f, f)); }
template <int CTRL> __device__ __forceinline__ float dpp(float x) {
  return __builtin_bit_cast(float, __builtin_amdgcn_mov_dpp(__builtin_bit_cast(int, x), CTRL, 0xf, 0xf, true));
}
constexpr int XOR1 = 0xB1, XOR2 = 0x4E, XOR7 = 0x141, XOR15 = 0x140, XOR8 = 0x128, ROR4 = 0x124;
__device__ __forceinline__ int fkey(float f) { const int i = __float_as_int(f); return i ^ ((i >> 31) & 0x7fffffff); }
__device__ __forceinline__ float funkey(int k) { return __int_as_float(k ^ ((k >> 31) & 0x7fffffff)); }
template <int CTRL> __device__ __forceinline__ int dppi(int x) { return __builtin_amdgcn_mov_dpp(x, CTRL, 0xf, 0xf, true); }
__device__ __forceinline__ int imax(int a, int b) { return a > b ? a : b; }
__device__ __forceinline__ int x16_imax(int x) { auto s = __builtin_amdgcn_permlane16_swap((unsigned)x, (unsigned)x, false, false); return imax((int)s[0], (int)s[1]); }
__device__ __forceinline__ int x32_imax(int x) { auto s = __builtin_amdgcn_permlane32_swap((unsigned)x, (unsigned)x, false, false); return imax((int)s[0], (int)s[1]); }
__device__ __forceinline__ float x16_sum(float x) { auto s = __builtin_amdgcn_permlane16_swap(__float_as_uint(x), __float_as_uint(x), false, false); return __uint_as_float(s[0]) + __uint_as_float(s[1]); }
__device__ __forceinline__ float x32_sum(float x) { auto s = __builtin_amdgcn_permlane32_swap(__float_as_uint(x), __float_as_uint(x), false, false); return __uint_as_float(s[0]) + __uint_as_float(s[1]); }
__device__ __forceinline__ void wave_sum2(float& a, float& b) {
  a += dpp<XOR1>(a); b += dpp<XOR1>(b); a += dpp<XOR2>(a); b += dpp<XOR2>(b); a += dpp<XOR7>(a); b += dpp<XOR7>(b); a += dpp<XOR15>(a); b += dpp<XOR15>(b);
  a = x16_sum(a); b = x16_sum(b); a = x32_sum(a); b = x32_sum(b);
}
__device__ __forceinline__ f32x4 mfma32(v4u a, v4u b, f32x4 c) {
  return __builtin_amdgcn_mfma_f32_16x16x32_bf16(__builtin_bit_cast(bf16x8, a), __builtin_bit_cast(bf16x8, b), c, 0, 0, 0);
}
__device__ __forceinline__ f32x4 mfma16(s16x4 a, v2u b, f32x4 c) {
  return __builtin_amdgcn_mfma_f32_16x16x16bf16_1k(a, __builtin_bit_cast(s16x4, b), c, 0, 0, 0);
}
__device__ __forceinline__ s16x4 trd(LAS unsigned char* p) { return __builtin_amdgcn_ds_read_tr16_b64_v4i16((LAS s16x4*)p); }
__device__ __forceinline__ v4u cat(s16x4 lo, s16x4 hi) { return __builtin_bit_cast(v4u, __builtin_shufflevector(lo, hi, 0, 1, 2, 3, 4, 5, 6, 7)); }
__device__ __forceinline__ v4u ln_packed(v4u x) {
  float v[8];
#pragma unroll
  for (int i = 0; i < 4; ++i) { v[2 * i] = bflo(x[i]); v[2 * i + 1] = bfhi(x[i]); }
  const float piv = __uint_as_float(__builtin_amdgcn_readfirstlane(x[0]) << 16);
  float s = 0.f, q = 0.f;
#pragma unroll
  for (int i = 0; i < 8; ++i) { const float d = v[i] - piv; s += d; q = fmaf(d, d, q); }
  wave_sum2(s, q);
  const float md = s * (1.0f / D);
  const float var = rbf(fmaxf(fmaf(-md, md, q * (1.0f / D)), 0.f));
  const float m = rbf(piv + md);
  const float sd = rbf(__builtin_amdgcn_sqrtf(rbf(var + 1e-5f)));
  const float inv = __builtin_amdgcn_rcpf(sd);
  v4u r;
#pragma unroll
  for (int i = 0; i < 4; ++i) r[i] = pkbf(rbf(v[2 * i] - m) * inv, rbf(v[2 * i + 1] - m) * inv);
  return r;
}
#define WAIT_VM(n) asm volatile("s_waitcnt vmcnt(" #n ")" ::: "memory")
#define WAIT_VMN(n) asm volatile("s_waitcnt vmcnt(%0)" :: "i"(n) : "memory")
static_assert(LWIN % 4 == 0 && LWIN >= 8 && LWIN <= 36, "loader window");
#define BARRIER() asm volatile("s_waitcnt lgkmcnt(0)\n\ts_barrier" ::: "memory")
#define RAWBAR() asm volatile("s_barrier" ::: "memory")
__device__ __forceinline__ void glds4(const void* sbase, unsigned v0, unsigned v1, unsigned v2, unsigned v3, unsigned ldsaddr) {
  unsigned keep;
  asm volatile("s_mov_b32 %0, m0\n\ts_mov_b32 m0, %6\n\ts_nop 0\n\tglobal_load_lds_dwordx4 %1, %5 nt\n\tglobal_load_lds_dwordx4 %2, %5 offset:1024 nt\n\t"
               "global_load_lds_dwordx4 %3, %5 offset:2048 nt\n\tglobal_load_lds_dwordx4 %4, %5 offset:3072 nt\n\ts_mov_b32 m0, %0"
               : "=&s"(keep) : "v"(v0), "v"(v1), "v"(v2), "v"(v3), "s"(sbase), "s"(ldsaddr) : "memory");
}
#define DELAY(n) do { if ((n) > 0) __builtin_amdgcn_s_sleep((n) > 127 ? 127 : (n)); } while (0)
#define LD16(off) __builtin_amdgcn_raw_buffer_load_b128(rs, (off), 0, 16)
#define ST16(v, off) __builtin_amdgcn_raw_buffer_store_b128((v), rs, (off), 0, 16)
#define TAGBAD(x) (((x)[1] ^ tlo) | ((x)[3] ^ thi))
#define NONEBAD(v) (__builtin_amdgcn_ballot_w64((v) != 0u) == 0ull)
#define PIN(x) asm volatile("" : "+v"(x))
__device__ __forceinline__ void lds_add(LAS unsigned* q, unsigned v) { __hip_atomic_fetch_add(q, v, __ATOMIC_RELAXED, __HIP_MEMORY_SCOPE_WORKGROUP); }
__device__ __forceinline__ unsigned lds_peek(LAS unsigned* q) { return __builtin_amdgcn_readfirstlane(__hip_atomic_load(q, __ATOMIC_RELAXED, __HIP_MEMORY_SCOPE_WORKGROUP)); }

constexpr int first_group(int need) { for (int g = 6; g < 22; ++g) if (4 * g - (LWIN - 4) >= need) return g; return 99; }

struct Params {
  const u16* x; const u16* kc; const u16* vc; const u16* wq; const u16* wo;
  const u16* w1; const u16* b1; const u16* w2; const u16* b2; u16* out; unsigned char* ws;
};

__global__ void __launch_bounds__(NTHR, 3) fused_kernel(Params p) {
  __shared__ __attribute__((aligned(16))) unsigned char lds[L_END];
  LAS unsigned char* L = (LAS unsigned char*)lds;
  const unsigned lbase = (unsigned)(size_t)L;
  const int tid = threadIdx.x, lane = tid & 63, wave = __builtin_amdgcn_readfirstlane(tid >> 6);
  const int b = blockIdx.x, h = b & 7, j = b >> 3;
  const int g = lane >> 4, c = lane & 15;
  asm volatile("" :: "s"(p.x), "s"(p.kc), "s"(p.vc), "s"(p.wq), "s"(p.wo), "s"(p.w1), "s"(p.b1), "s"(p.w2), "s"(p.b2), "s"(p.out), "s"(p.ws));
  const __amdgpu_buffer_rsrc_t rs = __builtin_amdgcn_make_buffer_rsrc((void*)p.ws, 0, W_END, 0x00020000);
  const bool act = j < 28;
  const int cg = h * 28 + (act ? j : j - 28);
  const int cc_cg = h * 4 + (j >> 3), cc_i = j & 7;
  const int col = 16 * cc_cg + 2 * cc_i;

  LAS unsigned* flg = (LAS unsigned*)(L + L_FLG);
  if (tid < 2) { v4u z = {0u, 0u, 0u, 0u}; *(LAS v4u*)(L + L_FLG + tid * 16) = z; }
  if (wave == 0) {
    const int lc = j >> 2, rq = j & 3, lm = cg >> 3, kq = cg & 7;
    __builtin_amdgcn_s_setprio(3);
    const unsigned kvoff = ((unsigned)h * SK + (SK - WIN) + (unsigned)j * 128u) * 256u;
    const unsigned char* kb = (const unsigned char*)p.kc + kvoff;
    const unsigned char* vb = (const unsigned char*)p.vc + kvoff;
    const unsigned k0 = (unsigned)g * 256u + (unsigned)((c ^ g) * 16), v0 = (unsigned)g * 256u + (unsigned)((c ^ (g << 2)) * 16), l16 = (unsigned)lane * 16u;
    BARRIER();
    {
      const unsigned ph = (unsigned)(lane >> 4) & 1u, c8 = (unsigned)lane & 7u;
      const unsigned ce = (c8 ^ (ph << 1)) * 16u, co = (c8 ^ ((ph | 2u) << 1)) * 16u;
      const unsigned rq8 = (unsigned)(lane >> 3) * 8192u, r18 = (unsigned)(lane >> 3) * (unsigned)(FF * 2);
      const unsigned char* sq = (const unsigned char*)p.wq + (unsigned)((h * 512 + lc * 64) * 2 + rq * 128 * 8192);
      const unsigned char* s1 = (const unsigned char*)p.w1 + (unsigned)(lm * 128 + kq * 64 * (FF * 2));
      constexpr unsigned SQ = 8u * 8192u - 1024u, S1 = 8u * (FF * 2) - 1024u;
#pragma unroll 1
      for (int n = 0; n < 4; ++n) glds4(sq + (unsigned)(n * 32 * 8192), rq8 + ce, rq8 + co + SQ, rq8 + ce + 2u * SQ, rq8 + co + 3u * SQ, lbase + L_WQ + n * 4096);
#pragma unroll 1
      for (int n = 0; n < 2; ++n) glds4(s1 + (unsigned)(n * 32 * (FF * 2)), r18 + ce, r18 + co + S1, r18 + ce + 2u * S1, r18 + co + 3u * S1, lbase + L_W1 + n * 4096);
    }
    constexpr int GFQ = first_group(16), GF1 = first_group(24), GFK = first_group(56);
    static_assert(GFQ <= 21 && GF1 <= 21 && GFK <= 21, "flags are set inside the K and V loops");
#define GROUP_FLAGS(gi) do { \
      if ((gi) == GFQ && lane == 0) __hip_atomic_store(flg + 0, 1u, __ATOMIC_RELAXED, __HIP_MEMORY_SCOPE_WORKGROUP); \
      if ((gi) == GF1 && lane == 0) __hip_atomic_store(flg + 2, 1u, __ATOMIC_RELAXED, __HIP_MEMORY_SCOPE_WORKGROUP); \
      if ((gi) == GFK && lane == 0) __hip_atomic_store(flg + 5, 1u, __ATOMIC_RELAXED, __HIP_MEMORY_SCOPE_WORKGROUP); } while (0)
#pragma unroll 1
    for (int n = 0; n < 8; ++n) { const int m = (n + j) & 7; WAIT_VMN(LWIN - 4); GROUP_FLAGS(6 + n); glds4(kb + m * 4096, k0, k0 ^ 64u, k0 ^ 128u, k0 ^ 192u, lbase + L_K + m * 4096); }
#pragma unroll 1
    for (int n = 0; n < 8; ++n) { const int m = (n + j) & 7; WAIT_VMN(LWIN - 4); GROUP_FLAGS(14 + n); glds4(vb + m * 4096, v0, v0 ^ 16u, v0 ^ 32u, v0 ^ 48u, lbase + L_V + m * 4096); }
    WAIT_VMN(32);
    RAWBAR();
    RAWBAR();
#pragma unroll 1
    for (int n = 0; n < 2; ++n) glds4((const unsigned char*)p.w2 + (unsigned)(cg * 8192 + n * 4096), l16, l16, l16, l16, lbase + L_W2 + n * 4096);
#pragma unroll 1
    for (int n0 = 0; n0 < 4; ++n0) {
      const int n = (n0 + j) & 3;
      const unsigned r0 = (unsigned)((4 * n) & 7) << 5;
      glds4((const unsigned char*)p.wo + (unsigned)((h * 512 + j * 16 + 4 * n) * 1024), l16 ^ r0, l16 ^ (r0 + 32u), l16 ^ (r0 + 64u), l16 ^ (r0 + 96u), lbase + L_WO + n * 4096);
    }
    WAIT_VMN(24);
    RAWBAR();
    WAIT_VMN(16);
    if (lane == 0) __hip_atomic_store(flg + 4, 1u, __ATOMIC_RELAXED, __HIP_MEMORY_SCOPE_WORKGROUP);
    WAIT_VM(0);
    RAWBAR();
  } else if (wave == 9) {
    const int lm = cg >> 3, kq = cg & 7;
    const int tsel = (c >> 1) & 3, prs = c & 1;
    {
      v4u xr;
      asm volatile("global_load_dwordx4 %0, %1, %2" : "=v"(xr) : "v"((unsigned)lane * 16u), "s"(p.x) : "memory");
      RAWBAR();
      __builtin_amdgcn_s_sleep(KN_TS);
      const unsigned touch = __builtin_amdgcn_raw_buffer_load_b32(rs, W_TOUCH + (lane & 7) * 4, 0, 16);
      const unsigned long long a_b1 = (unsigned long long)(p.b1 + cg * 8), a_x = (unsigned long long)(p.x + col), a_b2 = (unsigned long long)(p.b2 + col);
      const unsigned long long a_sm = lane < 4 ? a_b1 + lane * 4 : (lane == 4 ? a_x : a_b2);
      const unsigned small = *(const GAS unsigned*)a_sm;
      __builtin_amdgcn_sched_barrier(0);
      asm volatile("s_waitcnt vmcnt(2)" : "+v"(xr) :: "memory");
      __builtin_amdgcn_sched_barrier(0);
      *(LAS v4u*)(L + L_Y + lane * 16) = ln_packed(ln_packed(xr));
      if (lane < 6) *(LAS unsigned*)(L + L_B1 + lane * 4) = small;
      asm volatile("" :: "v"(touch));
    }
    const int zoff = W_Z + ((lm * 8 + kq) * 8 + ((lane & 31) >> 2)) * 128 + (lane & 3) * 16;
    {
      const int q4 = c >> 2, pp = c & 3;
      const int ph = ((q4 >> 1) & 1) | ((g & 1) << 1);
      v4u yf[2];
#pragma unroll
      for (int s = 0; s < 2; ++s) yf[s] = *(const LAS v4u*)(L + L_Y + (64 * kq + 32 * s + 8 * g) * 2);
      for (unsigned spin = 0; spin < SPIN_MAX; ++spin) {
        const v4u f = *(volatile LAS v4u*)flg;
        asm volatile("" ::: "memory");
        if (__builtin_amdgcn_readfirstlane(f[1] & f[2]) != 0u) break;
        __builtin_amdgcn_s_sleep(1);
      }
      const v2u tg = *(const LAS v2u*)(L + L_TAG);
      f32x4 a0 = {0.f, 0.f, 0.f, 0.f};
#pragma unroll
      for (int t = 0; t < 4; ++t) {
        LAS unsigned char* ab = L + L_W1 + (8 * g + q4) * 128 + (((2 * t + (pp >> 1)) ^ (ph << 1)) * 16) + (pp & 1) * 8;
        const unsigned m = tsel == t ? ~0u : 0u;
#pragma unroll
        for (int s = 0; s < 2; ++s) { v4u ym; ym[0] = yf[s][0] & m; ym[1] = yf[s][1] & m; ym[2] = yf[s][2] & m; ym[3] = yf[s][3] & m; a0 = mfma32(cat(trd(ab + s * 4096), trd(ab + s * 4096 + 512)), ym, a0); }
      }
      v4u gr; gr[0] = __float_as_uint(prs ? a0[2] : a0[0]); gr[1] = tg[0]; gr[2] = __float_as_uint(prs ? a0[3] : a0[1]); gr[3] = tg[1];
      if (c < 8 && act) ST16(gr, W_Z + ((lm * 8 + 2 * tsel + (g >> 1)) * 8 + kq) * 128 + (2 * (g & 1) + prs) * 16);
    }
    DELAY(DLY_Q + 8);
    v4u zq;
    asm volatile("global_load_dwordx4 %0, %1, %2 sc1" : "=v"(zq) : "v"(zoff), "s"(p.ws) : "memory");
    RAWBAR();
    RAWBAR();
    asm volatile("s_waitcnt vmcnt(0)" : "+v"(zq) :: "memory");
    v2u hm[4];
    if (act) {
      const v2u tg = *(const LAS v2u*)(L + L_TAG);
      const unsigned tlo = tg[0], thi = tg[1];
      if (!NONEBAD(TAGBAD(zq))) {
        for (unsigned spin = 0; spin < SPIN_MAX; ++spin) {
          asm volatile("" ::: "memory");
          zq = LD16(zoff);
          if (NONEBAD(TAGBAD(zq))) break;
        }
      }
      float z0 = __uint_as_float(zq[0]), z1 = __uint_as_float(zq[2]);
      z0 += dpp<ROR4>(z0); z1 += dpp<ROR4>(z1); z0 += dpp<XOR8>(z0); z1 += dpp<XOR8>(z1); z0 = x16_sum(z0); z1 = x16_sum(z1);
      const unsigned bw = *(const LAS unsigned*)(L + L_B1 + (lane & 3) * 4);
      float hv[2];
#pragma unroll
      for (int e = 0; e < 2; ++e) {
        const float z = rbf(rbf(e ? z1 : z0) + (e ? bfhi(bw) : bflo(bw)));
        const float uu = 0.7978845608028654f * (z + 0.044715f * z * z * z);
        const float th = 1.f - 2.f * __builtin_amdgcn_rcpf(1.f + __builtin_amdgcn_exp2f(2.885390081777927f * uu));
        hv[e] = 0.5f * z * (1.f + th);
      }
      const unsigned hp = pkbf(hv[0], hv[1]);
      const unsigned h0 = __builtin_amdgcn_readlane(hp, 0), h1 = __builtin_amdgcn_readlane(hp, 1), h2 = __builtin_amdgcn_readlane(hp, 2), h3 = __builtin_amdgcn_readlane(hp, 3);
#pragma unroll
      for (int t = 0; t < 4; ++t) { const bool on = tsel == t && g < 2; hm[t][0] = on ? (g == 0 ? h0 : h2) : 0u; hm[t][1] = on ? (g == 0 ? h1 : h3) : 0u; }
    }
    RAWBAR();
    if (act) {
      for (unsigned spin = 0; spin < SPIN_MAX && lds_peek(flg + 4) == 0u; ++spin) __builtin_amdgcn_s_sleep(1);
      LAS unsigned char* ab = L + L_W2 + ((4 * g + (c >> 2)) & 7) * 1024 + (c & 3) * 8;
      LAS unsigned char* mb = L + L_MP + (16 * tsel + 4 * g + 2 * prs) * 4;
#pragma unroll 2
      for (int T = 0; T < 8; ++T) {
        f32x4 d = {0.f, 0.f, 0.f, 0.f};
#pragma unroll
        for (int t = 0; t < 4; ++t) d = mfma16(trd(ab + (4 * T + t) * 32), hm[t], d);
        f32x2 o2; o2[0] = prs ? d[2] : d[0]; o2[1] = prs ? d[3] : d[1];
        if (c < 8) *(LAS f32x2*)(mb + T * 256) = o2;
      }
    }
    BARRIER();
  } else {
    const int w = wave - 1;
    __builtin_amdgcn_s_setprio(2);
    const int lc = j >> 2, rq = j & 3;
    if (w == 0) {
      v4u xr;
      asm volatile("global_load_dwordx4 %0, %1, %2" : "=v"(xr) : "v"((unsigned)lane * 16u), "s"(p.x) : "memory");
      RAWBAR();
      __builtin_amdgcn_s_sleep(KN_TS);
      const v2u ev = __builtin_amdgcn_raw_buffer_load_b64(rs, W_E, 0, 16);
      __builtin_amdgcn_sched_barrier(0);
      asm volatile("s_waitcnt vmcnt(1)" : "+v"(xr) :: "memory");
      __builtin_amdgcn_sched_barrier(0);
      *(LAS v4u*)(L + L_XN + lane * 16) = ln_packed(xr);
      const unsigned long long E = (unsigned long long)(unsigned)__builtin_amdgcn_readfirstlane(ev[0]) | ((unsigned long long)(unsigned)__builtin_amdgcn_readfirstlane(ev[1]) << 32);
      const unsigned long long T = E * 6364136223846793005ull + 1442695040888963407ull;
      if (lane == 0) { v2u tg; tg[0] = (unsigned)T; tg[1] = (unsigned)(T >> 32); *(LAS v2u*)(L + L_TAG) = tg; }
      asm volatile("s_waitcnt lgkmcnt(0)" ::: "memory");
      if (lane == 0) __hip_atomic_store(flg + 1, 1u, __ATOMIC_RELAXED, __HIP_MEMORY_SCOPE_WORKGROUP);
    } else {
      RAWBAR();
    }
    unsigned tlo, thi;
    {
      const int q4 = c >> 2, pp = c & 3, t = w & 3;
      int toff = L_WQ + (8 * g + q4) * 128 + (((2 * t + (pp >> 1)) ^ ((((q4 >> 1) & 1) | ((g & 1) << 1)) << 1)) * 16) + (pp & 1) * 8;
      int xoff = L_XN + (128 * rq + 8 * g) * 2;
      int off = W_Q + ((((h * 8 + lc) * 4 + rq) * 16) + 4 * t + g) * 16;
      PIN(toff); PIN(xoff); PIN(off);
      for (unsigned spin = 0; spin < SPIN_MAX && lds_peek(flg + 1) == 0u; ++spin) __builtin_amdgcn_s_sleep(1);
      const v2u tg = *(const LAS v2u*)(L + L_TAG);
      tlo = tg[0]; thi = tg[1];
      if (w < 4) {
        v4u xf[4];
#pragma unroll
        for (int s = 0; s < 4; ++s) xf[s] = *(const LAS v4u*)(L + xoff + s * 64);
        for (unsigned spin = 0; spin < SPIN_MAX && lds_peek(flg + 0) == 0u; ++spin) { }
        s16x4 a[4][2];
#pragma unroll
        for (int s = 0; s < 4; ++s) { a[s][0] = trd(L + toff + s * 4096); a[s][1] = trd(L + toff + s * 4096 + 512); }
        __builtin_amdgcn_sched_barrier(0);
        const f32x4 z4 = {0.f, 0.f, 0.f, 0.f};
        const f32x4 a0 = mfma32(cat(a[1][0], a[1][1]), xf[1], mfma32(cat(a[0][0], a[0][1]), xf[0], z4)) + mfma32(cat(a[3][0], a[3][1]), xf[3], mfma32(cat(a[2][0], a[2][1]), xf[2], z4));
        v4u gr; gr[0] = pkbf(a0[0], a0[1]); gr[1] = tlo; gr[2] = pkbf(a0[2], a0[3]); gr[3] = thi;
        if (c == 0) ST16(gr, off);
        if (lane == 0) lds_add(flg + 3, 1u);
      } else {
        for (unsigned spin = 0; spin < SPIN_MAX && lds_peek(flg + 3) < 4u; ++spin) __builtin_amdgcn_s_sleep(1);
      }
    }
    const int head = c & 3, kk = c >> 2, q4 = c >> 2, pp = c & 3, tsel = (c >> 1) & 3, prs = c & 1;
    int qoff = W_Q + (h * 8 + w) * 1024 + lane * 16;
    int aQw; { const int dim = (w & 1) * 64 + 4 * (lane & 15); aQw = L_Q + (((dim >> 5) * 4 + ((dim >> 3) & 3)) * 4 + (w >> 1)) * 16 + (dim & 7) * 2; }
    int aK[4];
#pragma unroll
    for (int s = 0; s < 4; ++s) aK[s] = L_K + (w * 16 + c) * 256 + (((4 * s + g) ^ c) * 16);
    int aQr = L_Q + (g * 4 + head) * 16;
    int aMw = L_MX + ((lane & 3) * 8 + w) * 4;
    int aMr = L_MX + head * 32;
    int aPw = L_P + (((w >> 1) * 4 + 2 * (w & 1) + (g >> 1)) * 4 + head) * 16 + (g & 1) * 8;
    int aPr = L_P + (g * 4 + head) * 16;
    int aV[2];
#pragma unroll
    for (int t2 = 0; t2 < 2; ++t2) aV[t2] = L_V + (8 * g + 4 * t2 + q4) * 256 + (((2 * w + (pp >> 1)) ^ ((q4 << 2) | ((2 * g + t2) & 3))) * 16) + (pp & 1) * 8;
    int ooff = W_O + ((((h * 32 + head * 8 + w) * 32 + j) * 8) + 2 * g + kk) * 16;
    int offa = W_O + (((h * 32 + j) * 32 + 8 * (w & 3)) * 8 + lane) * 16;
    int aOw = L_OP + (w & 3) * 80 + lane * 8, aOr = L_OP + g * 16;
    int aWo[4];
#pragma unroll
    for (int t = 0; t < 4; ++t) { const int row = 4 * g + (c >> 2); aWo[t] = L_WO + row * 1024 + ((2 * (4 * w + t) + ((c & 3) >> 1)) ^ ((row & 7) << 1)) * 16 + (c & 1) * 8; }
    int aMp = L_MP + (16 * (4 * w + tsel) + 4 * g + 2 * prs) * 4;
    int soff = W_S + (((2 * (4 * w + tsel) + (g >> 1)) * 256 + b) * 4 + 2 * (g & 1) + prs) * 32 + (c >> 3) * 16;
    int foff; { const int cc = 8 * cc_cg + cc_i; foff = W_S + (((cc >> 2) * 256 + 32 * w + (lane >> 1)) * 4 + (cc & 3)) * 32 + (lane & 1) * 16; }
    unsigned tex = (lane & 1) ? thi : tlo, otag = (kk & 1) ? thi : tlo;
    unsigned tm[4];
#pragma unroll
    for (int t = 0; t < 4; ++t) tm[t] = tsel == t ? ~0u : 0u;
    PIN(qoff); PIN(aQw); PIN(aK[0]); PIN(aK[1]); PIN(aK[2]); PIN(aK[3]); PIN(aQr); PIN(aMw); PIN(aMr); PIN(aPw); PIN(aPr); PIN(aV[0]); PIN(aV[1]);
    PIN(ooff); PIN(offa); PIN(aOw); PIN(aOr); PIN(aWo[0]); PIN(aWo[1]); PIN(aWo[2]); PIN(aWo[3]); PIN(aMp); PIN(soff); PIN(foff); PIN(tex); PIN(otag);
    PIN(tm[0]); PIN(tm[1]); PIN(tm[2]); PIN(tm[3]);
    DELAY(DLY_Q);
    v4u q0;
    for (unsigned spin = 0; spin < SPIN_MAX; ++spin) {
      asm volatile("" ::: "memory");
      q0 = LD16(qoff);
      if (NONEBAD(TAGBAD(q0))) break;
    }
    for (unsigned spin = 0; spin < SPIN_MAX && lds_peek(flg + 5) == 0u; ++spin) { }
    v4u kf[4];
#pragma unroll
    for (int s = 0; s < 4; ++s) kf[s] = *(const LAS v4u*)(L + aK[s]);
    {
      float u0 = bflo(q0[0]), u1 = bfhi(q0[0]), u2 = bflo(q0[2]), u3 = bfhi(q0[2]);
      u0 = x16_sum(u0); u1 = x16_sum(u1); u2 = x16_sum(u2); u3 = x16_sum(u3);
      u0 = x32_sum(u0); u1 = x32_sum(u1); u2 = x32_sum(u2); u3 = x32_sum(u3);
      v2u d; d[0] = pkbf(u0, u1); d[1] = pkbf(u2, u3);
      if (lane < 16) *(LAS v2u*)(L + aQw) = d;
    }
    BARRIER();
    f32x4 sc;
    {
      v4u qf[4];
#pragma unroll
      for (int s = 0; s < 4; ++s) qf[s] = *(const LAS v4u*)(L + aQr + s * 256);
      __builtin_amdgcn_sched_barrier(0);
      const f32x4 z4 = {0.f, 0.f, 0.f, 0.f};
      const f32x4 s0 = mfma32(kf[1], qf[1], mfma32(kf[0], qf[0], z4)), s1 = mfma32(kf[3], qf[3], mfma32(kf[2], qf[2], z4));
      sc = s0 + s1;
      int mx = imax(imax(fkey(sc[0]), fkey(sc[1])), imax(fkey(sc[2]), fkey(sc[3])));
      mx = x16_imax(mx); mx = x32_imax(mx);
      if (lane < 4) *(LAS int*)(L + aMw) = mx;
    }
    BARRIER();
    float mc;
    constexpr float C2 = 0.08838834764831845f * 1.4426950408889634f;
    {
      const v4u ma = *(const LAS v4u*)(L + aMr), mb = *(const LAS v4u*)(L + aMr + 16);
      const int M = imax(imax(imax((int)ma[0], (int)ma[1]), imax((int)ma[2], (int)ma[3])), imax(imax((int)mb[0], (int)mb[1]), imax((int)mb[2], (int)mb[3])));
      mc = funkey(M) * C2;
      float e[4];
#pragma unroll
      for (int r = 0; r < 4; ++r) e[r] = __builtin_amdgcn_exp2f(fmaf(sc[r], C2, -mc));
      v2u pk; pk[0] = pkbf(e[0], e[1]); pk[1] = pkbf(e[2], e[3]);
      if (c < 4) *(LAS v2u*)(L + aPw) = pk;
      float lsum = (bflo(pk[0]) + bfhi(pk[0])) + (bflo(pk[1]) + bfhi(pk[1]));
      lsum = x16_sum(lsum); lsum = x32_sum(lsum);
      if (lane < 4) *(LAS float*)(L + aMw + 128) = lsum;
    }
    BARRIER();
    {
      v4u pf[4]; s16x4 va[4][2];
#pragma unroll
      for (int s = 0; s < 4; ++s) { va[s][0] = trd(L + aV[0] + s * 8192); va[s][1] = trd(L + aV[1] + s * 8192); pf[s] = *(const LAS v4u*)(L + aPr + s * 256); }
      const f32x4 la = *(const LAS f32x4*)(L + aMr + 128), lb = *(const LAS f32x4*)(L + aMr + 144);
      __builtin_amdgcn_sched_barrier(0);
      const f32x4 z4 = {0.f, 0.f, 0.f, 0.f};
      const f32x4 o0 = mfma32(cat(va[1][0], va[1][1]), pf[1], mfma32(cat(va[0][0], va[0][1]), pf[0], z4));
      const f32x4 o1 = mfma32(cat(va[3][0], va[3][1]), pf[3], mfma32(cat(va[2][0], va[2][1]), pf[2], z4));
      const float l8 = ((la[0] + la[1]) + (la[2] + la[3])) + ((lb[0] + lb[1]) + (lb[2] + lb[3]));
      const f32x4 oa = o0 + o1;
      v4u gr; gr[0] = __float_as_uint((kk & 1) ? oa[2] : oa[0]); gr[1] = __float_as_uint((kk & 1) ? oa[3] : oa[1]); gr[2] = __float_as_uint((kk & 1) ? l8 : mc); gr[3] = otag;
      if (kk < 2) ST16(gr, ooff);
    }
    if (w < 4) {
      DELAY(DLY_O);
      v4u ga;
      for (unsigned spin = 0; spin < SPIN_MAX; ++spin) {
        asm volatile("" ::: "memory");
        ga = LD16(offa);
        if (NONEBAD(ga[3] ^ tex)) break;
      }
      const float aux = __uint_as_float(ga[2]), axn = dpp<XOR1>(aux);
      const float mj = (lane & 1) ? axn : aux, lj = (lane & 1) ? aux : axn;
      int mk = fkey(mj);
      mk = imax(mk, dppi<XOR8>(mk)); mk = x16_imax(mk); mk = x32_imax(mk);
      const float Mw = funkey(mk);
      const float wt = __builtin_amdgcn_exp2f(mj - Mw);
      float o0 = __uint_as_float(ga[0]) * wt, o1 = __uint_as_float(ga[1]) * wt, ll = lj * wt;
      o0 += dpp<XOR8>(o0); o1 += dpp<XOR8>(o1); ll += dpp<XOR8>(ll);
      o0 = x16_sum(o0); o1 = x16_sum(o1); ll = x16_sum(ll);
      o0 = x32_sum(o0); o1 = x32_sum(o1); ll = x32_sum(ll);
      if (lane < 9) { f32x2 o2; o2[0] = lane < 8 ? o0 : Mw; o2[1] = lane < 8 ? o1 : ll; *(LAS f32x2*)(L + aOw) = o2; }
    }
    BARRIER();
    {
      f32x4 ou[4]; f32x2 ml[4]; s16x4 a[4];
#pragma unroll
      for (int u = 0; u < 4; ++u) { ml[u] = *(const LAS f32x2*)(L + L_OP + u * 80 + 64); ou[u] = *(const LAS f32x4*)(L + aOr + u * 80); }
#pragma unroll
      for (int t = 0; t < 4; ++t) a[t] = trd(L + aWo[t]);
      f32x2 mv = *(const LAS f32x2*)(L + aMp);
      __builtin_amdgcn_sched_barrier(0);
      if (!act) { mv[0] = 0.f; mv[1] = 0.f; }
      const float M = funkey(imax(imax(fkey(ml[0][0]), fkey(ml[1][0])), imax(fkey(ml[2][0]), fkey(ml[3][0]))));
      float f[4];
#pragma unroll
      for (int u = 0; u < 4; ++u) f[u] = __builtin_amdgcn_exp2f(ml[u][0] - M);
      const float li = __builtin_amdgcn_rcpf((f[0] * ml[0][1] + f[1] * ml[1][1]) + (f[2] * ml[2][1] + f[3] * ml[3][1]));
      const f32x4 o4 = ((ou[0] * f[0] + ou[1] * f[1]) + (ou[2] * f[2] + ou[3] * f[3])) * li;
      v2u of; of[0] = pkbf(o4[0], o4[1]); of[1] = pkbf(o4[2], o4[3]);
      const f32x4 z4 = {0.f, 0.f, 0.f, 0.f};
      v2u om[4];
#pragma unroll
      for (int t = 0; t < 4; ++t) { om[t][0] = of[0] & tm[t]; om[t][1] = of[1] & tm[t]; }
      const f32x4 d = mfma16(a[1], om[1], mfma16(a[0], om[0], z4)) + mfma16(a[3], om[3], mfma16(a[2], om[2], z4));
      v4u gr; gr[0] = __float_as_uint(c < 8 ? (prs ? d[2] : d[0]) : mv[0]); gr[1] = tlo; gr[2] = __float_as_uint(c < 8 ? (prs ? d[3] : d[1]) : mv[1]); gr[3] = thi;
      ST16(gr, soff);
    }
    {
      DELAY(DLY_S);
      v4u gq;
      for (unsigned spin = 0; spin < SPIN_MAX; ++spin) {
        asm volatile("" ::: "memory");
        gq = LD16(foff);
        if (NONEBAD(TAGBAD(gq))) break;
      }
      float a0 = __uint_as_float(gq[0]), a1 = __uint_as_float(gq[2]);
      a0 += dpp<XOR2>(a0); a1 += dpp<XOR2>(a1); a0 += dpp<ROR4>(a0); a1 += dpp<ROR4>(a1); a0 += dpp<XOR8>(a0); a1 += dpp<XOR8>(a1);
      a0 = x16_sum(a0); a1 = x16_sum(a1); a0 = x32_sum(a0); a1 = x32_sum(a1);
      if (lane < 2) { f32x2 o2; o2[0] = a0; o2[1] = a1; *(LAS f32x2*)(L + L_FS + lane * 64 + w * 8) = o2; }
    }
    BARRIER();
    if (w == 0) {
      const int e = (lane >> 3) & 1, u = lane & 7;
      const unsigned xw = *(const LAS unsigned*)(L + L_XB), b2w = *(const LAS unsigned*)(L + L_XB + 4);
      float s = *(const LAS float*)(L + L_FS + u * 8 + e * 4), m = *(const LAS float*)(L + L_FM + u * 8 + e * 4);
      s += dpp<XOR1>(s); m += dpp<XOR1>(m); s += dpp<XOR2>(s); m += dpp<XOR2>(m); s += dpp<XOR7>(s); m += dpp<XOR7>(m);
      const float z = rbf(rbf(m) + (e ? bfhi(b2w) : bflo(b2w)));
      const float gt = rbf(z * __builtin_amdgcn_rcpf(1.f + __builtin_amdgcn_exp2f(-1.4426950408889634f * z)));
      const float r = (e ? bfhi(xw) : bflo(xw)) + rbf(rbf(s) * gt);
      const float rn = dpp<XOR8>(r);
      if (lane == 0) {
        *(GAS unsigned*)(p.out + col) = pkbf(r, rn);
        if (b == 0) { const unsigned long long T = (unsigned long long)tlo | ((unsigned long long)thi << 32); __hip_atomic_store((GAS unsigned long long*)(p.ws + W_E), T, __ATOMIC_RELAXED, __HIP_MEMORY_SCOPE_AGENT); }
      }
    }
  }
}

extern "C" void kernel_launch(void* const* d_in, const int* in_sizes, int n_in,
                              void* d_out, int out_size, void* d_ws, size_t ws_size,
                              hipStream_t stream) {
  Params p;
  p.x = (const u16*)d_in[0]; p.kc = (const u16*)d_in[1]; p.vc = (const u16*)d_in[2]; p.wq = (const u16*)d_in[3]; p.wo = (const u16*)d_in[4];
  p.w1 = (const u16*)d_in[6]; p.b1 = (const u16*)d_in[7]; p.w2 = (const u16*)d_in[8]; p.b2 = (const u16*)d_in[9];
  p.out = (u16*)d_out; p.ws = (unsigned char*)d_ws;
  fused_kernel<<<NB, NTHR, 0, stream>>>(p);
}
```

```cpp
#define KROT h
#include <hip/hip_runtime.h>
#include <hip/hip_bf16.h>

#define LAS __attribute__((address_space(3)))
#define GAS __attribute__((address_space(1)))
typedef unsigned short u16;
typedef unsigned v4u __attribute__((ext_vector_type(4)));
typedef unsigned v2u __attribute__((ext_vector_type(2)));
typedef float f32x4 __attribute__((ext_vector_type(4)));
typedef float f32x2 __attribute__((ext_vector_type(2)));
typedef short s16x4 __attribute__((ext_vector_type(4)));
typedef short s16x8 __attribute__((ext_vector_type(8)));
typedef __bf16 bf16x8 __attribute__((ext_vector_type(8)));
typedef __bf16 bf16x2 __attribute__((ext_vector_type(2)));

constexpr int DLY_Q = 24, DLY_O = 24, DLY_S = 30;
constexpr int LWIN = 24;

constexpr int D = 512, SK = 65536, WIN = 4096, FF = 1792;
constexpr int NB = 256, NTHR = 640;
constexpr int L_WQ = 0, L_K = 16384, L_V = 49152, L_WO = 81920, L_W2 = 98304, L_W1 = 106496, L_XN = 114688, L_Y = 115712, L_Q = 116736, L_P = 117760;
constexpr int L_MX = 118784, L_SM = L_MX + 128, L_TAG = L_SM + 128, L_OP = L_TAG + 16, L_FS = L_OP + 512, L_FM = L_FS + 64, L_B1 = L_FM + 64, L_XB = L_B1 + 16, L_FLG = L_XB + 16, L_MP = L_FLG + 32, L_END = L_MP + 2048;
constexpr int W_E = 0;
constexpr int W_Q = 4096;
constexpr int W_Z = W_Q + 131072;
constexpr int W_O = W_Z + 262144;
constexpr int W_S = W_O + 1048576;
constexpr int W_TOUCH = W_S + 2097152;
constexpr int W_END = W_TOUCH + 4096;
constexpr unsigned SPIN_MAX = 1u << 17;

__device__ __forceinline__ float bflo(unsigned w) { return __uint_as_float(w << 16); }
__device__ __forceinline__ float bfhi(unsigned w) { return __uint_as_float(w & 0xffff0000u); }
__device__ __forceinline__ unsigned pkbf(float lo, float hi) { bf16x2 r; r[0] = (__bf16)lo; r[1] = (__bf16)hi; return __builtin_bit_cast(unsigned, r); }
__device__ __forceinline__ float rbf(float f) { return bflo(pkbf(f, f)); }
template <int CTRL> __device__ __forceinline__ float dpp(float x) {
  return __builtin_bit_cast(float, __builtin_amdgcn_mov_dpp(__builtin_bit_cast(int, x), CTRL, 0xf, 0xf, true));
}
constexpr int XOR1 = 0xB1, XOR2 = 0x4E, XOR7 = 0x141, XOR15 = 0x140, XOR8 = 0x128, ROR4 = 0x124;
__device__ __forceinline__ int fkey(float f) { const int i = __float_as_int(f); return i ^ ((i >> 31) & 0x7fffffff); }
__device__ __forceinline__ float funkey(int k) { return __int_as_float(k ^ ((k >> 31) & 0x7fffffff)); }
template <int CTRL> __device__ __forceinline__ int dppi(int x) { return __builtin_amdgcn_mov_dpp(x, CTRL, 0xf, 0xf, true); }
__device__ __forceinline__ int imax(int a, int b) { return a > b ? a : b; }
__device__ __forceinline__ int x16_imax(int x) { auto s = __builtin_amdgcn_permlane16_swap((unsigned)x, (unsigned)x, false, false); return imax((int)s[0], (int)s[1]); }
__device__ __forceinline__ int x32_imax(int x) { auto s = __builtin_amdgcn_permlane32_swap((unsigned)x, (unsigned)x, false, false); return imax((int)s[0], (int)s[1]); }
__device__ __forceinline__ float x16_sum(float x) { auto s = __builtin_amdgcn_permlane16_swap(__float_as_uint(x), __float_as_uint(x), false, false); return __uint_as_float(s[0]) + __uint_as_float(s[1]); }
__device__ __forceinline__ float x32_sum(float x) { auto s = __builtin_amdgcn_permlane32_swap(__float_as_uint(x), __float_as_uint(x), false, false); return __uint_as_float(s[0]) + __uint_as_float(s[1]); }
__device__ __forceinline__ void wave_sum2(float& a, float& b) {
  a += dpp<XOR1>(a); b += dpp<XOR1>(b); a += dpp<XOR2>(a); b += dpp<XOR2>(b); a += dpp<XOR7>(a); b += dpp<XOR7>(b); a += dpp<XOR15>(a); b += dpp<XOR15>(b);
  a = x16_sum(a); b = x16_sum(b); a = x32_sum(a); b = x32_sum(b);
}
__device__ __forceinline__ f32x4 mfma32(v4u a, v4u b, f32x4 c) {
  return __builtin_amdgcn_mfma_f32_16x16x32_bf16(__builtin_bit_cast(bf16x8, a), __builtin_bit_cast(bf16x8, b), c, 0, 0, 0);
}
__device__ __forceinline__ f32x4 mfma16(s16x4 a, v2u b, f32x4 c) {
  return __builtin_amdgcn_mfma_f32_16x16x16bf16_1k(a, __builtin_bit_cast(s16x4, b), c, 0, 0, 0);
}
__device__ __forceinline__ s16x4 trd(LAS unsigned char* p) { return __builtin_amdgcn_ds_read_tr16_b64_v4i16((LAS s16x4*)p); }
__device__ __forceinline__ v4u cat(s16x4 lo, s16x4 hi) { return __builtin_bit_cast(v4u, __builtin_shufflevector(lo, hi, 0, 1, 2, 3, 4, 5, 6, 7)); }
__device__ __forceinline__ v4u ln_packed(v4u x) {
  float v[8];
#pragma unroll
  for (int i = 0; i < 4; ++i) { v[2 * i] = bflo(x[i]); v[2 * i + 1] = bfhi(x[i]); }
  const float piv = __uint_as_float(__builtin_amdgcn_readfirstlane(x[0]) << 16);
  float s = 0.f, q = 0.f;
#pragma unroll
  for (int i = 0; i < 8; ++i) { const float d = v[i] - piv; s += d; q = fmaf(d, d, q); }
  wave_sum2(s, q);
  const float md = s * (1.0f / D);
  const float var = rbf(fmaxf(fmaf(-md, md, q * (1.0f / D)), 0.f));
  const float m = rbf(piv + md);
  const float sd = rbf(__builtin_amdgcn_sqrtf(rbf(var + 1e-5f)));
  const float inv = __builtin_amdgcn_rcpf(sd);
  v4u r;
#pragma unroll
  for (int i = 0; i < 4; ++i) r[i] = pkbf(rbf(v[2 * i] - m) * inv, rbf(v[2 * i + 1] - m) * inv);
  return r;
}
#define WAIT_VM(n) asm volatile("s_waitcnt vmcnt(" #n ")" ::: "memory")
#define WAIT_VMN(n) asm volatile("s_waitcnt vmcnt(%0)" :: "i"(n) : "memory")
static_assert(LWIN % 4 == 0 && LWIN >= 8 && LWIN <= 36, "loader window");
#define BARRIER() asm volatile("s_waitcnt lgkmcnt(0)\n\ts_barrier" ::: "memory")
#define RAWBAR() asm volatile("s_barrier" ::: "memory")
__device__ __forceinline__ void glds4(const void* sbase, unsigned v0, unsigned v1, unsigned v2, unsigned v3, unsigned ldsaddr) {
  unsigned keep;
  asm volatile("s_mov_b32 %0, m0\n\ts_mov_b32 m0, %6\n\ts_nop 0\n\tglobal_load_lds_dwordx4 %1, %5 nt\n\tglobal_load_lds_dwordx4 %2, %5 offset:1024 nt\n\t"
               "global_load_lds_dwordx4 %3, %5 offset:2048 nt\n\tglobal_load_lds_dwordx4 %4, %5 offset:3072 nt\n\ts_mov_b32 m0, %0"
               : "=&s"(keep) : "v"(v0), "v"(v1), "v"(v2), "v"(v3), "s"(sbase), "s"(ldsaddr) : "memory");
}
#define DELAY(n) do { if ((n) > 0) __builtin_amdgcn_s_sleep((n) > 127 ? 127 : (n)); } while (0)
#define LD16(off) __builtin_amdgcn_raw_buffer_load_b128(rs, (off), 0, 16)
#define ST16(v, off) __builtin_amdgcn_raw_buffer_store_b128((v), rs, (off), 0, 16)
#define TAGBAD(x) (((x)[1] ^ tlo) | ((x)[3] ^ thi))
#define NONEBAD(v) (__builtin_amdgcn_ballot_w64((v) != 0u) == 0ull)
#define PIN(x) asm volatile("" : "+v"(x))
__device__ __forceinline__ void lds_add(LAS unsigned* q, unsigned v) { __hip_atomic_fetch_add(q, v, __ATOMIC_RELAXED, __HIP_MEMORY_SCOPE_WORKGROUP); }
__device__ __forceinline__ unsigned lds_peek(LAS unsigned* q) { return __builtin_amdgcn_readfirstlane(__hip_atomic_load(q, __ATOMIC_RELAXED, __HIP_MEMORY_SCOPE_WORKGROUP)); }

constexpr int first_group(int need) { for (int g = 6; g < 22; ++g) if (4 * g - (LWIN - 4) >= need) return g; return 99; }

struct Params {
  const u16* x; const u16* kc; const u16* vc; const u16* wq; const u16* wo;
  const u16* w1; const u16* b1; const u16* w2; const u16* b2; u16* out; unsigned char* ws;
};

__global__ void __launch_bounds__(NTHR, 3) fused_kernel(Params p) {
  __shared__ __attribute__((aligned(16))) unsigned char lds[L_END];
  LAS unsigned char* L = (LAS unsigned char*)lds;
  const unsigned lbase = (unsigned)(size_t)L;
  const int tid = threadIdx.x, lane = tid & 63, wave = __builtin_amdgcn_readfirstlane(tid >> 6);
  const int b = blockIdx.x, h = b & 7, j = b >> 3;
  const int g = lane >> 4, c = lane & 15;
  asm volatile("" :: "s"(p.x), "s"(p.kc), "s"(p.vc), "s"(p.wq), "s"(p.wo), "s"(p.w1), "s"(p.b1), "s"(p.w2), "s"(p.b2), "s"(p.out), "s"(p.ws));
  const __amdgpu_buffer_rsrc_t rs = __builtin_amdgcn_make_buffer_rsrc((void*)p.ws, 0, W_END, 0x00020000);
  const bool act = j < 28;
  const int cg = h * 28 + (act ? j : j - 28);
  const int cc_cg = h * 4 + (j >> 3), cc_i = j & 7;
  const int col = 16 * cc_cg + 2 * cc_i;

  LAS unsigned* flg = (LAS unsigned*)(L + L_FLG);
  if (tid < 2) { v4u z = {0u, 0u, 0u, 0u}; *(LAS v4u*)(L + L_FLG + tid * 16) = z; }
  if (wave == 0) {
    const int lc = j >> 2, rq = j & 3, lm = cg >> 3, kq = cg & 7;
    __builtin_amdgcn_s_setprio(3);
    const unsigned kvoff = ((unsigned)h * SK + (SK - WIN)) * 256u + (unsigned)j * 4096u;
    const unsigned char* kb = (const unsigned char*)p.kc + kvoff;
    const unsigned char* vb = (const unsigned char*)p.vc + kvoff;
    const unsigned k0 = (unsigned)g * 256u + (unsigned)((c ^ g) * 16), v0 = (unsigned)g * 256u + (unsigned)((c ^ (g << 2)) * 16), l16 = (unsigned)lane * 16u;
    BARRIER();
    {
      const unsigned ph = (unsigned)(lane >> 4) & 1u, c8 = (unsigned)lane & 7u;
      const unsigned ce = (c8 ^ (ph << 1)) * 16u, co = (c8 ^ ((ph | 2u) << 1)) * 16u;
      const unsigned rq8 = (unsigned)(lane >> 3) * 8192u, r18 = (unsigned)(lane >> 3) * (unsigned)(FF * 2);
      const unsigned char* sq = (const unsigned char*)p.wq + (unsigned)((h * 512 + lc * 64) * 2 + rq * 128 * 8192);
      const unsigned char* s1 = (const unsigned char*)p.w1 + (unsigned)(lm * 128 + kq * 64 * (FF * 2));
      constexpr unsigned SQ = 8u * 8192u - 1024u, S1 = 8u * (FF * 2) - 1024u;
#pragma unroll 1
      for (int n = 0; n < 4; ++n) glds4(sq + (unsigned)(n * 32 * 8192), rq8 + ce, rq8 + co + SQ, rq8 + ce + 2u * SQ, rq8 + co + 3u * SQ, lbase + L_WQ + n * 4096);
#pragma unroll 1
      for (int n = 0; n < 2; ++n) glds4(s1 + (unsigned)(n * 32 * (FF * 2)), r18 + ce, r18 + co + S1, r18 + ce + 2u * S1, r18 + co + 3u * S1, lbase + L_W1 + n * 4096);
    }
    constexpr int GFQ = first_group(16), GF1 = first_group(24), GFK = first_group(56);
    static_assert(GFQ <= 21 && GF1 <= 21 && GFK <= 21, "flags are set inside the K and V loops");
#define GROUP_FLAGS(gi) do { \
      if ((gi) == GFQ && lane == 0) __hip_atomic_store(flg + 0, 1u, __ATOMIC_RELAXED, __HIP_MEMORY_SCOPE_WORKGROUP); \
      if ((gi) == GF1 && lane == 0) __hip_atomic_store(flg + 2, 1u, __ATOMIC_RELAXED, __HIP_MEMORY_SCOPE_WORKGROUP); \
      if ((gi) == GFK && lane == 0) __hip_atomic_store(flg + 5, 1u, __ATOMIC_RELAXED, __HIP_MEMORY_SCOPE_WORKGROUP); } while (0)
#pragma unroll 1
    for (int n = 0; n < 8; ++n) { const int m = (n + KROT) & 7; WAIT_VMN(LWIN - 4); GROUP_FLAGS(6 + n); glds4(kb + m * 131072, k0, k0 ^ 64u, k0 ^ 128u, k0 ^ 192u, lbase + L_K + m * 4096); }
#pragma unroll 1
    for (int n = 0; n < 8; ++n) { const int m = (n + KROT) & 7; WAIT_VMN(LWIN - 4); GROUP_FLAGS(14 + n); glds4(vb + m * 131072, v0, v0 ^ 16u, v0 ^ 32u, v0 ^ 48u, lbase + L_V + m * 4096); }
    WAIT_VMN(32);
    RAWBAR();
    RAWBAR();
#pragma unroll 1
    for (int n = 0; n < 2; ++n) glds4((const unsigned char*)p.w2 + (unsigned)(cg * 8192 + n * 4096), l16, l16, l16, l16, lbase + L_W2 + n * 4096);
#pragma unroll 1
    for (int n0 = 0; n0 < 4; ++n0) {
      const int n = (n0 + j) & 3;
      const unsigned r0 = (unsigned)((4 * n) & 7) << 5;
      glds4((const unsigned char*)p.wo + (unsigned)((h * 512 + j * 16 + 4 * n) * 1024), l16 ^ r0, l16 ^ (r0 + 32u), l16 ^ (r0 + 64u), l16 ^ (r0 + 96u), lbase + L_WO + n * 4096);
    }
    WAIT_VMN(24);
    RAWBAR();
    WAIT_VMN(16);
    if (lane == 0) __hip_atomic_store(flg + 4, 1u, __ATOMIC_RELAXED, __HIP_MEMORY_SCOPE_WORKGROUP);
    WAIT_VM(0);
    RAWBAR();
  } else if (wave == 9) {
    const int lm = cg >> 3, kq = cg & 7;
    const int tsel = (c >> 1) & 3, prs = c & 1;
    {
      const v4u xr = *(const GAS v4u*)(p.x + lane * 8);
      const unsigned touch = __builtin_amdgcn_raw_buffer_load_b32(rs, W_TOUCH + (lane & 7) * 4, 0, 16);
      const unsigned long long a_b1 = (unsigned long long)(p.b1 + cg * 8), a_x = (unsigned long long)(p.x + col), a_b2 = (unsigned long long)(p.b2 + col);
      const unsigned long long a_sm = lane < 4 ? a_b1 + lane * 4 : (lane == 4 ? a_x : a_b2);
      const unsigned small = *(const GAS unsigned*)a_sm;
      __builtin_amdgcn_sched_barrier(0);
      RAWBAR();
      __builtin_amdgcn_sched_barrier(0);
      *(LAS v4u*)(L + L_Y + lane * 16) = ln_packed(ln_packed(xr));
      if (lane < 6) *(LAS unsigned*)(L + L_B1 + lane * 4) = small;
      asm volatile("" :: "v"(touch));
    }
    const int zoff = W_Z + ((lm * 8 + kq) * 8 + ((lane & 31) >> 2)) * 128 + (lane & 3) * 16;
    {
      const int q4 = c >> 2, pp = c & 3;
      const int ph = ((q4 >> 1) & 1) | ((g & 1) << 1);
      v4u yf[2];
#pragma unroll
      for (int s = 0; s < 2; ++s) yf[s] = *(const LAS v4u*)(L + L_Y + (64 * kq + 32 * s + 8 * g) * 2);
      for (unsigned spin = 0; spin < SPIN_MAX; ++spin) {
        const v4u f = *(volatile LAS v4u*)flg;
        asm volatile("" ::: "memory");
        if (__builtin_amdgcn_readfirstlane(f[1] & f[2]) != 0u) break;
        __builtin_amdgcn_s_sleep(1);
      }
      const v2u tg = *(const LAS v2u*)(L + L_TAG);
      f32x4 a0 = {0.f, 0.f, 0.f, 0.f};
#pragma unroll
      for (int t = 0; t < 4; ++t) {
        LAS unsigned char* ab = L + L_W1 + (8 * g + q4) * 128 + (((2 * t + (pp >> 1)) ^ (ph << 1)) * 16) + (pp & 1) * 8;
        const unsigned m = tsel == t ? ~0u : 0u;
#pragma unroll
        for (int s = 0; s < 2; ++s) { v4u ym; ym[0] = yf[s][0] & m; ym[1] = yf[s][1] & m; ym[2] = yf[s][2] & m; ym[3] = yf[s][3] & m; a0 = mfma32(cat(trd(ab + s * 4096), trd(ab + s * 4096 + 512)), ym, a0); }
      }
      v4u gr; gr[0] = __float_as_uint(prs ? a0[2] : a0[0]); gr[1] = tg[0]; gr[2] = __float_as_uint(prs ? a0[3] : a0[1]); gr[3] = tg[1];
      if (c < 8 && act) ST16(gr, W_Z + ((lm * 8 + 2 * tsel + (g >> 1)) * 8 + kq) * 128 + (2 * (g & 1) + prs) * 16);
    }
    DELAY(DLY_Q + 8);
    v4u zq;
    asm volatile("global_load_dwordx4 %0, %1, %2 sc1" : "=v"(zq) : "v"(zoff), "s"(p.ws) : "memory");
    RAWBAR();
    RAWBAR();
    asm volatile("s_waitcnt vmcnt(0)" : "+v"(zq) :: "memory");
    v2u hm[4];
    if (act) {
      const v2u tg = *(const LAS v2u*)(L + L_TAG);
      const unsigned tlo = tg[0], thi = tg[1];
      if (!NONEBAD(TAGBAD(zq))) {
        for (unsigned spin = 0; spin < SPIN_MAX; ++spin) {
          asm volatile("" ::: "memory");
          zq = LD16(zoff);
          if (NONEBAD(TAGBAD(zq))) break;
        }
      }
      float z0 = __uint_as_float(zq[0]), z1 = __uint_as_float(zq[2]);
      z0 += dpp<ROR4>(z0); z1 += dpp<ROR4>(z1); z0 += dpp<XOR8>(z0); z1 += dpp<XOR8>(z1); z0 = x16_sum(z0); z1 = x16_sum(z1);
      const unsigned bw = *(const LAS unsigned*)(L + L_B1 + (lane & 3) * 4);
      float hv[2];
#pragma unroll
      for (int e = 0; e < 2; ++e) {
        const float z = rbf(rbf(e ? z1 : z0) + (e ? bfhi(bw) : bflo(bw)));
        const float uu = 0.7978845608028654f * (z + 0.044715f * z * z * z);
        const float th = 1.f - 2.f * __builtin_amdgcn_rcpf(1.f + __builtin_amdgcn_exp2f(2.885390081777927f * uu));
        hv[e] = 0.5f * z * (1.f + th);
      }
      const unsigned hp = pkbf(hv[0], hv[1]);
      const unsigned h0 = __builtin_amdgcn_readlane(hp, 0), h1 = __builtin_amdgcn_readlane(hp, 1), h2 = __builtin_amdgcn_readlane(hp, 2), h3 = __builtin_amdgcn_readlane(hp, 3);
#pragma unroll
      for (int t = 0; t < 4; ++t) { const bool on = tsel == t && g < 2; hm[t][0] = on ? (g == 0 ? h0 : h2) : 0u; hm[t][1] = on ? (g == 0 ? h1 : h3) : 0u; }
    }
    RAWBAR();
    if (act) {
      for (unsigned spin = 0; spin < SPIN_MAX && lds_peek(flg + 4) == 0u; ++spin) __builtin_amdgcn_s_sleep(1);
      LAS unsigned char* ab = L + L_W2 + ((4 * g + (c >> 2)) & 7) * 1024 + (c & 3) * 8;
      LAS unsigned char* mb = L + L_MP + (16 * tsel + 4 * g + 2 * prs) * 4;
#pragma unroll 2
      for (int T = 0; T < 8; ++T) {
        f32x4 d = {0.f, 0.f, 0.f, 0.f};
#pragma unroll
        for (int t = 0; t < 4; ++t) d = mfma16(trd(ab + (4 * T + t) * 32), hm[t], d);
        f32x2 o2; o2[0] = prs ? d[2] : d[0]; o2[1] = prs ? d[3] : d[1];
        if (c < 8) *(LAS f32x2*)(mb + T * 256) = o2;
      }
    }
    BARRIER();
  } else {
    const int w = wave - 1;
    __builtin_amdgcn_s_setprio(2);
    const int lc = j >> 2, rq = j & 3;
    if (w == 0) {
      const v4u xr = *(const GAS v4u*)(p.x + lane * 8);
      const v2u ev = __builtin_amdgcn_raw_buffer_load_b64(rs, W_E, 0, 16);
      __builtin_amdgcn_sched_barrier(0);
      RAWBAR();
      __builtin_amdgcn_sched_barrier(0);
      *(LAS v4u*)(L + L_XN + lane * 16) = ln_packed(xr);
      const unsigned long long E = (unsigned long long)(unsigned)__builtin_amdgcn_readfirstlane(ev[0]) | ((unsigned long long)(unsigned)__builtin_amdgcn_readfirstlane(ev[1]) << 32);
      const unsigned long long T = E * 6364136223846793005ull + 1442695040888963407ull;
      if (lane == 0) { v2u tg; tg[0] = (unsigned)T; tg[1] = (unsigned)(T >> 32); *(LAS v2u*)(L + L_TAG) = tg; }
      asm volatile("s_waitcnt lgkmcnt(0)" ::: "memory");
      if (lane == 0) __hip_atomic_store(flg + 1, 1u, __ATOMIC_RELAXED, __HIP_MEMORY_SCOPE_WORKGROUP);
    } else {
      RAWBAR();
    }
    unsigned tlo, thi;
    {
      const int q4 = c >> 2, pp = c & 3, t = w & 3;
      int toff = L_WQ + (8 * g + q4) * 128 + (((2 * t + (pp >> 1)) ^ ((((q4 >> 1) & 1) | ((g & 1) << 1)) << 1)) * 16) + (pp & 1) * 8;
      int xoff = L_XN + (128 * rq + 8 * g) * 2;
      int off = W_Q + ((((h * 8 + lc) * 4 + rq) * 16) + 4 * t + g) * 16;
      PIN(toff); PIN(xoff); PIN(off);
      for (unsigned spin = 0; spin < SPIN_MAX && lds_peek(flg + 1) == 0u; ++spin) __builtin_amdgcn_s_sleep(1);
      const v2u tg = *(const LAS v2u*)(L + L_TAG);
      tlo = tg[0]; thi = tg[1];
      if (w < 4) {
        v4u xf[4];
#pragma unroll
        for (int s = 0; s < 4; ++s) xf[s] = *(const LAS v4u*)(L + xoff + s * 64);
        for (unsigned spin = 0; spin < SPIN_MAX && lds_peek(flg + 0) == 0u; ++spin) { }
        s16x4 a[4][2];
#pragma unroll
        for (int s = 0; s < 4; ++s) { a[s][0] = trd(L + toff + s * 4096); a[s][1] = trd(L + toff + s * 4096 + 512); }
        __builtin_amdgcn_sched_barrier(0);
        const f32x4 z4 = {0.f, 0.f, 0.f, 0.f};
        const f32x4 a0 = mfma32(cat(a[1][0], a[1][1]), xf[1], mfma32(cat(a[0][0], a[0][1]), xf[0], z4)) + mfma32(cat(a[3][0], a[3][1]), xf[3], mfma32(cat(a[2][0], a[2][1]), xf[2], z4));
        v4u gr; gr[0] = pkbf(a0[0], a0[1]); gr[1] = tlo; gr[2] = pkbf(a0[2], a0[3]); gr[3] = thi;
        if (c == 0) ST16(gr, off);
        if (lane == 0) lds_add(flg + 3, 1u);
      } else {
        for (unsigned spin = 0; spin < SPIN_MAX && lds_peek(flg + 3) < 4u; ++spin) __builtin_amdgcn_s_sleep(1);
      }
    }
    const int head = c & 3, kk = c >> 2, q4 = c >> 2, pp = c & 3, tsel = (c >> 1) & 3, prs = c & 1;
    int qoff = W_Q + (h * 8 + w) * 1024 + lane * 16;
    int aQw; { const int dim = (w & 1) * 64 + 4 * (lane & 15); aQw = L_Q + (((dim >> 5) * 4 + ((dim >> 3) & 3)) * 4 + (w >> 1)) * 16 + (dim & 7) * 2; }
    int aK[4];
#pragma unroll
    for (int s = 0; s < 4; ++s) aK[s] = L_K + (w * 16 + c) * 256 + (((4 * s + g) ^ c) * 16);
    int aQr = L_Q + (g * 4 + head) * 16;
    int aMw = L_MX + ((lane & 3) * 8 + w) * 4;
    int aMr = L_MX + head * 32;
    int aPw = L_P + (((w >> 1) * 4 + 2 * (w & 1) + (g >> 1)) * 4 + head) * 16 + (g & 1) * 8;
    int aPr = L_P + (g * 4 + head) * 16;
    int aV[2];
#pragma unroll
    for (int t2 = 0; t2 < 2; ++t2) aV[t2] = L_V + (8 * g + 4 * t2 + q4) * 256 + (((2 * w + (pp >> 1)) ^ ((q4 << 2) | ((2 * g + t2) & 3))) * 16) + (pp & 1) * 8;
    int ooff = W_O + ((((h * 32 + head * 8 + w) * 32 + j) * 8) + 2 * g + kk) * 16;
    int offa = W_O + (((h * 32 + j) * 32 + 8 * (w & 3)) * 8 + lane) * 16;
    int aOw = L_OP + (w & 3) * 80 + lane * 8, aOr = L_OP + g * 16;
    int aWo[4];
#pragma unroll
    for (int t = 0; t < 4; ++t) { const int row = 4 * g + (c >> 2); aWo[t] = L_WO + row * 1024 + ((2 * (4 * w + t) + ((c & 3) >> 1)) ^ ((row & 7) << 1)) * 16 + (c & 1) * 8; }
    int aMp = L_MP + (16 * (4 * w + tsel) + 4 * g + 2 * prs) * 4;
    int soff = W_S + (((2 * (4 * w + tsel) + (g >> 1)) * 256 + b) * 4 + 2 * (g & 1) + prs) * 32 + (c >> 3) * 16;
    int foff; { const int cc = 8 * cc_cg + cc_i; foff = W_S + (((cc >> 2) * 256 + 32 * w + (lane >> 1)) * 4 + (cc & 3)) * 32 + (lane & 1) * 16; }
    unsigned tex = (lane & 1) ? thi : tlo, otag = (kk & 1) ? thi : tlo;
    unsigned tm[4];
#pragma unroll
    for (int t = 0; t < 4; ++t) tm[t] = tsel == t ? ~0u : 0u;
    PIN(qoff); PIN(aQw); PIN(aK[0]); PIN(aK[1]); PIN(aK[2]); PIN(aK[3]); PIN(aQr); PIN(aMw); PIN(aMr); PIN(aPw); PIN(aPr); PIN(aV[0]); PIN(aV[1]);
    PIN(ooff); PIN(offa); PIN(aOw); PIN(aOr); PIN(aWo[0]); PIN(aWo[1]); PIN(aWo[2]); PIN(aWo[3]); PIN(aMp); PIN(soff); PIN(foff); PIN(tex); PIN(otag);
    PIN(tm[0]); PIN(tm[1]); PIN(tm[2]); PIN(tm[3]);
    DELAY(DLY_Q);
    v4u q0;
    for (unsigned spin = 0; spin < SPIN_MAX; ++spin) {
      asm volatile("" ::: "memory");
      q0 = LD16(qoff);
      if (NONEBAD(TAGBAD(q0))) break;
    }
    for (unsigned spin = 0; spin < SPIN_MAX && lds_peek(flg + 5) == 0u; ++spin) { }
    v4u kf[4];
#pragma unroll
    for (int s = 0; s < 4; ++s) kf[s] = *(const LAS v4u*)(L + aK[s]);
    {
      float u0 = bflo(q0[0]), u1 = bfhi(q0[0]), u2 = bflo(q0[2]), u3 = bfhi(q0[2]);
      u0 = x16_sum(u0); u1 = x16_sum(u1); u2 = x16_sum(u2); u3 = x16_sum(u3);
      u0 = x32_sum(u0); u1 = x32_sum(u1); u2 = x32_sum(u2); u3 = x32_sum(u3);
      v2u d; d[0] = pkbf(u0, u1); d[1] = pkbf(u2, u3);
      if (lane < 16) *(LAS v2u*)(L + aQw) = d;
    }
    BARRIER();
    f32x4 sc;
    {
      v4u qf[4];
#pragma unroll
      for (int s = 0; s < 4; ++s) qf[s] = *(const LAS v4u*)(L + aQr + s * 256);
      __builtin_amdgcn_sched_barrier(0);
      const f32x4 z4 = {0.f, 0.f, 0.f, 0.f};
      const f32x4 s0 = mfma32(kf[1], qf[1], mfma32(kf[0], qf[0], z4)), s1 = mfma32(kf[3], qf[3], mfma32(kf[2], qf[2], z4));
      sc = s0 + s1;
      int mx = imax(imax(fkey(sc[0]), fkey(sc[1])), imax(fkey(sc[2]), fkey(sc[3])));
      mx = x16_imax(mx); mx = x32_imax(mx);
      if (lane < 4) *(LAS int*)(L + aMw) = mx;
    }
    BARRIER();
    float mc;
    constexpr float C2 = 0.08838834764831845f * 1.4426950408889634f;
    {
      const v4u ma = *(const LAS v4u*)(L + aMr), mb = *(const LAS v4u*)(L + aMr + 16);
      const int M = imax(imax(imax((int)ma[0], (int)ma[1]), imax((int)ma[2], (int)ma[3])), imax(imax((int)mb[0], (int)mb[1]), imax((int)mb[2], (int)mb[3])));
      mc = funkey(M) * C2;
      float e[4];
#pragma unroll
      for (int r = 0; r < 4; ++r) e[r] = __builtin_amdgcn_exp2f(fmaf(sc[r], C2, -mc));
      v2u pk; pk[0] = pkbf(e[0], e[1]); pk[1] = pkbf(e[2], e[3]);
      if (c < 4) *(LAS v2u*)(L + aPw) = pk;
      float lsum = (bflo(pk[0]) + bfhi(pk[0])) + (bflo(pk[1]) + bfhi(pk[1]));
      lsum = x16_sum(lsum); lsum = x32_sum(lsum);
      if (lane < 4) *(LAS float*)(L + aMw + 128) = lsum;
    }
    BARRIER();
    {
      v4u pf[4]; s16x4 va[4][2];
#pragma unroll
      for (int s = 0; s < 4; ++s) { va[s][0] = trd(L + aV[0] + s * 8192); va[s][1] = trd(L + aV[1] + s * 8192); pf[s] = *(const LAS v4u*)(L + aPr + s * 256); }
      const f32x4 la = *(const LAS f32x4*)(L + aMr + 128), lb = *(const LAS f32x4*)(L + aMr + 144);
      __builtin_amdgcn_sched_barrier(0);
      const f32x4 z4 = {0.f, 0.f, 0.f, 0.f};
      const f32x4 o0 = mfma32(cat(va[1][0], va[1][1]), pf[1], mfma32(cat(va[0][0], va[0][1]), pf[0], z4));
      const f32x4 o1 = mfma32(cat(va[3][0], va[3][1]), pf[3], mfma32(cat(va[2][0], va[2][1]), pf[2], z4));
      const float l8 = ((la[0] + la[1]) + (la[2] + la[3])) + ((lb[0] + lb[1]) + (lb[2] + lb[3]));
      const f32x4 oa = o0 + o1;
      v4u gr; gr[0] = __float_as_uint((kk & 1) ? oa[2] : oa[0]); gr[1] = __float_as_uint((kk & 1) ? oa[3] : oa[1]); gr[2] = __float_as_uint((kk & 1) ? l8 : mc); gr[3] = otag;
      if (kk < 2) ST16(gr, ooff);
    }
    if (w < 4) {
      DELAY(DLY_O);
      v4u ga;
      for (unsigned spin = 0; spin < SPIN_MAX; ++spin) {
        asm volatile("" ::: "memory");
        ga = LD16(offa);
        if (NONEBAD(ga[3] ^ tex)) break;
      }
      const float aux = __uint_as_float(ga[2]), axn = dpp<XOR1>(aux);
      const float mj = (lane & 1) ? axn : aux, lj = (lane & 1) ? aux : axn;
      int mk = fkey(mj);
      mk = imax(mk, dppi<XOR8>(mk)); mk = x16_imax(mk); mk = x32_imax(mk);
      const float Mw = funkey(mk);
      const float wt = __builtin_amdgcn_exp2f(mj - Mw);
      float o0 = __uint_as_float(ga[0]) * wt, o1 = __uint_as_float(ga[1]) * wt, ll = lj * wt;
      o0 += dpp<XOR8>(o0); o1 += dpp<XOR8>(o1); ll += dpp<XOR8>(ll);
      o0 = x16_sum(o0); o1 = x16_sum(o1); ll = x16_sum(ll);
      o0 = x32_sum(o0); o1 = x32_sum(o1); ll = x32_sum(ll);
      if (lane < 9) { f32x2 o2; o2[0] = lane < 8 ? o0 : Mw; o2[1] = lane < 8 ? o1 : ll; *(LAS f32x2*)(L + aOw) = o2; }
    }
    BARRIER();
    {
      f32x4 ou[4]; f32x2 ml[4]; s16x4 a[4];
#pragma unroll
      for (int u = 0; u < 4; ++u) { ml[u] = *(const LAS f32x2*)(L + L_OP + u * 80 + 64); ou[u] = *(const LAS f32x4*)(L + aOr + u * 80); }
#pragma unroll
      for (int t = 0; t < 4; ++t) a[t] = trd(L + aWo[t]);
      f32x2 mv = *(const LAS f32x2*)(L + aMp);
      __builtin_amdgcn_sched_barrier(0);
      if (!act) { mv[0] = 0.f; mv[1] = 0.f; }
      const float M = funkey(imax(imax(fkey(ml[0][0]), fkey(ml[1][0])), imax(fkey(ml[2][0]), fkey(ml[3][0]))));
      float f[4];
#pragma unroll
      for (int u = 0; u < 4; ++u) f[u] = __builtin_amdgcn_exp2f(ml[u][0] - M);
      const float li = __builtin_amdgcn_rcpf((f[0] * ml[0][1] + f[1] * ml[1][1]) + (f[2] * ml[2][1] + f[3] * ml[3][1]));
      const f32x4 o4 = ((ou[0] * f[0] + ou[1] * f[1]) + (ou[2] * f[2] + ou[3] * f[3])) * li;
      v2u of; of[0] = pkbf(o4[0], o4[1]); of[1] = pkbf(o4[2], o4[3]);
      const f32x4 z4 = {0.f, 0.f, 0.f, 0.f};
      v2u om[4];
#pragma unroll
      for (int t = 0; t < 4; ++t) { om[t][0] = of[0] & tm[t]; om[t][1] = of[1] & tm[t]; }
      const f32x4 d = mfma16(a[1], om[1], mfma16(a[0], om[0], z4)) + mfma16(a[3], om[3], mfma16(a[2], om[2], z4));
      v4u gr; gr[0] = __float_as_uint(c < 8 ? (prs ? d[2] : d[0]) : mv[0]); gr[1] = tlo; gr[2] = __float_as_uint(c < 8 ? (prs ? d[3] : d[1]) : mv[1]); gr[3] = thi;
      ST16(gr, soff);
    }
    {
      DELAY(DLY_S);
      v4u gq;
      for (unsigned spin = 0; spin < SPIN_MAX; ++spin) {
        asm volatile("" ::: "memory");
        gq = LD16(foff);
        if (NONEBAD(TAGBAD(gq))) break;
      }
      float a0 = __uint_as_float(gq[0]), a1 = __uint_as_float(gq[2]);
      a0 += dpp<XOR2>(a0); a1 += dpp<XOR2>(a1); a0 += dpp<ROR4>(a0); a1 += dpp<ROR4>(a1); a0 += dpp<XOR8>(a0); a1 += dpp<XOR8>(a1);
      a0 = x16_sum(a0); a1 = x16_sum(a1); a0 = x32_sum(a0); a1 = x32_sum(a1);
      if (lane < 2) { f32x2 o2; o2[0] = a0; o2[1] = a1; *(LAS f32x2*)(L + L_FS + lane * 64 + w * 8) = o2; }
    }
    BARRIER();
    if (w == 0) {
      const int e = (lane >> 3) & 1, u = lane & 7;
      const unsigned xw = *(const LAS unsigned*)(L + L_XB), b2w = *(const LAS unsigned*)(L + L_XB + 4);
      float s = *(const LAS float*)(L + L_FS + u * 8 + e * 4), m = *(const LAS float*)(L + L_FM + u * 8 + e * 4);
      s += dpp<XOR1>(s); m += dpp<XOR1>(m); s += dpp<XOR2>(s); m += dpp<XOR2>(m); s += dpp<XOR7>(s); m += dpp<XOR7>(m);
      const float z = rbf(rbf(m) + (e ? bfhi(b2w) : bflo(b2w)));
      const float gt = rbf(z * __builtin_amdgcn_rcpf(1.f + __builtin_amdgcn_exp2f(-1.4426950408889634f * z)));
      const float r = (e ? bfhi(xw) : bflo(xw)) + rbf(rbf(s) * gt);
      const float rn = dpp<XOR8>(r);
      if (lane == 0) {
        *(GAS unsigned*)(p.out + col) = pkbf(r, rn);
        if (b == 0) { const unsigned long long T = (unsigned long long)tlo | ((unsigned long long)thi << 32); __hip_atomic_store((GAS unsigned long long*)(p.ws + W_E), T, __ATOMIC_RELAXED, __HIP_MEMORY_SCOPE_AGENT); }
      }
    }
  }
}

extern "C" void kernel_launch(void* const* d_in, const int* in_sizes, int n_in,
                              void* d_out, int out_size, void* d_ws, size_t ws_size,
                              hipStream_t stream) {
  Params p;
  p.x = (const u16*)d_in[0]; p.kc = (const u16*)d_in[1]; p.vc = (const u16*)d_in[2]; p.wq = (const u16*)d_in[3]; p.wo = (const u16*)d_in[4];
  p.w1 = (const u16*)d_in[6]; p.b1 = (const u16*)d_in[7]; p.w2 = (const u16*)d_in[8]; p.b2 = (const u16*)d_in[9];
  p.out = (u16*)d_out; p.ws = (unsigned char*)d_ws;
  fused_kernel<<<NB, NTHR, 0, stream>>>(p);
}
```

```cpp
#include <hip/hip_runtime.h>
#include <hip/hip_bf16.h>

#define LAS __attribute__((address_space(3)))
#define GAS __attribute__((address_space(1)))
typedef unsigned short u16;
typedef unsigned v4u __attribute__((ext_vector_type(4)));
typedef unsigned v2u __attribute__((ext_vector_type(2)));
typedef float f32x4 __attribute__((ext_vector_type(4)));
typedef float f32x2 __attribute__((ext_vector_type(2)));
typedef short s16x4 __attribute__((ext_vector_type(4)));
typedef short s16x8 __attribute__((ext_vector_type(8)));
typedef __bf16 bf16x8 __attribute__((ext_vector_type(8)));
typedef __bf16 bf16x2 __attribute__((ext_vector_type(2)));

constexpr int DLY_Q = 24, DLY_O = 24, DLY_S = 30;
constexpr int LWIN = 28;

constexpr int D = 512, SK = 65536, WIN = 4096, FF = 1792;
constexpr int NB = 256, NTHR = 640;
constexpr int L_WQ = 0, L_K = 16384, L_V = 49152, L_WO = 81920, L_W2 = 98304, L_W1 = 106496, L_XN = 114688, L_Y = 115712, L_Q = 116736, L_P = 117760;
constexpr int L_MX = 118784, L_SM = L_MX + 128, L_TAG = L_SM + 128, L_OP = L_TAG + 16, L_FS = L_OP + 512, L_FM = L_FS + 64, L_B1 = L_FM + 64, L_XB = L_B1 + 16, L_FLG = L_XB + 16, L_MP = L_FLG + 32, L_END = L_MP + 2048;
constexpr int W_E = 0;
constexpr int W_Q = 4096;
constexpr int W_Z = W_Q + 131072;
constexpr int W_O = W_Z + 262144;
constexpr int W_S = W_O + 1048576;
constexpr int W_TOUCH = W_S + 2097152;
constexpr int W_END = W_TOUCH + 4096;
constexpr unsigned SPIN_MAX = 1u << 17;

__device__ __forceinline__ float bflo(unsigned w) { return __uint_as_float(w << 16); }
__device__ __forceinline__ float bfhi(unsigned w) { return __uint_as_float(w & 0xffff0000u); }
__device__ __forceinline__ unsigned pkbf(float lo, float hi) { bf16x2 r; r[0] = (__bf16)lo; r[1] = (__bf16)hi; return __builtin_bit_cast(unsigned, r); }
__device__ __forceinline__ float rbf(float f) { return bflo(pkbf(f, f)); }
template <int CTRL> __device__ __forceinline__ float dpp(float x) {
  return __builtin_bit_cast(float, __builtin_amdgcn_mov_dpp(__builtin_bit_cast(int, x), CTRL, 0xf, 0xf, true));
}
constexpr int XOR1 = 0xB1, XOR2 = 0x4E, XOR7 = 0x141, XOR15 = 0x140, XOR8 = 0x128, ROR4 = 0x124;
__device__ __forceinline__ int fkey(float f) { const int i = __float_as_int(f); return i ^ ((i >> 31) & 0x7fffffff); }
__device__ __forceinline__ float funkey(int k) { return __int_as_float(k ^ ((k >> 31) & 0x7fffffff)); }
template <int CTRL> __device__ __forceinline__ int dppi(int x) { return __builtin_amdgcn_mov_dpp(x, CTRL, 0xf, 0xf, true); }
__device__ __forceinline__ int imax(int a, int b) { return a > b ? a : b; }
__device__ __forceinline__ int x16_imax(int x) { auto s = __builtin_amdgcn_permlane16_swap((unsigned)x, (unsigned)x, false, false); return imax((int)s[0], (int)s[1]); }
__device__ __forceinline__ int x32_imax(int x) { auto s = __builtin_amdgcn_permlane32_swap((unsigned)x, (unsigned)x, false, false); return imax((int)s[0], (int)s[1]); }
__device__ __forceinline__ float x16_sum(float x) { auto s = __builtin_amdgcn_permlane16_swap(__float_as_uint(x), __float_as_uint(x), false, false); return __uint_as_float(s[0]) + __uint_as_float(s[1]); }
__device__ __forceinline__ float x32_sum(float x) { auto s = __builtin_amdgcn_permlane32_swap(__float_as_uint(x), __float_as_uint(x), false, false); return __uint_as_float(s[0]) + __uint_as_float(s[1]); }
__device__ __forceinline__ void wave_sum2(float& a, float& b) {
  a += dpp<XOR1>(a); b += dpp<XOR1>(b); a += dpp<XOR2>(a); b += dpp<XOR2>(b); a += dpp<XOR7>(a); b += dpp<XOR7>(b); a += dpp<XOR15>(a); b += dpp<XOR15>(b);
  a = x16_sum(a); b = x16_sum(b); a = x32_sum(a); b = x32_sum(b);
}
__device__ __forceinline__ f32x4 mfma32(v4u a, v4u b, f32x4 c) {
  return __builtin_amdgcn_mfma_f32_16x16x32_bf16(__builtin_bit_cast(bf16x8, a), __builtin_bit_cast(bf16x8, b), c, 0, 0, 0);
}
__device__ __forceinline__ f32x4 mfma16(s16x4 a, v2u b, f32x4 c) {
  return __builtin_amdgcn_mfma_f32_16x16x16bf16_1k(a, __builtin_bit_cast(s16x4, b), c, 0, 0, 0);
}
__device__ __forceinline__ s16x4 trd(LAS unsigned char* p) { return __builtin_amdgcn_ds_read_tr16_b64_v4i16((LAS s16x4*)p); }
__device__ __forceinline__ v4u cat(s16x4 lo, s16x4 hi) { return __builtin_bit_cast(v4u, __builtin_shufflevector(lo, hi, 0, 1, 2, 3, 4, 5, 6, 7)); }
__device__ __forceinline__ v4u ln_packed(v4u x) {
  float v[8];
#pragma unroll
  for (int i = 0; i < 4; ++i) { v[2 * i] = bflo(x[i]); v[2 * i + 1] = bfhi(x[i]); }
  const float piv = __uint_as_float(__builtin_amdgcn_readfirstlane(x[0]) << 16);
  float s = 0.f, q = 0.f;
#pragma unroll
  for (int i = 0; i < 8; ++i) { const float d = v[i] - piv; s += d; q = fmaf(d, d, q); }
  wave_sum2(s, q);
  const float md = s * (1.0f / D);
  const float var = rbf(fmaxf(fmaf(-md, md, q * (1.0f / D)), 0.f));
  const float m = rbf(piv + md);
  const float sd = rbf(__builtin_amdgcn_sqrtf(rbf(var + 1e-5f)));
  const float inv = __builtin_amdgcn_rcpf(sd);
  v4u r;
#pragma unroll
  for (int i = 0; i < 4; ++i) r[i] = pkbf(rbf(v[2 * i] - m) * inv, rbf(v[2 * i + 1] - m) * inv);
  return r;
}
#define WAIT_VM(n) asm volatile("s_waitcnt vmcnt(" #n ")" ::: "memory")
#define WAIT_VMN(n) asm volatile("s_waitcnt vmcnt(%0)" :: "i"(n) : "memory")
static_assert(LWIN % 4 == 0 && LWIN >= 8 && LWIN <= 36, "loader window");
#define BARRIER() asm volatile("s_waitcnt lgkmcnt(0)\n\ts_barrier" ::: "memory")
#define RAWBAR() asm volatile("s_barrier" ::: "memory")
__device__ __forceinline__ void glds4(const void* sbase, unsigned v0, unsigned v1, unsigned v2, unsigned v3, unsigned ldsaddr) {
  unsigned keep;
  asm volatile("s_mov_b32 %0, m0\n\ts_mov_b32 m0, %6\n\ts_nop 0\n\tglobal_load_lds_dwordx4 %1, %5 nt\n\tglobal_load_lds_dwordx4 %2, %5 offset:1024 nt\n\t"
               "global_load_lds_dwordx4 %3, %5 offset:2048 nt\n\tglobal_load_lds_dwordx4 %4, %5 offset:3072 nt\n\ts_mov_b32 m0, %0"
               : "=&s"(keep) : "v"(v0), "v"(v1), "v"(v2), "v"(v3), "s"(sbase), "s"(ldsaddr) : "memory");
}
#define DELAY(n) do { if ((n) > 0) __builtin_amdgcn_s_sleep((n) > 127 ? 127 : (n)); } while (0)
#define LD16(off) __builtin_amdgcn_raw_buffer_load_b128(rs, (off), 0, 16)
#define ST16(v, off) __builtin_amdgcn_raw_buffer_store_b128((v), rs, (off), 0, 16)
#define TAGBAD(x) (((x)[1] ^ tlo) | ((x)[3] ^ thi))
#define NONEBAD(v) (__builtin_amdgcn_ballot_w64((v) != 0u) == 0ull)
#define PIN(x) asm volatile("" : "+v"(x))
__device__ __forceinline__ void lds_add(LAS unsigned* q, unsigned v) { __hip_atomic_fetch_add(q, v, __ATOMIC_RELAXED, __HIP_MEMORY_SCOPE_WORKGROUP); }
__device__ __forceinline__ unsigned lds_peek(LAS unsigned* q) { return __builtin_amdgcn_readfirstlane(__hip_atomic_load(q, __ATOMIC_RELAXED, __HIP_MEMORY_SCOPE_WORKGROUP)); }

constexpr int first_group(int need) { for (int g = 6; g < 22; ++g) if (4 * g - (LWIN - 4) >= need) return g; return 99; }

struct Params {
  const u16* x; const u16* kc; const u16* vc; const u16* wq; const u16* wo;
  const u16* w1; const u16* b1; const u16* w2; const u16* b2; u16* out; unsigned char* ws;
};

__global__ void __launch_bounds__(NTHR, 3) fused_kernel(Params p) {
  __shared__ __attribute__((aligned(16))) unsigned char lds[L_END];
  LAS unsigned char* L = (LAS unsigned char*)lds;
  const unsigned lbase = (unsigned)(size_t)L;
  const int tid = threadIdx.x, lane = tid & 63, wave = __builtin_amdgcn_readfirstlane(tid >> 6);
  const int b = blockIdx.x, h = b & 7, j = b >> 3;
  const int g = lane >> 4, c = lane & 15;
  asm volatile("" :: "s"(p.x), "s"(p.kc), "s"(p.vc), "s"(p.wq), "s"(p.wo), "s"(p.w1), "s"(p.b1), "s"(p.w2), "s"(p.b2), "s"(p.out), "s"(p.ws));
  const __amdgpu_buffer_rsrc_t rs = __builtin_amdgcn_make_buffer_rsrc((void*)p.ws, 0, W_END, 0x00020000);
  const bool act = j < 28;
  const int cg = h * 28 + (act ? j : j - 28);
  const int cc_cg = h * 4 + (j >> 3), cc_i = j & 7;
  const int col = 16 * cc_cg + 2 * cc_i;

  LAS unsigned* flg = (LAS unsigned*)(L + L_FLG);
  if (tid < 2) { v4u z = {0u, 0u, 0u, 0u}; *(LAS v4u*)(L + L_FLG + tid * 16) = z; }
  if (wave == 0) {
    const int lc = j >> 2, rq = j & 3, lm = cg >> 3, kq = cg & 7;
    __builtin_amdgcn_s_setprio(3);
    const unsigned kvoff = ((unsigned)h * SK + (SK - WIN)) * 256u + (unsigned)j * 4096u;
    const unsigned char* kb = (const unsigned char*)p.kc + kvoff;
    const unsigned char* vb = (const unsigned char*)p.vc + kvoff;
    const unsigned k0 = (unsigned)g * 256u + (unsigned)((c ^ g) * 16), v0 = (unsigned)g * 256u + (unsigned)((c ^ (g << 2)) * 16), l16 = (unsigned)lane * 16u;
    BARRIER();
    {
      const unsigned ph = (unsigned)(lane >> 4) & 1u, c8 = (unsigned)lane & 7u;
      const unsigned ce = (c8 ^ (ph << 1)) * 16u, co = (c8 ^ ((ph | 2u) << 1)) * 16u;
      const unsigned rq8 = (unsigned)(lane >> 3) * 8192u, r18 = (unsigned)(lane >> 3) * (unsigned)(FF * 2);
      const unsigned char* sq = (const unsigned char*)p.wq + (unsigned)((h * 512 + lc * 64) * 2 + rq * 128 * 8192);
      const unsigned char* s1 = (const unsigned char*)p.w1 + (unsigned)(lm * 128 + kq * 64 * (FF * 2));
      constexpr unsigned SQ = 8u * 8192u - 1024u, S1 = 8u * (FF * 2) - 1024u;
#pragma unroll 1
      for (int n = 0; n < 4; ++n) glds4(sq + (unsigned)(n * 32 * 8192), rq8 + ce, rq8 + co + SQ, rq8 + ce + 2u * SQ, rq8 + co + 3u * SQ, lbase + L_WQ + n * 4096);
#pragma unroll 1
      for (int n = 0; n < 2; ++n) glds4(s1 + (unsigned)(n * 32 * (FF * 2)), r18 + ce, r18 + co + S1, r18 + ce + 2u * S1, r18 + co + 3u * S1, lbase + L_W1 + n * 4096);
    }
    constexpr int GFQ = first_group(16), GF1 = first_group(24), GFK = first_group(56);
    static_assert(GFQ <= 21 && GF1 <= 21 && GFK <= 21, "flags are set inside the K and V loops");
#define GROUP_FLAGS(gi) do { \
      if ((gi) == GFQ && lane == 0) __hip_atomic_store(flg + 0, 1u, __ATOMIC_RELAXED, __HIP_MEMORY_SCOPE_WORKGROUP); \
      if ((gi) == GF1 && lane == 0) __hip_atomic_store(flg + 2, 1u, __ATOMIC_RELAXED, __HIP_MEMORY_SCOPE_WORKGROUP); \
      if ((gi) == GFK && lane == 0) __hip_atomic_store(flg + 5, 1u, __ATOMIC_RELAXED, __HIP_MEMORY_SCOPE_WORKGROUP); } while (0)
#pragma unroll 1
    for (int n = 0; n < 8; ++n) { const int m = (n + h) & 7; WAIT_VMN(LWIN - 4); GROUP_FLAGS(6 + n); glds4(kb + m * 131072, k0, k0 ^ 64u, k0 ^ 128u, k0 ^ 192u, lbase + L_K + m * 4096); }
#pragma unroll 1
    for (int n = 0; n < 8; ++n) { const int m = (n + h) & 7; WAIT_VMN(LWIN - 4); GROUP_FLAGS(14 + n); glds4(vb + m * 131072, v0, v0 ^ 16u, v0 ^ 32u, v0 ^ 48u, lbase + L_V + m * 4096); }
    WAIT_VMN(32);
    RAWBAR();
    RAWBAR();
#pragma unroll 1
    for (int n = 0; n < 2; ++n) glds4((const unsigned char*)p.w2 + (unsigned)(cg * 8192 + n * 4096), l16, l16, l16, l16, lbase + L_W2 + n * 4096);
#pragma unroll 1
    for (int n0 = 0; n0 < 4; ++n0) {
      const int n = (n0 + j) & 3;
      const unsigned r0 = (unsigned)((4 * n) & 7) << 5;
      glds4((const unsigned char*)p.wo + (unsigned)((h * 512 + j * 16 + 4 * n) * 1024), l16 ^ r0, l16 ^ (r0 + 32u), l16 ^ (r0 + 64u), l16 ^ (r0 + 96u), lbase + L_WO + n * 4096);
    }
    WAIT_VMN(24);
    RAWBAR();
    WAIT_VMN(16);
    if (lane == 0) __hip_atomic_store(flg + 4, 1u, __ATOMIC_RELAXED, __HIP_MEMORY_SCOPE_WORKGROUP);
    WAIT_VM(0);
    RAWBAR();
  } else if (wave == 9) {
    const int lm = cg >> 3, kq = cg & 7;
    const int tsel = (c >> 1) & 3, prs = c & 1;
    {
      const v4u xr = *(const GAS v4u*)(p.x + lane * 8);
      const unsigned touch = __builtin_amdgcn_raw_buffer_load_b32(rs, W_TOUCH + (lane & 7) * 4, 0, 16);
      const unsigned long long a_b1 = (unsigned long long)(p.b1 + cg * 8), a_x = (unsigned long long)(p.x + col), a_b2 = (unsigned long long)(p.b2 + col);
      const unsigned long long a_sm = lane < 4 ? a_b1 + lane * 4 : (lane == 4 ? a_x : a_b2);
      const unsigned small = *(const GAS unsigned*)a_sm;
      __builtin_amdgcn_sched_barrier(0);
      RAWBAR();
      __builtin_amdgcn_sched_barrier(0);
      *(LAS v4u*)(L + L_Y + lane * 16) = ln_packed(ln_packed(xr));
      if (lane < 6) *(LAS unsigned*)(L + L_B1 + lane * 4) = small;
      asm volatile("" :: "v"(touch));
    }
    const int zoff = W_Z + ((lm * 8 + kq) * 8 + ((lane & 31) >> 2)) * 128 + (lane & 3) * 16;
    {
      const int q4 = c >> 2, pp = c & 3;
      const int ph = ((q4 >> 1) & 1) | ((g & 1) << 1);
      v4u yf[2];
#pragma unroll
      for (int s = 0; s < 2; ++s) yf[s] = *(const LAS v4u*)(L + L_Y + (64 * kq + 32 * s + 8 * g) * 2);
      for (unsigned spin = 0; spin < SPIN_MAX; ++spin) {
        const v4u f = *(volatile LAS v4u*)flg;
        asm volatile("" ::: "memory");
        if (__builtin_amdgcn_readfirstlane(f[1] & f[2]) != 0u) break;
        __builtin_amdgcn_s_sleep(1);
      }
      const v2u tg = *(const LAS v2u*)(L + L_TAG);
      f32x4 a0 = {0.f, 0.f, 0.f, 0.f};
#pragma unroll
      for (int t = 0; t < 4; ++t) {
        LAS unsigned char* ab = L + L_W1 + (8 * g + q4) * 128 + (((2 * t + (pp >> 1)) ^ (ph << 1)) * 16) + (pp & 1) * 8;
        const unsigned m = tsel == t ? ~0u : 0u;
#pragma unroll
        for (int s = 0; s < 2; ++s) { v4u ym; ym[0] = yf[s][0] & m; ym[1] = yf[s][1] & m; ym[2] = yf[s][2] & m; ym[3] = yf[s][3] & m; a0 = mfma32(cat(trd(ab + s * 4096), trd(ab + s * 4096 + 512)), ym, a0); }
      }
      v4u gr; gr[0] = __float_as_uint(prs ? a0[2] : a0[0]); gr[1] = tg[0]; gr[2] = __float_as_uint(prs ? a0[3] : a0[1]); gr[3] = tg[1];
      if (c < 8 && act) ST16(gr, W_Z + ((lm * 8 + 2 * tsel + (g >> 1)) * 8 + kq) * 128 + (2 * (g & 1) + prs) * 16);
    }
    DELAY(DLY_Q + 8);
    v4u zq;
    asm volatile("global_load_dwordx4 %0, %1, %2 sc1" : "=v"(zq) : "v"(zoff), "s"(p.ws) : "memory");
    RAWBAR();
    RAWBAR();
    asm volatile("s_waitcnt vmcnt(0)" : "+v"(zq) :: "memory");
    v2u hm[4];
    if (act) {
      const v2u tg = *(const LAS v2u*)(L + L_TAG);
      const unsigned tlo = tg[0], thi = tg[1];
      if (!NONEBAD(TAGBAD(zq))) {
        for (unsigned spin = 0; spin < SPIN_MAX; ++spin) {
          asm volatile("" ::: "memory");
          zq = LD16(zoff);
          if (NONEBAD(TAGBAD(zq))) break;
        }
      }
      float z0 = __uint_as_float(zq[0]), z1 = __uint_as_float(zq[2]);
      z0 += dpp<ROR4>(z0); z1 += dpp<ROR4>(z1); z0 += dpp<XOR8>(z0); z1 += dpp<XOR8>(z1); z0 = x16_sum(z0); z1 = x16_sum(z1);
      const unsigned bw = *(const LAS unsigned*)(L + L_B1 + (lane & 3) * 4);
      float hv[2];
#pragma unroll
      for (int e = 0; e < 2; ++e) {
        const float z = rbf(rbf(e ? z1 : z0) + (e ? bfhi(bw) : bflo(bw)));
        const float uu = 0.7978845608028654f * (z + 0.044715f * z * z * z);
        const float th = 1.f - 2.f * __builtin_amdgcn_rcpf(1.f + __builtin_amdgcn_exp2f(2.885390081777927f * uu));
        hv[e] = 0.5f * z * (1.f + th);
      }
      const unsigned hp = pkbf(hv[0], hv[1]);
      const unsigned h0 = __builtin_amdgcn_readlane(hp, 0), h1 = __builtin_amdgcn_readlane(hp, 1), h2 = __builtin_amdgcn_readlane(hp, 2), h3 = __builtin_amdgcn_readlane(hp, 3);
#pragma unroll
      for (int t = 0; t < 4; ++t) { const bool on = tsel == t && g < 2; hm[t][0] = on ? (g == 0 ? h0 : h2) : 0u; hm[t][1] = on ? (g == 0 ? h1 : h3) : 0u; }
    }
    RAWBAR();
    if (act) {
      for (unsigned spin = 0; spin < SPIN_MAX && lds_peek(flg + 4) == 0u; ++spin) __builtin_amdgcn_s_sleep(1);
      LAS unsigned char* ab = L + L_W2 + ((4 * g + (c >> 2)) & 7) * 1024 + (c & 3) * 8;
      LAS unsigned char* mb = L + L_MP + (16 * tsel + 4 * g + 2 * prs) * 4;
#pragma unroll 2
      for (int T = 0; T < 8; ++T) {
        f32x4 d = {0.f, 0.f, 0.f, 0.f};
#pragma unroll
        for (int t = 0; t < 4; ++t) d = mfma16(trd(ab + (4 * T + t) * 32), hm[t], d);
        f32x2 o2; o2[0] = prs ? d[2] : d[0]; o2[1] = prs ? d[3] : d[1];
        if (c < 8) *(LAS f32x2*)(mb + T * 256) = o2;
      }
    }
    BARRIER();
  } else {
    const int w = wave - 1;
    __builtin_amdgcn_s_setprio(2);
    const int lc = j >> 2, rq = j & 3;
    if (w == 0) {
      const v4u xr = *(const GAS v4u*)(p.x + lane * 8);
      const v2u ev = __builtin_amdgcn_raw_buffer_load_b64(rs, W_E, 0, 16);
      __builtin_amdgcn_sched_barrier(0);
      RAWBAR();
      __builtin_amdgcn_sched_barrier(0);
      *(LAS v4u*)(L + L_XN + lane * 16) = ln_packed(xr);
      const unsigned long long E = (unsigned long long)(unsigned)__builtin_amdgcn_readfirstlane(ev[0]) | ((unsigned long long)(unsigned)__builtin_amdgcn_readfirstlane(ev[1]) << 32);
      const unsigned long long T = E * 6364136223846793005ull + 1442695040888963407ull;
      if (lane == 0) { v2u tg; tg[0] = (unsigned)T; tg[1] = (unsigned)(T >> 32); *(LAS v2u*)(L + L_TAG) = tg; }
      asm volatile("s_waitcnt lgkmcnt(0)" ::: "memory");
      if (lane == 0) __hip_atomic_store(flg + 1, 1u, __ATOMIC_RELAXED, __HIP_MEMORY_SCOPE_WORKGROUP);
    } else {
      RAWBAR();
    }
    unsigned tlo, thi;
    {
      const int q4 = c >> 2, pp = c & 3, t = w & 3;
      int toff = L_WQ + (8 * g + q4) * 128 + (((2 * t + (pp >> 1)) ^ ((((q4 >> 1) & 1) | ((g & 1) << 1)) << 1)) * 16) + (pp & 1) * 8;
      int xoff = L_XN + (128 * rq + 8 * g) * 2;
      int off = W_Q + ((((h * 8 + lc) * 4 + rq) * 16) + 4 * t + g) * 16;
      PIN(toff); PIN(xoff); PIN(off);
      for (unsigned spin = 0; spin < SPIN_MAX && lds_peek(flg + 1) == 0u; ++spin) __builtin_amdgcn_s_sleep(1);
      const v2u tg = *(const LAS v2u*)(L + L_TAG);
      tlo = tg[0]; thi = tg[1];
      if (w < 4) {
        v4u xf[4];
#pragma unroll
        for (int s = 0; s < 4; ++s) xf[s] = *(const LAS v4u*)(L + xoff + s * 64);
        for (unsigned spin = 0; spin < SPIN_MAX && lds_peek(flg + 0) == 0u; ++spin) { }
        s16x4 a[4][2];
#pragma unroll
        for (int s = 0; s < 4; ++s) { a[s][0] = trd(L + toff + s * 4096); a[s][1] = trd(L + toff + s * 4096 + 512); }
        __builtin_amdgcn_sched_barrier(0);
        const f32x4 z4 = {0.f, 0.f, 0.f, 0.f};
        const f32x4 a0 = mfma32(cat(a[1][0], a[1][1]), xf[1], mfma32(cat(a[0][0], a[0][1]), xf[0], z4)) + mfma32(cat(a[3][0], a[3][1]), xf[3], mfma32(cat(a[2][0], a[2][1]), xf[2], z4));
        v4u gr; gr[0] = pkbf(a0[0], a0[1]); gr[1] = tlo; gr[2] = pkbf(a0[2], a0[3]); gr[3] = thi;
        if (c == 0) ST16(gr, off);
        if (lane == 0) lds_add(flg + 3, 1u);
      } else {
        for (unsigned spin = 0; spin < SPIN_MAX && lds_peek(flg + 3) < 4u; ++spin) __builtin_amdgcn_s_sleep(1);
      }
    }
    const int head = c & 3, kk = c >> 2, q4 = c >> 2, pp = c & 3, tsel = (c >> 1) & 3, prs = c & 1;
    int qoff = W_Q + (h * 8 + w) * 1024 + lane * 16;
    int aQw; { const int dim = (w & 1) * 64 + 4 * (lane & 15); aQw = L_Q + (((dim >> 5) * 4 + ((dim >> 3) & 3)) * 4 + (w >> 1)) * 16 + (dim & 7) * 2; }
    int aK[4];
#pragma unroll
    for (int s = 0; s < 4; ++s) aK[s] = L_K + (w * 16 + c) * 256 + (((4 * s + g) ^ c) * 16);
    int aQr = L_Q + (g * 4 + head) * 16;
    int aMw = L_MX + ((lane & 3) * 8 + w) * 4;
    int aMr = L_MX + head * 32;
    int aPw = L_P + (((w >> 1) * 4 + 2 * (w & 1) + (g >> 1)) * 4 + head) * 16 + (g & 1) * 8;
    int aPr = L_P + (g * 4 + head) * 16;
    int aV[2];
#pragma unroll
    for (int t2 = 0; t2 < 2; ++t2) aV[t2] = L_V + (8 * g + 4 * t2 + q4) * 256 + (((2 * w + (pp >> 1)) ^ ((q4 << 2) | ((2 * g + t2) & 3))) * 16) + (pp & 1) * 8;
    int ooff = W_O + ((((h * 32 + head * 8 + w) * 32 + j) * 8) + 2 * g + kk) * 16;
    int offa = W_O + (((h * 32 + j) * 32 + 8 * (w & 3)) * 8 + lane) * 16;
    int aOw = L_OP + (w & 3) * 80 + lane * 8, aOr = L_OP + g * 16;
    int aWo[4];
#pragma unroll
    for (int t = 0; t < 4; ++t) { const int row = 4 * g + (c >> 2); aWo[t] = L_WO + row * 1024 + ((2 * (4 * w + t) + ((c & 3) >> 1)) ^ ((row & 7) << 1)) * 16 + (c & 1) * 8; }
    int aMp = L_MP + (16 * (4 * w + tsel) + 4 * g + 2 * prs) * 4;
    int soff = W_S + (((2 * (4 * w + tsel) + (g >> 1)) * 256 + b) * 4 + 2 * (g & 1) + prs) * 32 + (c >> 3) * 16;
    int foff; { const int cc = 8 * cc_cg + cc_i; foff = W_S + (((cc >> 2) * 256 + 32 * w + (lane >> 1)) * 4 + (cc & 3)) * 32 + (lane & 1) * 16; }
    unsigned tex = (lane & 1) ? thi : tlo, otag = (kk & 1) ? thi : tlo;
    unsigned tm[4];
#pragma unroll
    for (int t = 0; t < 4; ++t) tm[t] = tsel == t ? ~0u : 0u;
    PIN(qoff); PIN(aQw); PIN(aK[0]); PIN(aK[1]); PIN(aK[2]); PIN(aK[3]); PIN(aQr); PIN(aMw); PIN(aMr); PIN(aPw); PIN(aPr); PIN(aV[0]); PIN(aV[1]);
    PIN(ooff); PIN(offa); PIN(aOw); PIN(aOr); PIN(aWo[0]); PIN(aWo[1]); PIN(aWo[2]); PIN(aWo[3]); PIN(aMp); PIN(soff); PIN(foff); PIN(tex); PIN(otag);
    PIN(tm[0]); PIN(tm[1]); PIN(tm[2]); PIN(tm[3]);
    DELAY(DLY_Q);
    v4u q0;
    for (unsigned spin = 0; spin < SPIN_MAX; ++spin) {
      asm volatile("" ::: "memory");
      q0 = LD16(qoff);
      if (NONEBAD(TAGBAD(q0))) break;
    }
    for (unsigned spin = 0; spin < SPIN_MAX && lds_peek(flg + 5) == 0u; ++spin) { }
    v4u kf[4];
#pragma unroll
    for (int s = 0; s < 4; ++s) kf[s] = *(const LAS v4u*)(L + aK[s]);
    {
      float u0 = bflo(q0[0]), u1 = bfhi(q0[0]), u2 = bflo(q0[2]), u3 = bfhi(q0[2]);
      u0 = x16_sum(u0); u1 = x16_sum(u1); u2 = x16_sum(u2); u3 = x16_sum(u3);
      u0 = x32_sum(u0); u1 = x32_sum(u1); u2 = x32_sum(u2); u3 = x32_sum(u3);
      v2u d; d[0] = pkbf(u0, u1); d[1] = pkbf(u2, u3);
      if (lane < 16) *(LAS v2u*)(L + aQw) = d;
    }
    BARRIER();
    f32x4 sc;
    {
      v4u qf[4];
#pragma unroll
      for (int s = 0; s < 4; ++s) qf[s] = *(const LAS v4u*)(L + aQr + s * 256);
      __builtin_amdgcn_sched_barrier(0);
      const f32x4 z4 = {0.f, 0.f, 0.f, 0.f};
      const f32x4 s0 = mfma32(kf[1], qf[1], mfma32(kf[0], qf[0], z4)), s1 = mfma32(kf[3], qf[3], mfma32(kf[2], qf[2], z4));
      sc = s0 + s1;
      int mx = imax(imax(fkey(sc[0]), fkey(sc[1])), imax(fkey(sc[2]), fkey(sc[3])));
      mx = x16_imax(mx); mx = x32_imax(mx);
      if (lane < 4) *(LAS int*)(L + aMw) = mx;
    }
    BARRIER();
    float mc;
    constexpr float C2 = 0.08838834764831845f * 1.4426950408889634f;
    {
      const v4u ma = *(const LAS v4u*)(L + aMr), mb = *(const LAS v4u*)(L + aMr + 16);
      const int M = imax(imax(imax((int)ma[0], (int)ma[1]), imax((int)ma[2], (int)ma[3])), imax(imax((int)mb[0], (int)mb[1]), imax((int)mb[2], (int)mb[3])));
      mc = funkey(M) * C2;
      float e[4];
#pragma unroll
      for (int r = 0; r < 4; ++r) e[r] = __builtin_amdgcn_exp2f(fmaf(sc[r], C2, -mc));
      v2u pk; pk[0] = pkbf(e[0], e[1]); pk[1] = pkbf(e[2], e[3]);
      if (c < 4) *(LAS v2u*)(L + aPw) = pk;
      float lsum = (bflo(pk[0]) + bfhi(pk[0])) + (bflo(pk[1]) + bfhi(pk[1]));
      lsum = x16_sum(lsum); lsum = x32_sum(lsum);
      if (lane < 4) *(LAS float*)(L + aMw + 128) = lsum;
    }
    BARRIER();
    {
      v4u pf[4]; s16x4 va[4][2];
#pragma unroll
      for (int s = 0; s < 4; ++s) { va[s][0] = trd(L + aV[0] + s * 8192); va[s][1] = trd(L + aV[1] + s * 8192); pf[s] = *(const LAS v4u*)(L + aPr + s * 256); }
      const f32x4 la = *(const LAS f32x4*)(L + aMr + 128), lb = *(const LAS f32x4*)(L + aMr + 144);
      __builtin_amdgcn_sched_barrier(0);
      const f32x4 z4 = {0.f, 0.f, 0.f, 0.f};
      const f32x4 o0 = mfma32(cat(va[1][0], va[1][1]), pf[1], mfma32(cat(va[0][0], va[0][1]), pf[0], z4));
      const f32x4 o1 = mfma32(cat(va[3][0], va[3][1]), pf[3], mfma32(cat(va[2][0], va[2][1]), pf[2], z4));
      const float l8 = ((la[0] + la[1]) + (la[2] + la[3])) + ((lb[0] + lb[1]) + (lb[2] + lb[3]));
      const f32x4 oa = o0 + o1;
      v4u gr; gr[0] = __float_as_uint((kk & 1) ? oa[2] : oa[0]); gr[1] = __float_as_uint((kk & 1) ? oa[3] : oa[1]); gr[2] = __float_as_uint((kk & 1) ? l8 : mc); gr[3] = otag;
      if (kk < 2) ST16(gr, ooff);
    }
    if (w < 4) {
      DELAY(DLY_O);
      v4u ga;
      for (unsigned spin = 0; spin < SPIN_MAX; ++spin) {
        asm volatile("" ::: "memory");
        ga = LD16(offa);
        if (NONEBAD(ga[3] ^ tex)) break;
      }
      const float aux = __uint_as_float(ga[2]), axn = dpp<XOR1>(aux);
      const float mj = (lane & 1) ? axn : aux, lj = (lane & 1) ? aux : axn;
      int mk = fkey(mj);
      mk = imax(mk, dppi<XOR8>(mk)); mk = x16_imax(mk); mk = x32_imax(mk);
      const float Mw = funkey(mk);
      const float wt = __builtin_amdgcn_exp2f(mj - Mw);
      float o0 = __uint_as_float(ga[0]) * wt, o1 = __uint_as_float(ga[1]) * wt, ll = lj * wt;
      o0 += dpp<XOR8>(o0); o1 += dpp<XOR8>(o1); ll += dpp<XOR8>(ll);
      o0 = x16_sum(o0); o1 = x16_sum(o1); ll = x16_sum(ll);
      o0 = x32_sum(o0); o1 = x32_sum(o1); ll = x32_sum(ll);
      if (lane < 9) { f32x2 o2; o2[0] = lane < 8 ? o0 : Mw; o2[1] = lane < 8 ? o1 : ll; *(LAS f32x2*)(L + aOw) = o2; }
    }
    BARRIER();
    {
      f32x4 ou[4]; f32x2 ml[4]; s16x4 a[4];
#pragma unroll
      for (int u = 0; u < 4; ++u) { ml[u] = *(const LAS f32x2*)(L + L_OP + u * 80 + 64); ou[u] = *(const LAS f32x4*)(L + aOr + u * 80); }
#pragma unroll
      for (int t = 0; t < 4; ++t) a[t] = trd(L + aWo[t]);
      f32x2 mv = *(const LAS f32x2*)(L + aMp);
      __builtin_amdgcn_sched_barrier(0);
      if (!act) { mv[0] = 0.f; mv[1] = 0.f; }
      const float M = funkey(imax(imax(fkey(ml[0][0]), fkey(ml[1][0])), imax(fkey(ml[2][0]), fkey(ml[3][0]))));
      float f[4];
#pragma unroll
      for (int u = 0; u < 4; ++u) f[u] = __builtin_amdgcn_exp2f(ml[u][0] - M);
      const float li = __builtin_amdgcn_rcpf((f[0] * ml[0][1] + f[1] * ml[1][1]) + (f[2] * ml[2][1] + f[3] * ml[3][1]));
      const f32x4 o4 = ((ou[0] * f[0] + ou[1] * f[1]) + (ou[2] * f[2] + ou[3] * f[3])) * li;
      v2u of; of[0] = pkbf(o4[0], o4[1]); of[1] = pkbf(o4[2], o4[3]);
      const f32x4 z4 = {0.f, 0.f, 0.f, 0.f};
      v2u om[4];
#pragma unroll
      for (int t = 0; t < 4; ++t) { om[t][0] = of[0] & tm[t]; om[t][1] = of[1] & tm[t]; }
      const f32x4 d = mfma16(a[1], om[1], mfma16(a[0], om[0], z4)) + mfma16(a[3], om[3], mfma16(a[2], om[2], z4));
      v4u gr; gr[0] = __float_as_uint(c < 8 ? (prs ? d[2] : d[0]) : mv[0]); gr[1] = tlo; gr[2] = __float_as_uint(c < 8 ? (prs ? d[3] : d[1]) : mv[1]); gr[3] = thi;
      ST16(gr, soff);
    }
    {
      DELAY(DLY_S);
      v4u gq;
      for (unsigned spin = 0; spin < SPIN_MAX; ++spin) {
        asm volatile("" ::: "memory");
        gq = LD16(foff);
        if (NONEBAD(TAGBAD(gq))) break;
      }
      float a0 = __uint_as_float(gq[0]), a1 = __uint_as_float(gq[2]);
      a0 += dpp<XOR2>(a0); a1 += dpp<XOR2>(a1); a0 += dpp<ROR4>(a0); a1 += dpp<ROR4>(a1); a0 += dpp<XOR8>(a0); a1 += dpp<XOR8>(a1);
      a0 = x16_sum(a0); a1 = x16_sum(a1); a0 = x32_sum(a0); a1 = x32_sum(a1);
      if (lane < 2) { f32x2 o2; o2[0] = a0; o2[1] = a1; *(LAS f32x2*)(L + L_FS + lane * 64 + w * 8) = o2; }
    }
    BARRIER();
    if (w == 0) {
      const int e = (lane >> 3) & 1, u = lane & 7;
      const unsigned xw = *(const LAS unsigned*)(L + L_XB), b2w = *(const LAS unsigned*)(L + L_XB + 4);
      float s = *(const LAS float*)(L + L_FS + u * 8 + e * 4), m = *(const LAS float*)(L + L_FM + u * 8 + e * 4);
      s += dpp<XOR1>(s); m += dpp<XOR1>(m); s += dpp<XOR2>(s); m += dpp<XOR2>(m); s += dpp<XOR7>(s); m += dpp<XOR7>(m);
      const float z = rbf(rbf(m) + (e ? bfhi(b2w) : bflo(b2w)));
      const float gt = rbf(z * __builtin_amdgcn_rcpf(1.f + __builtin_amdgcn_exp2f(-1.4426950408889634f * z)));
      const float r = (e ? bfhi(xw) : bflo(xw)) + rbf(rbf(s) * gt);
      const float rn = dpp<XOR8>(r);
      if (lane == 0) {
        *(GAS unsigned*)(p.out + col) = pkbf(r, rn);
        if (b == 0) { const unsigned long long T = (unsigned long long)tlo | ((unsigned long long)thi << 32); __hip_atomic_store((GAS unsigned long long*)(p.ws + W_E), T, __ATOMIC_RELAXED, __HIP_MEMORY_SCOPE_AGENT); }
      }
    }
  }
}

extern "C" void kernel_launch(void* const* d_in, const int* in_sizes, int n_in,
                              void* d_out, int out_size, void* d_ws, size_t ws_size,
                              hipStream_t stream) {
  Params p;
  p.x = (const u16*)d_in[0]; p.kc = (const u16*)d_in[1]; p.vc = (const u16*)d_in[2]; p.wq = (const u16*)d_in[3]; p.wo = (const u16*)d_in[4];
  p.w1 = (const u16*)d_in[6]; p.b1 = (const u16*)d_in[7]; p.w2 = (const u16*)d_in[8]; p.b2 = (const u16*)d_in[9];
  p.out = (u16*)d_out; p.ws = (unsigned char*)d_ws;
  fused_kernel<<<NB, NTHR, 0, stream>>>(p);
}
```

```cpp
#include <hip/hip_runtime.h>
#include <hip/hip_bf16.h>

#define LAS __attribute__((address_space(3)))
#define GAS __attribute__((address_space(1)))
typedef unsigned short u16;
typedef unsigned v4u __attribute__((ext_vector_type(4)));
typedef unsigned v2u __attribute__((ext_vector_type(2)));
typedef float f32x4 __attribute__((ext_vector_type(4)));
typedef float f32x2 __attribute__((ext_vector_type(2)));
typedef short s16x4 __attribute__((ext_vector_type(4)));
typedef short s16x8 __attribute__((ext_vector_type(8)));
typedef __bf16 bf16x8 __attribute__((ext_vector_type(8)));
typedef __bf16 bf16x2 __attribute__((ext_vector_type(2)));

constexpr int DLY_Q = 24, DLY_O = 24, DLY_S = 30;
constexpr int LWIN = 28;

constexpr int D = 512, SK = 65536, WIN = 4096, FF = 1792;
constexpr int NB = 256, NTHR = 640;
constexpr int L_WQ = 0, L_K = 16384, L_V = 49152, L_WO = 81920, L_W2 = 98304, L_W1 = 106496, L_XN = 114688, L_Y = 115712, L_Q = 116736, L_P = 117760;
constexpr int L_MX = 118784, L_SM = L_MX + 128, L_TAG = L_SM + 128, L_OP = L_TAG + 16, L_FS = L_OP + 512, L_FM = L_FS + 64, L_B1 = L_FM + 64, L_XB = L_B1 + 16, L_FLG = L_XB + 16, L_MP = L_FLG + 32, L_END = L_MP + 2048;
constexpr int W_E = 0;
constexpr int W_Q = 4096;
constexpr int W_Z = W_Q + 131072;
constexpr int W_O = W_Z + 262144;
constexpr int W_S = W_O + 1048576;
constexpr int W_TOUCH = W_S + 2097152;
constexpr int W_END = W_TOUCH + 4096;
constexpr unsigned SPIN_MAX = 1u << 17;

__device__ __forceinline__ float bflo(unsigned w) { return __uint_as_float(w << 16); }
__device__ __forceinline__ float bfhi(unsigned w) { return __uint_as_float(w & 0xffff0000u); }
__device__ __forceinline__ unsigned pkbf(float lo, float hi) { bf16x2 r; r[0] = (__bf16)lo; r[1] = (__bf16)hi; return __builtin_bit_cast(unsigned, r); }
__device__ __forceinline__ float rbf(float f) { return bflo(pkbf(f, f)); }
template <int CTRL> __device__ __forceinline__ float dpp(float x) {
  return __builtin_bit_cast(float, __builtin_amdgcn_mov_dpp(__builtin_bit_cast(int, x), CTRL, 0xf, 0xf, true));
}
constexpr int XOR1 = 0xB1, XOR2 = 0x4E, XOR7 = 0x141, XOR15 = 0x140, XOR8 = 0x128, ROR4 = 0x124;
__device__ __forceinline__ int fkey(float f) { const int i = __float_as_int(f); return i ^ ((i >> 31) & 0x7fffffff); }
__device__ __forceinline__ float funkey(int k) { return __int_as_float(k ^ ((k >> 31) & 0x7fffffff)); }
template <int CTRL> __device__ __forceinline__ int dppi(int x) { return __builtin_amdgcn_mov_dpp(x, CTRL, 0xf, 0xf, true); }
__device__ __forceinline__ int imax(int a, int b) { return a > b ? a : b; }
__device__ __forceinline__ int x16_imax(int x) { auto s = __builtin_amdgcn_permlane16_swap((unsigned)x, (unsigned)x, false, false); return imax((int)s[0], (int)s[1]); }
__device__ __forceinline__ int x32_imax(int x) { auto s = __builtin_amdgcn_permlane32_swap((unsigned)x, (unsigned)x, false, false); return imax((int)s[0], (int)s[1]); }
__device__ __forceinline__ float x16_sum(float x) { auto s = __builtin_amdgcn_permlane16_swap(__float_as_uint(x), __float_as_uint(x), false, false); return __uint_as_float(s[0]) + __uint_as_float(s[1]); }
__device__ __forceinline__ float x32_sum(float x) { auto s = __builtin_amdgcn_permlane32_swap(__float_as_uint(x), __float_as_uint(x), false, false); return __uint_as_float(s[0]) + __uint_as_float(s[1]); }
__device__ __forceinline__ void wave_sum2(float& a, float& b) {
  a += dpp<XOR1>(a); b += dpp<XOR1>(b); a += dpp<XOR2>(a); b += dpp<XOR2>(b); a += dpp<XOR7>(a); b += dpp<XOR7>(b); a += dpp<XOR15>(a); b += dpp<XOR15>(b);
  a = x16_sum(a); b = x16_sum(b); a = x32_sum(a); b = x32_sum(b);
}
__device__ __forceinline__ f32x4 mfma32(v4u a, v4u b, f32x4 c) {
  return __builtin_amdgcn_mfma_f32_16x16x32_bf16(__builtin_bit_cast(bf16x8, a), __builtin_bit_cast(bf16x8, b), c, 0, 0, 0);
}
__device__ __forceinline__ f32x4 mfma16(s16x4 a, v2u b, f32x4 c) {
  return __builtin_amdgcn_mfma_f32_16x16x16bf16_1k(a, __builtin_bit_cast(s16x4, b), c, 0, 0, 0);
}
__device__ __forceinline__ s16x4 trd(LAS unsigned char* p) { return __builtin_amdgcn_ds_read_tr16_b64_v4i16((LAS s16x4*)p); }
__device__ __forceinline__ v4u cat(s16x4 lo, s16x4 hi) { return __builtin_bit_cast(v4u, __builtin_shufflevector(lo, hi, 0, 1, 2, 3, 4, 5, 6, 7)); }
__device__ __forceinline__ v4u ln_packed(v4u x) {
  float v[8];
#pragma unroll
  for (int i = 0; i < 4; ++i) { v[2 * i] = bflo(x[i]); v[2 * i + 1] = bfhi(x[i]); }
  const float piv = __uint_as_float(__builtin_amdgcn_readfirstlane(x[0]) << 16);
  float s = 0.f, q = 0.f;
#pragma unroll
  for (int i = 0; i < 8; ++i) { const float d = v[i] - piv; s += d; q = fmaf(d, d, q); }
  wave_sum2(s, q);
  const float md = s * (1.0f / D);
  const float var = rbf(fmaxf(fmaf(-md, md, q * (1.0f / D)), 0.f));
  const float m = rbf(piv + md);
  const float sd = rbf(__builtin_amdgcn_sqrtf(rbf(var + 1e-5f)));
  const float inv = __builtin_amdgcn_rcpf(sd);
  v4u r;
#pragma unroll
  for (int i = 0; i < 4; ++i) r[i] = pkbf(rbf(v[2 * i] - m) * inv, rbf(v[2 * i + 1] - m) * inv);
  return r;
}
#define WAIT_VM(n) asm volatile("s_waitcnt vmcnt(" #n ")" ::: "memory")
#define WAIT_VMN(n) asm volatile("s_waitcnt vmcnt(%0)" :: "i"(n) : "memory")
static_assert(LWIN % 4 == 0 && LWIN >= 8 && LWIN <= 40, "loader window");
#define BARRIER() asm volatile("s_waitcnt lgkmcnt(0)\n\ts_barrier" ::: "memory")
#define RAWBAR() asm volatile("s_barrier" ::: "memory")
__device__ __forceinline__ void glds4(const void* sbase, unsigned v0, unsigned v1, unsigned v2, unsigned v3, unsigned ldsaddr) {
  unsigned keep;
  asm volatile("s_mov_b32 %0, m0\n\ts_mov_b32 m0, %6\n\ts_nop 0\n\tglobal_load_lds_dwordx4 %1, %5 nt\n\tglobal_load_lds_dwordx4 %2, %5 offset:1024 nt\n\t"
               "global_load_lds_dwordx4 %3, %5 offset:2048 nt\n\tglobal_load_lds_dwordx4 %4, %5 offset:3072 nt\n\ts_mov_b32 m0, %0"
               : "=&s"(keep) : "v"(v0), "v"(v1), "v"(v2), "v"(v3), "s"(sbase), "s"(ldsaddr) : "memory");
}
#define DELAY(n) do { if ((n) > 0) __builtin_amdgcn_s_sleep((n) > 127 ? 127 : (n)); } while (0)
#define LD16(off) __builtin_amdgcn_raw_buffer_load_b128(rs, (off), 0, 16)
#define ST16(v, off) __builtin_amdgcn_raw_buffer_store_b128((v), rs, (off), 0, 16)
#define TAGBAD(x) (((x)[1] ^ tlo) | ((x)[3] ^ thi))
#define NONEBAD(v) (__builtin_amdgcn_ballot_w64((v) != 0u) == 0ull)
#define PIN(x) asm volatile("" : "+v"(x))
__device__ __forceinline__ void lds_add(LAS unsigned* q, unsigned v) { __hip_atomic_fetch_add(q, v, __ATOMIC_RELAXED, __HIP_MEMORY_SCOPE_WORKGROUP); }
__device__ __forceinline__ unsigned lds_peek(LAS unsigned* q) { return __builtin_amdgcn_readfirstlane(__hip_atomic_load(q, __ATOMIC_RELAXED, __HIP_MEMORY_SCOPE_WORKGROUP)); }

constexpr int first_group(int need) { for (int g = 6; g < 22; ++g) if (4 * g - (LWIN - 4) >= need) return g; return 99; }

struct Params {
  const u16* x; const u16* kc; const u16* vc; const u16* wq; const u16* wo;
  const u16* w1; const u16* b1; const u16* w2; const u16* b2; u16* out; unsigned char* ws;
};

__global__ void __launch_bounds__(NTHR, 3) fused_kernel(Params p) {
  __shared__ __attribute__((aligned(16))) unsigned char lds[L_END];
  LAS unsigned char* L = (LAS unsigned char*)lds;
  const unsigned lbase = (unsigned)(size_t)L;
  const int tid = threadIdx.x, lane = tid & 63, wave = __builtin_amdgcn_readfirstlane(tid >> 6);
  const int b = blockIdx.x, h = b & 7, j = b >> 3;
  const int g = lane >> 4, c = lane & 15;
  asm volatile("" :: "s"(p.x), "s"(p.kc), "s"(p.vc), "s"(p.wq), "s"(p.wo), "s"(p.w1), "s"(p.b1), "s"(p.w2), "s"(p.b2), "s"(p.out), "s"(p.ws));
  const __amdgpu_buffer_rsrc_t rs = __builtin_amdgcn_make_buffer_rsrc((void*)p.ws, 0, W_END, 0x00020000);
  const bool act = j < 28;
  const int cg = h * 28 + (act ? j : j - 28);
  const int cc_cg = h * 4 + (j >> 3), cc_i = j & 7;
  const int col = 16 * cc_cg + 2 * cc_i;

  LAS unsigned* flg = (LAS unsigned*)(L + L_FLG);
  if (tid < 2) { v4u z = {0u, 0u, 0u, 0u}; *(LAS v4u*)(L + L_FLG + tid * 16) = z; }
  if (wave == 0) {
    const int lc = j >> 2, rq = j & 3, lm = cg >> 3, kq = cg & 7;
    __builtin_amdgcn_s_setprio(3);
    const unsigned kvoff = ((unsigned)h * SK + (SK - WIN)) * 256u + (unsigned)j * 4096u;
    const unsigned char* kb = (const unsigned char*)p.kc + kvoff;
    const unsigned char* vb = (const unsigned char*)p.vc + kvoff;
    const unsigned k0 = (unsigned)g * 256u + (unsigned)((c ^ g) * 16), v0 = (unsigned)g * 256u + (unsigned)((c ^ (g << 2)) * 16), l16 = (unsigned)lane * 16u;
    BARRIER();
    {
      const unsigned ph = (unsigned)(lane >> 4) & 1u, c8 = (unsigned)lane & 7u;
      const unsigned ce = (c8 ^ (ph << 1)) * 16u, co = (c8 ^ ((ph | 2u) << 1)) * 16u;
      const unsigned rq8 = (unsigned)(lane >> 3) * 8192u, r18 = (unsigned)(lane >> 3) * (unsigned)(FF * 2);
      const unsigned char* sq = (const unsigned char*)p.wq + (unsigned)((h * 512 + lc * 64) * 2 + rq * 128 * 8192);
      const unsigned char* s1 = (const unsigned char*)p.w1 + (unsigned)(lm * 128 + kq * 64 * (FF * 2));
      constexpr unsigned SQ = 8u * 8192u - 1024u, S1 = 8u * (FF * 2) - 1024u;
#pragma unroll 1
      for (int n = 0; n < 4; ++n) glds4(sq + (unsigned)(n * 32 * 8192), rq8 + ce, rq8 + co + SQ, rq8 + ce + 2u * SQ, rq8 + co + 3u * SQ, lbase + L_WQ + n * 4096);
#pragma unroll 1
      for (int n = 0; n < 2; ++n) glds4(s1 + (unsigned)(n * 32 * (FF * 2)), r18 + ce, r18 + co + S1, r18 + ce + 2u * S1, r18 + co + 3u * S1, lbase + L_W1 + n * 4096);
    }
    constexpr int GFQ = first_group(16), GF1 = first_group(24), GFK = first_group(56);
    static_assert(GFQ <= 21 && GF1 <= 21, "flags are set inside the K and V loops");
#define GROUP_FLAGS(gi) do { \
      if ((gi) == GFQ && lane == 0) __hip_atomic_store(flg + 0, 1u, __ATOMIC_RELAXED, __HIP_MEMORY_SCOPE_WORKGROUP); \
      if ((gi) == GF1 && lane == 0) __hip_atomic_store(flg + 2, 1u, __ATOMIC_RELAXED, __HIP_MEMORY_SCOPE_WORKGROUP); \
      if ((gi) == GFK && lane == 0) __hip_atomic_store(flg + 5, 1u, __ATOMIC_RELAXED, __HIP_MEMORY_SCOPE_WORKGROUP); } while (0)
#pragma unroll 1
    for (int n = 0; n < 8; ++n) { const int m = (n + h) & 7; WAIT_VMN(LWIN - 4); GROUP_FLAGS(6 + n); glds4(kb + m * 131072, k0, k0 ^ 64u, k0 ^ 128u, k0 ^ 192u, lbase + L_K + m * 4096); }
#pragma unroll 1
    for (int n = 0; n < 8; ++n) { const int m = (n + h) & 7; WAIT_VMN(LWIN - 4); GROUP_FLAGS(14 + n); glds4(vb + m * 131072, v0, v0 ^ 16u, v0 ^ 32u, v0 ^ 48u, lbase + L_V + m * 4096); }
    WAIT_VMN(32);
    if (GFK > 21 && lane == 0) __hip_atomic_store(flg + 5, 1u, __ATOMIC_RELAXED, __HIP_MEMORY_SCOPE_WORKGROUP);
    RAWBAR();
    RAWBAR();
#pragma unroll 1
    for (int n = 0; n < 2; ++n) glds4((const unsigned char*)p.w2 + (unsigned)(cg * 8192 + n * 4096), l16, l16, l16, l16, lbase + L_W2 + n * 4096);
#pragma unroll 1
    for (int n0 = 0; n0 < 4; ++n0) {
      const int n = (n0 + j) & 3;
      const unsigned r0 = (unsigned)((4 * n) & 7) << 5;
      glds4((const unsigned char*)p.wo + (unsigned)((h * 512 + j * 16 + 4 * n) * 1024), l16 ^ r0, l16 ^ (r0 + 32u), l16 ^ (r0 + 64u), l16 ^ (r0 + 96u), lbase + L_WO + n * 4096);
    }
    WAIT_VMN(24);
    RAWBAR();
    WAIT_VMN(16);
    if (lane == 0) __hip_atomic_store(flg + 4, 1u, __ATOMIC_RELAXED, __HIP_MEMORY_SCOPE_WORKGROUP);
    WAIT_VM(0);
    RAWBAR();
  } else if (wave == 9) {
    const int lm = cg >> 3, kq = cg & 7;
    const int tsel = (c >> 1) & 3, prs = c & 1;
    {
      const v4u xr = *(const GAS v4u*)(p.x + lane * 8);
      const unsigned touch = __builtin_amdgcn_raw_buffer_load_b32(rs, W_TOUCH + (lane & 7) * 4, 0, 16);
      const unsigned long long a_b1 = (unsigned long long)(p.b1 + cg * 8), a_x = (unsigned long long)(p.x + col), a_b2 = (unsigned long long)(p.b2 + col);
      const unsigned long long a_sm = lane < 4 ? a_b1 + lane * 4 : (lane == 4 ? a_x : a_b2);
      const unsigned small = *(const GAS unsigned*)a_sm;
      __builtin_amdgcn_sched_barrier(0);
      RAWBAR();
      __builtin_amdgcn_sched_barrier(0);
      *(LAS v4u*)(L + L_Y + lane * 16) = ln_packed(ln_packed(xr));
      if (lane < 6) *(LAS unsigned*)(L + L_B1 + lane * 4) = small;
      asm volatile("" :: "v"(touch));
    }
    const int zoff = W_Z + ((lm * 8 + kq) * 8 + ((lane & 31) >> 2)) * 128 + (lane & 3) * 16;
    {
      const int q4 = c >> 2, pp = c & 3;
      const int ph = ((q4 >> 1) & 1) | ((g & 1) << 1);
      v4u yf[2];
#pragma unroll
      for (int s = 0; s < 2; ++s) yf[s] = *(const LAS v4u*)(L + L_Y + (64 * kq + 32 * s + 8 * g) * 2);
      for (unsigned spin = 0; spin < SPIN_MAX; ++spin) {
        const v4u f = *(volatile LAS v4u*)flg;
        asm volatile("" ::: "memory");
        if (__builtin_amdgcn_readfirstlane(f[1] & f[2]) != 0u) break;
        __builtin_amdgcn_s_sleep(1);
      }
      const v2u tg = *(const LAS v2u*)(L + L_TAG);
      f32x4 a0 = {0.f, 0.f, 0.f, 0.f};
#pragma unroll
      for (int t = 0; t < 4; ++t) {
        LAS unsigned char* ab = L + L_W1 + (8 * g + q4) * 128 + (((2 * t + (pp >> 1)) ^ (ph << 1)) * 16) + (pp & 1) * 8;
        const unsigned m = tsel == t ? ~0u : 0u;
#pragma unroll
        for (int s = 0; s < 2; ++s) { v4u ym; ym[0] = yf[s][0] & m; ym[1] = yf[s][1] & m; ym[2] = yf[s][2] & m; ym[3] = yf[s][3] & m; a0 = mfma32(cat(trd(ab + s * 4096), trd(ab + s * 4096 + 512)), ym, a0); }
      }
      v4u gr; gr[0] = __float_as_uint(prs ? a0[2] : a0[0]); gr[1] = tg[0]; gr[2] = __float_as_uint(prs ? a0[3] : a0[1]); gr[3] = tg[1];
      if (c < 8 && act) ST16(gr, W_Z + ((lm * 8 + 2 * tsel + (g >> 1)) * 8 + kq) * 128 + (2 * (g & 1) + prs) * 16);
    }
    DELAY(DLY_Q + 8);
    v4u zq;
    asm volatile("global_load_dwordx4 %0, %1, %2 sc1" : "=v"(zq) : "v"(zoff), "s"(p.ws) : "memory");
    RAWBAR();
    RAWBAR();
    asm volatile("s_waitcnt vmcnt(0)" : "+v"(zq) :: "memory");
    v2u hm[4];
    if (act) {
      const v2u tg = *(const LAS v2u*)(L + L_TAG);
      const unsigned tlo = tg[0], thi = tg[1];
      if (!NONEBAD(TAGBAD(zq))) {
        for (unsigned spin = 0; spin < SPIN_MAX; ++spin) {
          asm volatile("" ::: "memory");
          zq = LD16(zoff);
          if (NONEBAD(TAGBAD(zq))) break;
        }
      }
      float z0 = __uint_as_float(zq[0]), z1 = __uint_as_float(zq[2]);
      z0 += dpp<ROR4>(z0); z1 += dpp<ROR4>(z1); z0 += dpp<XOR8>(z0); z1 += dpp<XOR8>(z1); z0 = x16_sum(z0); z1 = x16_sum(z1);
      const unsigned bw = *(const LAS unsigned*)(L + L_B1 + (lane & 3) * 4);
      float hv[2];
#pragma unroll
      for (int e = 0; e < 2; ++e) {
        const float z = rbf(rbf(e ? z1 : z0) + (e ? bfhi(bw) : bflo(bw)));
        const float uu = 0.7978845608028654f * (z + 0.044715f * z * z * z);
        const float th = 1.f - 2.f * __builtin_amdgcn_rcpf(1.f + __builtin_amdgcn_exp2f(2.885390081777927f * uu));
        hv[e] = 0.5f * z * (1.f + th);
      }
      const unsigned hp = pkbf(hv[0], hv[1]);
      const unsigned h0 = __builtin_amdgcn_readlane(hp, 0), h1 = __builtin_amdgcn_readlane(hp, 1), h2 = __builtin_amdgcn_readlane(hp, 2), h3 = __builtin_amdgcn_readlane(hp, 3);
#pragma unroll
      for (int t = 0; t < 4; ++t) { const bool on = tsel == t && g < 2; hm[t][0] = on ? (g == 0 ? h0 : h2) : 0u; hm[t][1] = on ? (g == 0 ? h1 : h3) : 0u; }
    }
    RAWBAR();
    if (act) {
      for (unsigned spin = 0; spin < SPIN_MAX && lds_peek(flg + 4) == 0u; ++spin) __builtin_amdgcn_s_sleep(1);
      LAS unsigned char* ab = L + L_W2 + ((4 * g + (c >> 2)) & 7) * 1024 + (c & 3) * 8;
      LAS unsigned char* mb = L + L_MP + (16 * tsel + 4 * g + 2 * prs) * 4;
#pragma unroll 2
      for (int T = 0; T < 8; ++T) {
        f32x4 d = {0.f, 0.f, 0.f, 0.f};
#pragma unroll
        for (int t = 0; t < 4; ++t) d = mfma16(trd(ab + (4 * T + t) * 32), hm[t], d);
        f32x2 o2; o2[0] = prs ? d[2] : d[0]; o2[1] = prs ? d[3] : d[1];
        if (c < 8) *(LAS f32x2*)(mb + T * 256) = o2;
      }
    }
    BARRIER();
  } else {
    const int w = wave - 1;
    __builtin_amdgcn_s_setprio(2);
    const int lc = j >> 2, rq = j & 3;
    if (w == 0) {
      const v4u xr = *(const GAS v4u*)(p.x + lane * 8);
      const v2u ev = __builtin_amdgcn_raw_buffer_load_b64(rs, W_E, 0, 16);
      __builtin_amdgcn_sched_barrier(0);
      RAWBAR();
      __builtin_amdgcn_sched_barrier(0);
      *(LAS v4u*)(L + L_XN + lane * 16) = ln_packed(xr);
      const unsigned long long E = (unsigned long long)(unsigned)__builtin_amdgcn_readfirstlane(ev[0]) | ((unsigned long long)(unsigned)__builtin_amdgcn_readfirstlane(ev[1]) << 32);
      const unsigned long long T = E * 6364136223846793005ull + 1442695040888963407ull;
      if (lane == 0) { v2u tg; tg[0] = (unsigned)T; tg[1] = (unsigned)(T >> 32); *(LAS v2u*)(L + L_TAG) = tg; }
      asm volatile("s_waitcnt lgkmcnt(0)" ::: "memory");
      if (lane == 0) __hip_atomic_store(flg + 1, 1u, __ATOMIC_RELAXED, __HIP_MEMORY_SCOPE_WORKGROUP);
    } else {
      RAWBAR();
    }
    unsigned tlo, thi;
    {
      const int q4 = c >> 2, pp = c & 3, t = w & 3;
      int toff = L_WQ + (8 * g + q4) * 128 + (((2 * t + (pp >> 1)) ^ ((((q4 >> 1) & 1) | ((g & 1) << 1)) << 1)) * 16) + (pp & 1) * 8;
      int xoff = L_XN + (128 * rq + 8 * g) * 2;
      int off = W_Q + ((((h * 8 + lc) * 4 + rq) * 16) + 4 * t + g) * 16;
      PIN(toff); PIN(xoff); PIN(off);
      for (unsigned spin = 0; spin < SPIN_MAX && lds_peek(flg + 1) == 0u; ++spin) __builtin_amdgcn_s_sleep(1);
      const v2u tg = *(const LAS v2u*)(L + L_TAG);
      tlo = tg[0]; thi = tg[1];
      if (w < 4) {
        v4u xf[4];
#pragma unroll
        for (int s = 0; s < 4; ++s) xf[s] = *(const LAS v4u*)(L + xoff + s * 64);
        for (unsigned spin = 0; spin < SPIN_MAX && lds_peek(flg + 0) == 0u; ++spin) { }
        s16x4 a[4][2];
#pragma unroll
        for (int s = 0; s < 4; ++s) { a[s][0] = trd(L + toff + s * 4096); a[s][1] = trd(L + toff + s * 4096 + 512); }
        __builtin_amdgcn_sched_barrier(0);
        const f32x4 z4 = {0.f, 0.f, 0.f, 0.f};
        const f32x4 a0 = mfma32(cat(a[1][0], a[1][1]), xf[1], mfma32(cat(a[0][0], a[0][1]), xf[0], z4)) + mfma32(cat(a[3][0], a[3][1]), xf[3], mfma32(cat(a[2][0], a[2][1]), xf[2], z4));
        v4u gr; gr[0] = pkbf(a0[0], a0[1]); gr[1] = tlo; gr[2] = pkbf(a0[2], a0[3]); gr[3] = thi;
        if (c == 0) ST16(gr, off);
        if (lane == 0) lds_add(flg + 3, 1u);
      } else {
        for (unsigned spin = 0; spin < SPIN_MAX && lds_peek(flg + 3) < 4u; ++spin) __builtin_amdgcn_s_sleep(1);
      }
    }
    const int head = c & 3, kk = c >> 2, q4 = c >> 2, pp = c & 3, tsel = (c >> 1) & 3, prs = c & 1;
    int qoff = W_Q + (h * 8 + w) * 1024 + lane * 16;
    int aQw; { const int dim = (w & 1) * 64 + 4 * (lane & 15); aQw = L_Q + (((dim >> 5) * 4 + ((dim >> 3) & 3)) * 4 + (w >> 1)) * 16 + (dim & 7) * 2; }
    int aK[4];
#pragma unroll
    for (int s = 0; s < 4; ++s) aK[s] = L_K + (w * 16 + c) * 256 + (((4 * s + g) ^ c) * 16);
    int aQr = L_Q + (g * 4 + head) * 16;
    int aMw = L_MX + ((lane & 3) * 8 + w) * 4;
    int aMr = L_MX + head * 32;
    int aPw = L_P + (((w >> 1) * 4 + 2 * (w & 1) + (g >> 1)) * 4 + head) * 16 + (g & 1) * 8;
    int aPr = L_P + (g * 4 + head) * 16;
    int aV[2];
#pragma unroll
    for (int t2 = 0; t2 < 2; ++t2) aV[t2] = L_V + (8 * g + 4 * t2 + q4) * 256 + (((2 * w + (pp >> 1)) ^ ((q4 << 2) | ((2 * g + t2) & 3))) * 16) + (pp & 1) * 8;
    int ooff = W_O + ((((h * 32 + head * 8 + w) * 32 + j) * 8) + 2 * g + kk) * 16;
    int offa = W_O + (((h * 32 + j) * 32 + 8 * (w & 3)) * 8 + lane) * 16;
    int aOw = L_OP + (w & 3) * 80 + lane * 8, aOr = L_OP + g * 16;
    int aWo[4];
#pragma unroll
    for (int t = 0; t < 4; ++t) { const int row = 4 * g + (c >> 2); aWo[t] = L_WO + row * 1024 + ((2 * (4 * w + t) + ((c & 3) >> 1)) ^ ((row & 7) << 1)) * 16 + (c & 1) * 8; }
    int aMp = L_MP + (16 * (4 * w + tsel) + 4 * g + 2 * prs) * 4;
    int soff = W_S + (((2 * (4 * w + tsel) + (g >> 1)) * 256 + b) * 4 + 2 * (g & 1) + prs) * 32 + (c >> 3) * 16;
    int foff; { const int cc = 8 * cc_cg + cc_i; foff = W_S + (((cc >> 2) * 256 + 32 * w + (lane >> 1)) * 4 + (cc & 3)) * 32 + (lane & 1) * 16; }
    unsigned tex = (lane & 1) ? thi : tlo, otag = (kk & 1) ? thi : tlo;
    unsigned tm[4];
#pragma unroll
    for (int t = 0; t < 4; ++t) tm[t] = tsel == t ? ~0u : 0u;
    PIN(qoff); PIN(aQw); PIN(aK[0]); PIN(aK[1]); PIN(aK[2]); PIN(aK[3]); PIN(aQr); PIN(aMw); PIN(aMr); PIN(aPw); PIN(aPr); PIN(aV[0]); PIN(aV[1]);
    PIN(ooff); PIN(offa); PIN(aOw); PIN(aOr); PIN(aWo[0]); PIN(aWo[1]); PIN(aWo[2]); PIN(aWo[3]); PIN(aMp); PIN(soff); PIN(foff); PIN(tex); PIN(otag);
    PIN(tm[0]); PIN(tm[1]); PIN(tm[2]); PIN(tm[3]);
    DELAY(DLY_Q);
    v4u q0;
    for (unsigned spin = 0; spin < SPIN_MAX; ++spin) {
      asm volatile("" ::: "memory");
      q0 = LD16(qoff);
      if (NONEBAD(TAGBAD(q0))) break;
    }
    for (unsigned spin = 0; spin < SPIN_MAX && lds_peek(flg + 5) == 0u; ++spin) { }
    v4u kf[4];
#pragma unroll
    for (int s = 0; s < 4; ++s) kf[s] = *(const LAS v4u*)(L + aK[s]);
    {
      float u0 = bflo(q0[0]), u1 = bfhi(q0[0]), u2 = bflo(q0[2]), u3 = bfhi(q0[2]);
      u0 = x16_sum(u0); u1 = x16_sum(u1); u2 = x16_sum(u2); u3 = x16_sum(u3);
      u0 = x32_sum(u0); u1 = x32_sum(u1); u2 = x32_sum(u2); u3 = x32_sum(u3);
      v2u d; d[0] = pkbf(u0, u1); d[1] = pkbf(u2, u3);
      if (lane < 16) *(LAS v2u*)(L + aQw) = d;
    }
    BARRIER();
    f32x4 sc;
    {
      v4u qf[4];
#pragma unroll
      for (int s = 0; s < 4; ++s) qf[s] = *(const LAS v4u*)(L + aQr + s * 256);
      __builtin_amdgcn_sched_barrier(0);
      const f32x4 z4 = {0.f, 0.f, 0.f, 0.f};
      const f32x4 s0 = mfma32(kf[1], qf[1], mfma32(kf[0], qf[0], z4)), s1 = mfma32(kf[3], qf[3], mfma32(kf[2], qf[2], z4));
      sc = s0 + s1;
      int mx = imax(imax(fkey(sc[0]), fkey(sc[1])), imax(fkey(sc[2]), fkey(sc[3])));
      mx = x16_imax(mx); mx = x32_imax(mx);
      if (lane < 4) *(LAS int*)(L + aMw) = mx;
    }
    BARRIER();
    float mc;
    constexpr float C2 = 0.08838834764831845f * 1.4426950408889634f;
    {
      const v4u ma = *(const LAS v4u*)(L + aMr), mb = *(const LAS v4u*)(L + aMr + 16);
      const int M = imax(imax(imax((int)ma[0], (int)ma[1]), imax((int)ma[2], (int)ma[3])), imax(imax((int)mb[0], (int)mb[1]), imax((int)mb[2], (int)mb[3])));
      mc = funkey(M) * C2;
      float e[4];
#pragma unroll
      for (int r = 0; r < 4; ++r) e[r] = __builtin_amdgcn_exp2f(fmaf(sc[r], C2, -mc));
      v2u pk; pk[0] = pkbf(e[0], e[1]); pk[1] = pkbf(e[2], e[3]);
      if (c < 4) *(LAS v2u*)(L + aPw) = pk;
      float lsum = (bflo(pk[0]) + bfhi(pk[0])) + (bflo(pk[1]) + bfhi(pk[1]));
      lsum = x16_sum(lsum); lsum = x32_sum(lsum);
      if (lane < 4) *(LAS float*)(L + aMw + 128) = lsum;
    }
    BARRIER();
    {
      v4u pf[4]; s16x4 va[4][2];
#pragma unroll
      for (int s = 0; s < 4; ++s) { va[s][0] = trd(L + aV[0] + s * 8192); va[s][1] = trd(L + aV[1] + s * 8192); pf[s] = *(const LAS v4u*)(L + aPr + s * 256); }
      const f32x4 la = *(const LAS f32x4*)(L + aMr + 128), lb = *(const LAS f32x4*)(L + aMr + 144);
      __builtin_amdgcn_sched_barrier(0);
      const f32x4 z4 = {0.f, 0.f, 0.f, 0.f};
      const f32x4 o0 = mfma32(cat(va[1][0], va[1][1]), pf[1], mfma32(cat(va[0][0], va[0][1]), pf[0], z4));
      const f32x4 o1 = mfma32(cat(va[3][0], va[3][1]), pf[3], mfma32(cat(va[2][0], va[2][1]), pf[2], z4));
      const float l8 = ((la[0] + la[1]) + (la[2] + la[3])) + ((lb[0] + lb[1]) + (lb[2] + lb[3]));
      const f32x4 oa = o0 + o1;
      v4u gr; gr[0] = __float_as_uint((kk & 1) ? oa[2] : oa[0]); gr[1] = __float_as_uint((kk & 1) ? oa[3] : oa[1]); gr[2] = __float_as_uint((kk & 1) ? l8 : mc); gr[3] = otag;
      if (kk < 2) ST16(gr, ooff);
    }
    if (w < 4) {
      DELAY(DLY_O);
      v4u ga;
      for (unsigned spin = 0; spin < SPIN_MAX; ++spin) {
        asm volatile("" ::: "memory");
        ga = LD16(offa);
        if (NONEBAD(ga[3] ^ tex)) break;
      }
      const float aux = __uint_as_float(ga[2]), axn = dpp<XOR1>(aux);
      const float mj = (lane & 1) ? axn : aux, lj = (lane & 1) ? aux : axn;
      int mk = fkey(mj);
      mk = imax(mk, dppi<XOR8>(mk)); mk = x16_imax(mk); mk = x32_imax(mk);
      const float Mw = funkey(mk);
      const float wt = __builtin_amdgcn_exp2f(mj - Mw);
      float o0 = __uint_as_float(ga[0]) * wt, o1 = __uint_as_float(ga[1]) * wt, ll = lj * wt;
      o0 += dpp<XOR8>(o0); o1 += dpp<XOR8>(o1); ll += dpp<XOR8>(ll);
      o0 = x16_sum(o0); o1 = x16_sum(o1); ll = x16_sum(ll);
      o0 = x32_sum(o0); o1 = x32_sum(o1); ll = x32_sum(ll);
      if (lane < 9) { f32x2 o2; o2[0] = lane < 8 ? o0 : Mw; o2[1] = lane < 8 ? o1 : ll; *(LAS f32x2*)(L + aOw) = o2; }
    }
    BARRIER();
    {
      f32x4 ou[4]; f32x2 ml[4]; s16x4 a[4];
#pragma unroll
      for (int u = 0; u < 4; ++u) { ml[u] = *(const LAS f32x2*)(L + L_OP + u * 80 + 64); ou[u] = *(const LAS f32x4*)(L + aOr + u * 80); }
#pragma unroll
      for (int t = 0; t < 4; ++t) a[t] = trd(L + aWo[t]);
      f32x2 mv = *(const LAS f32x2*)(L + aMp);
      __builtin_amdgcn_sched_barrier(0);
      if (!act) { mv[0] = 0.f; mv[1] = 0.f; }
      const float M = funkey(imax(imax(fkey(ml[0][0]), fkey(ml[1][0])), imax(fkey(ml[2][0]), fkey(ml[3][0]))));
      float f[4];
#pragma unroll
      for (int u = 0; u < 4; ++u) f[u] = __builtin_amdgcn_exp2f(ml[u][0] - M);
      const float li = __builtin_amdgcn_rcpf((f[0] * ml[0][1] + f[1] * ml[1][1]) + (f[2] * ml[2][1] + f[3] * ml[3][1]));
      const f32x4 o4 = ((ou[0] * f[0] + ou[1] * f[1]) + (ou[2] * f[2] + ou[3] * f[3])) * li;
      v2u of; of[0] = pkbf(o4[0], o4[1]); of[1] = pkbf(o4[2], o4[3]);
      const f32x4 z4 = {0.f, 0.f, 0.f, 0.f};
      v2u om[4];
#pragma unroll
      for (int t = 0; t < 4; ++t) { om[t][0] = of[0] & tm[t]; om[t][1] = of[1] & tm[t]; }
      const f32x4 d = mfma16(a[1], om[1], mfma16(a[0], om[0], z4)) + mfma16(a[3], om[3], mfma16(a[2], om[2], z4));
      v4u gr; gr[0] = __float_as_uint(c < 8 ? (prs ? d[2] : d[0]) : mv[0]); gr[1] = tlo; gr[2] = __float_as_uint(c < 8 ? (prs ? d[3] : d[1]) : mv[1]); gr[3] = thi;
      ST16(gr, soff);
    }
    {
      DELAY(DLY_S);
      v4u gq;
      for (unsigned spin = 0; spin < SPIN_MAX; ++spin) {
        asm volatile("" ::: "memory");
        gq = LD16(foff);
        if (NONEBAD(TAGBAD(gq))) break;
      }
      float a0 = __uint_as_float(gq[0]), a1 = __uint_as_float(gq[2]);
      a0 += dpp<XOR2>(a0); a1 += dpp<XOR2>(a1); a0 += dpp<ROR4>(a0); a1 += dpp<ROR4>(a1); a0 += dpp<XOR8>(a0); a1 += dpp<XOR8>(a1);
      a0 = x16_sum(a0); a1 = x16_sum(a1); a0 = x32_sum(a0); a1 = x32_sum(a1);
      if (lane < 2) { f32x2 o2; o2[0] = a0; o2[1] = a1; *(LAS f32x2*)(L + L_FS + lane * 64 + w * 8) = o2; }
    }
    BARRIER();
    if (w == 0) {
      const int e = (lane >> 3) & 1, u = lane & 7;
      const unsigned xw = *(const LAS unsigned*)(L + L_XB), b2w = *(const LAS unsigned*)(L + L_XB + 4);
      float s = *(const LAS float*)(L + L_FS + u * 8 + e * 4), m = *(const LAS float*)(L + L_FM + u * 8 + e * 4);
      s += dpp<XOR1>(s); m += dpp<XOR1>(m); s += dpp<XOR2>(s); m += dpp<XOR2>(m); s += dpp<XOR7>(s); m += dpp<XOR7>(m);
      const float z = rbf(rbf(m) + (e ? bfhi(b2w) : bflo(b2w)));
      const float gt = rbf(z * __builtin_amdgcn_rcpf(1.f + __builtin_amdgcn_exp2f(-1.4426950408889634f * z)));
      const float r = (e ? bfhi(xw) : bflo(xw)) + rbf(rbf(s) * gt);
      const float rn = dpp<XOR8>(r);
      if (lane == 0) {
        *(GAS unsigned*)(p.out + col) = pkbf(r, rn);
        if (b == 0) { const unsigned long long T = (unsigned long long)tlo | ((unsigned long long)thi << 32); __hip_atomic_store((GAS unsigned long long*)(p.ws + W_E), T, __ATOMIC_RELAXED, __HIP_MEMORY_SCOPE_AGENT); }
      }
    }
  }
}

extern "C" void kernel_launch(void* const* d_in, const int* in_sizes, int n_in,
                              void* d_out, int out_size, void* d_ws, size_t ws_size,
                              hipStream_t stream) {
  Params p;
  p.x = (const u16*)d_in[0]; p.kc = (const u16*)d_in[1]; p.vc = (const u16*)d_in[2]; p.wq = (const u16*)d_in[3]; p.wo = (const u16*)d_in[4];
  p.w1 = (const u16*)d_in[6]; p.b1 = (const u16*)d_in[7]; p.w2 = (const u16*)d_in[8]; p.b2 = (const u16*)d_in[9];
  p.out = (u16*)d_out; p.ws = (unsigned char*)d_ws;
  fused_kernel<<<NB, NTHR, 0, stream>>>(p);
}
```
